```python
import torch

def _softmax(x, axis=-1):
    return torch.softmax(x, dim=axis)
def _gelu(x):
    return torch.nn.functional.gelu(x, approximate="tanh")
def _silu(x):
    return torch.nn.functional.silu(x)
def _sigmoid(x):
    return torch.sigmoid(x)
def _layernorm(x, axis=-1, eps=1e-5):
    m = x.mean(dim=axis, keepdim=True)
    v = x.var(dim=axis, keepdim=True, unbiased=False)
    return (x - m) / torch.sqrt(v + eps)
def _scatter_add(tgt, idx, src, axis=0):
    return tgt.clone().index_add_(axis, idx, src)
def _segment_sum(src, seg, n):
    out = torch.zeros((n,) + src.shape[1:], dtype=src.dtype)
    return out.index_add_(0, seg, src)
def _pad(x, pw):
    flat = tuple(v for pair in reversed(pw) for v in pair)
    return torch.nn.functional.pad(x, flat)

kernel_name = 'parallel_sum__ssm_conv_s5__glu_gated_bilinear__prompt'

def setup_inputs():

    _w = (lambda a: (lambda t: t.to(torch.bfloat16) if t.is_floating_point() else t)(torch.as_tensor(a)))
    def _A_setup():
        B, S, D = 1, 2048, 640
        P = D
        Lambda_re = -0.5 * (lambda _x: torch.exp(torch.as_tensor(_x)))((lambda *s: torch.randn(*s, dtype=torch.bfloat16) * 0.2)(P))
        Lambda_im = (lambda *s: torch.randn(*s, dtype=torch.bfloat16) * 0.8)(P) * 1.0
        B_re = (lambda *s: torch.randn(*s, dtype=torch.bfloat16) * 0.2)(P, D) * (D ** -0.5)
        B_im = (lambda *s: torch.randn(*s, dtype=torch.bfloat16) * 0.2)(P, D) * (D ** -0.5)
        C_re = (lambda *s: torch.randn(*s, dtype=torch.bfloat16) * 0.2)(D, P) * (P ** -0.5)
        C_im = (lambda *s: torch.randn(*s, dtype=torch.bfloat16) * 0.2)(D, P) * (P ** -0.5)
        log_dt = (lambda *s: torch.randn(*s, dtype=torch.bfloat16) * 0.2)(P) * 0.3 - 2.0
        Dskip = (lambda *s: torch.randn(*s, dtype=torch.bfloat16) * 0.05)(D)
        return {
            "x": _w((lambda *s: torch.randn(*s, dtype=torch.bfloat16) * 0.05)(B, S, D)),
            "Lambda_re": _w(Lambda_re),
            "Lambda_im": _w(Lambda_im),
            "B_re": _w(B_re),
            "B_im": _w(B_im),
            "C_re": _w(C_re),
            "C_im": _w(C_im),
            "log_dt": _w(log_dt),
            "Dskip": _w(Dskip),
        }
    _A_in = _A_setup()

    def _B_setup():
        B, S, D, F = 1, 2048, 640, 1728
        return {
            "x": _w((lambda *s: torch.randn(*s, dtype=torch.bfloat16) * 0.05)(B, S, D)),
            "w_gate": _w((lambda *s: torch.randn(*s, dtype=torch.bfloat16) * 0.4)(D, F) * (D ** -0.5)),
            "w_up": _w((lambda *s: torch.randn(*s, dtype=torch.bfloat16) * 0.4)(D, F) * (D ** -0.5)),
            "w_down": _w((lambda *s: torch.randn(*s, dtype=torch.bfloat16) * 0.4)(F, D) * (F ** -0.5)),
        }
    _B_in = _B_setup()

    return {
        "A_x": _A_in["x"],
        "A_Lambda_re": _A_in["Lambda_re"],
        "A_Lambda_im": _A_in["Lambda_im"],
        "A_B_re": _A_in["B_re"],
        "A_B_im": _A_in["B_im"],
        "A_C_re": _A_in["C_re"],
        "A_C_im": _A_in["C_im"],
        "A_log_dt": _A_in["log_dt"],
        "A_Dskip": _A_in["Dskip"],
        "B_x": _B_in["x"],
        "B_w_gate": _B_in["w_gate"],
        "B_w_up": _B_in["w_up"],
        "B_w_down": _B_in["w_down"],
    }

def reference(A_x, A_Lambda_re, A_Lambda_im, A_B_re, A_B_im, A_C_re, A_C_im, A_log_dt, A_Dskip, B_x, B_w_gate, B_w_up, B_w_down):
    def _A_ref(x, Lambda_re, Lambda_im, B_re, B_im, C_re, C_im, log_dt, Dskip):
        Bsz, L, H = x.shape
        Lam = Lambda_re + 1j * Lambda_im
        Bmat = B_re + 1j * B_im
        Cmat = C_re + 1j * C_im
        dt = torch.exp(log_dt)
        Lam_bar = torch.exp(dt * Lam)
        B_bar = ((Lam_bar - 1.0) / Lam)[:, None] * Bmat
        Bu = torch.einsum("bld,pd->blp", x + 0j, B_bar)
        h = torch.zeros_like(Bu[:, 0, :])
        hs = []
        for t in range(L):
            h = Lam_bar[None, :] * h + Bu[:, t, :]
            hs.append(h)
        hseq = torch.stack(hs, 1)
        y = 2.0 * torch.einsum("blp,dp->bld", hseq, Cmat).real + x * Dskip[None, None, :]
        return y

    def _B_ref(x, w_gate, w_up, w_down):
        g = torch.einsum("bsd,df->bsf", x, w_gate)
        u = torch.einsum("bsd,df->bsf", x, w_up)
        h = g * u
        return torch.einsum("bsf,fd->bsd", h, w_down)

    x = A_x
    xn = _layernorm(x, axis=-1)
    return x + _A_ref(xn, A_Lambda_re, A_Lambda_im, A_B_re, A_B_im, A_C_re, A_C_im, A_log_dt, A_Dskip) + _B_ref(xn, B_w_gate, B_w_up, B_w_down)
```

```cpp
#include <hip/hip_runtime.h>
#include <hip/hip_bf16.h>
#include <stdint.h>

typedef unsigned short u16;
__device__ __forceinline__ float bf2f(u16 v) { return __uint_as_float(((unsigned)v) << 16); }
__device__ __forceinline__ u16 f2bf(float f) { unsigned u = __float_as_uint(f); u += 0x7fffu + ((u >> 16) & 1u); return (u16)(u >> 16); }
__device__ __forceinline__ float rbf(float f) { return bf2f(f2bf(f)); }

constexpr int L = 2048, D = 640, P = 640, F = 1728;

__global__ void ln_kernel(const u16* x, u16* xn) {
  int row = blockIdx.x; int t = threadIdx.x;
  float v[10]; float s = 0.f;
  for (int i = 0; i < 10; ++i) { v[i] = bf2f(x[row * D + t + 64 * i]); s += v[i]; }
  for (int o = 32; o; o >>= 1) s += __shfl_xor(s, o);
  float mean = s / D;
  float q = 0.f;
  for (int i = 0; i < 10; ++i) { float d = v[i] - mean; q += d * d; }
  for (int o = 32; o; o >>= 1) q += __shfl_xor(q, o);
  float var = rbf(q / D);
  float mb = rbf(mean);
  float sd = rbf(sqrtf(rbf(var + 1e-5f)));
  for (int i = 0; i < 10; ++i) xn[row * D + t + 64 * i] = f2bf(rbf(v[i] - mb) / sd);
}

__global__ void g1_ssm(const u16* xn, const u16* Bre, const u16* Bim, float* vre, float* vim) {
  int p = blockIdx.x * 64 + threadIdx.x; int l = blockIdx.y;
  float a = 0.f, b = 0.f;
  for (int d = 0; d < D; ++d) { float xv = bf2f(xn[l * D + d]); a += xv * bf2f(Bre[p * D + d]); b += xv * bf2f(Bim[p * D + d]); }
  vre[l * P + p] = a; vim[l * P + p] = b;
}
__global__ void g1_glu(const u16* xn, const u16* wg, const u16* wu, u16* h) {
  int f = blockIdx.x * 64 + threadIdx.x; int l = blockIdx.y;
  float a = 0.f, b = 0.f;
  for (int d = 0; d < D; ++d) { float xv = bf2f(xn[l * D + d]); a += xv * bf2f(wg[d * F + f]); b += xv * bf2f(wu[d * F + f]); }
  h[l * F + f] = f2bf(rbf(a) * rbf(b));
}
__global__ void scan(const u16* lre, const u16* lim, const u16* ldt, float* vre, float* vim) {
  int p = blockIdx.x * 64 + threadIdx.x;
  float ar = bf2f(lre[p]), ai = bf2f(lim[p]);
  float dt = rbf(expf(bf2f(ldt[p])));
  float e = expf(dt * ar); float lr = e * cosf(dt * ai), li = e * sinf(dt * ai);
  float nr = lr - 1.f, ni = li; float den = ar * ar + ai * ai;
  float cr = (nr * ar + ni * ai) / den, ci = (ni * ar - nr * ai) / den;
  float hr = 0.f, hi = 0.f;
  for (int l = 0; l < L; ++l) {
    float a = vre[l * P + p], b = vim[l * P + p];
    float br = cr * a - ci * b, bi = cr * b + ci * a;
    float tr = lr * hr - li * hi + br, ti = lr * hi + li * hr + bi;
    hr = tr; hi = ti;
    vre[l * P + p] = hr; vim[l * P + p] = hi;
  }
}
__global__ void g2(const u16* x, const u16* xn, const float* hre, const float* him, const u16* Cre, const u16* Cim,
                   const u16* dsk, const u16* h, const u16* wd, float* out) {
  int d = blockIdx.x * 64 + threadIdx.x; int l = blockIdx.y;
  float a = 0.f;
  for (int p = 0; p < P; ++p) a += hre[l * P + p] * bf2f(Cre[d * P + p]) - him[l * P + p] * bf2f(Cim[d * P + p]);
  float b = 0.f;
  for (int f = 0; f < F; ++f) b += bf2f(h[l * F + f]) * bf2f(wd[f * D + d]);
  float y = 2.f * a + rbf(bf2f(xn[l * D + d]) * bf2f(dsk[d]));
  out[l * D + d] = bf2f(x[l * D + d]) + y + rbf(b);
}

extern "C" void kernel_launch(void* const* d_in, const int* in_sizes, int n_in,
                              void* d_out, int out_size, void* d_ws, size_t ws_size,
                              hipStream_t stream) {
  const u16* x = (const u16*)d_in[0];
  const u16* lre = (const u16*)d_in[1];
  const u16* lim = (const u16*)d_in[2];
  const u16* Bre = (const u16*)d_in[3];
  const u16* Bim = (const u16*)d_in[4];
  const u16* Cre = (const u16*)d_in[5];
  const u16* Cim = (const u16*)d_in[6];
  const u16* ldt = (const u16*)d_in[7];
  const u16* dsk = (const u16*)d_in[8];
  const u16* wg = (const u16*)d_in[10];
  const u16* wu = (const u16*)d_in[11];
  const u16* wd = (const u16*)d_in[12];
  char* ws = (char*)d_ws;
  u16* xn = (u16*)ws; ws += L * D * 2;
  float* vre = (float*)ws; ws += L * P * 4;
  float* vim = (float*)ws; ws += L * P * 4;
  u16* h = (u16*)ws;
  ln_kernel<<<L, 64, 0, stream>>>(x, xn);
  g1_ssm<<<dim3(P / 64, L), 64, 0, stream>>>(xn, Bre, Bim, vre, vim);
  g1_glu<<<dim3(F / 64, L), 64, 0, stream>>>(xn, wg, wu, h);
  scan<<<P / 64, 64, 0, stream>>>(lre, lim, ldt, vre, vim);
  g2<<<dim3(D / 64, L), 64, 0, stream>>>(x, xn, vre, vim, Cre, Cim, dsk, h, wd, (float*)d_out);
}
```

```cpp
#include <hip/hip_runtime.h>
#include <stdint.h>

#define GAS __attribute__((address_space(1)))
#define LAS __attribute__((address_space(3)))
#define DI __device__ __forceinline__
typedef unsigned short u16;
typedef short bf16x8 __attribute__((ext_vector_type(8)));
typedef short s16x4 __attribute__((ext_vector_type(4)));
typedef __bf16 bf2 __attribute__((ext_vector_type(2)));
typedef float f32x4 __attribute__((ext_vector_type(4)));
typedef float f32x2 __attribute__((ext_vector_type(2)));
typedef unsigned v4u __attribute__((ext_vector_type(4)));
typedef unsigned v2u __attribute__((ext_vector_type(2)));

#define XMAP 2

constexpr int L_ = 2048, D_ = 640, F_ = 1728;
constexpr int NS2 = 94;
constexpr unsigned WS_EPOCH = 0;
constexpr unsigned WS_STAT = 4096;
constexpr unsigned WS_AGG = WS_STAT + 2048 * 16;
constexpr unsigned WS_FLAGH = WS_AGG + 8 * 640 * 16;
constexpr unsigned WS_FLAGW = WS_FLAGH + 32 * 8 * 256;
constexpr unsigned WS_CTL_END = WS_FLAGW + 8 * 8 * 128;
constexpr unsigned WS_W2T = 262144;
constexpr unsigned W2T_CT = NS2 * 5 * 1024;
constexpr unsigned WS_H = WS_W2T + 8 * W2T_CT;
constexpr unsigned H_RT = NS2 * 4 * 1024;
constexpr unsigned WS_END = WS_H + 32 * H_RT;
static_assert(WS_CTL_END <= WS_W2T, "ws map");
constexpr unsigned SLOT = 53248, OX = 0, OWB = 32768, OWS = 49152;
constexpr unsigned L_ST = 3 * SLOT;
constexpr unsigned L_CS = L_ST + 2048;
constexpr unsigned L_CSS = L_CS + 512;
constexpr unsigned L_LAM = L_CSS + 128;
constexpr unsigned L_COEF = L_LAM + 512;
constexpr unsigned L_AGG = SLOT;
constexpr unsigned L_CIN = SLOT + 16384;
constexpr unsigned L_TR = 2 * SLOT;
constexpr unsigned L_JOB = 2 * SLOT;
constexpr unsigned LDS_BYTES = 163840;
static_assert(L_COEF + 512 <= LDS_BYTES, "lds map");
constexpr int SPIN_CAP = 1 << 15;

DI float bf2f(unsigned v) { return __uint_as_float(v << 16); }
DI float bfhi(unsigned v) { return __uint_as_float(v & 0xffff0000u); }
DI unsigned pk2(float a, float b) { bf2 v = {(__bf16)a, (__bf16)b}; return __builtin_bit_cast(unsigned, v); }
DI float rbf(float a) { return bf2f(pk2(a, 0.f) & 0xffffu); }
DI float dot2ones(unsigned a, float c) {
  bf2 ones = {(__bf16)1.0f, (__bf16)1.0f};
  return __builtin_amdgcn_fdot2_f32_bf16(__builtin_bit_cast(bf2, a), ones, c, false);
}
DI float colsum4(bf16x8 f, float c) {
  v4u u = __builtin_bit_cast(v4u, f);
  unsigned a = u.x, b = u.y, d = u.z, e = u.w;
  c = dot2ones(a, c); c = dot2ones(b, c); c = dot2ones(d, c); c = dot2ones(e, c);
  return c;
}
DI f32x4 mfma(bf16x8 a, bf16x8 b, f32x4 c) { return __builtin_amdgcn_mfma_f32_16x16x32_bf16(a, b, c, 0, 0, 0); }
DI v4u mkrsrc(const void* p, unsigned bytes) {
  uint64_t a = (uint64_t)p;
  v4u r; r.x = (unsigned)a; r.y = (unsigned)(a >> 32) & 0xffffu; r.z = bytes; r.w = 0x00020000u;
  return r;
}
DI void glds16(v4u rs, unsigned voff, unsigned soff, unsigned ldsaddr) {
  asm volatile("s_mov_b32 m0, %3\n\ts_nop 0\n\tbuffer_load_dwordx4 %0, %1, %2 offen lds" ::"v"(voff), "s"(rs), "s"(soff), "s"(ldsaddr) : "memory");
}
template <class T> DI T ldsr(unsigned a) { return *(LAS T*)(uintptr_t)a; }
template <class T> DI void ldsw(unsigned a, T v) { *(LAS T*)(uintptr_t)a = v; }
DI bf16x8 trread2(unsigned a0, unsigned a1) {
  s16x4 lo = __builtin_amdgcn_ds_read_tr16_b64_v4i16((LAS s16x4*)(uintptr_t)a0);
  s16x4 hi = __builtin_amdgcn_ds_read_tr16_b64_v4i16((LAS s16x4*)(uintptr_t)a1);
  return __builtin_shufflevector(lo, hi, 0, 1, 2, 3, 4, 5, 6, 7);
}
#define WAITV(n) asm volatile("s_waitcnt vmcnt(" #n ")" ::: "memory")
#define BAR() asm volatile("s_waitcnt lgkmcnt(0)\n\ts_barrier" ::: "memory")
#define SB0() __builtin_amdgcn_sched_barrier(0)
DI float shx(float v, int m) { return __shfl_xor(v, m); }
struct cpx { float r, i; };
DI cpx cmul(cpx a, cpx b) { return {a.r * b.r - a.i * b.i, a.r * b.i + a.i * b.r}; }
DI cpx cfma(cpx a, cpx b, cpx c) { return {fmaf(a.r, b.r, fmaf(-a.i, b.i, c.r)), fmaf(a.r, b.i, fmaf(a.i, b.r, c.i))}; }

#define STAMP(k) do {} while (0)
struct Ctx {
  const u16 *x, *bre, *bim, *cre, *cim, *wg, *wu, *wd;
  char* ws;
  __amdgpu_buffer_rsrc_t rctl;
  unsigned T, T2;
  int lane, w, rb, cg;
};

struct Frags { bf16x8 x[4]; bf16x8 w[4]; bf16x8 s[2]; };

template <int TYPE> struct G1 {
  v4u rx, rw0, rw1, rs;
  unsigned vx0, vx1, vw, vs;
  unsigned sx, sw, ss;
  unsigned dw;
  unsigned ax[2][2], aw[4][2], as[2][2];
  f32x4 acc[4][4];
  f32x4 accs[2][2];
  float cs0, cs1;
  int w;

  DI void init(const Ctx& c) {
    const int l = c.lane, i = l & 15, q = l >> 4;
    w = c.w;
    const int wt = w & 3, wn = w >> 2;
    rx = mkrsrc(c.x, L_ * D_ * 2);
    {
      unsigned ch = (l & 7) ^ ((l >> 3) & 3);
      vx0 = (l >> 3) * 1280 + ch * 16;
      vx1 = (l >> 3) * 1280 + (ch ^ 4) * 16;
      sx = (c.rb * 256 + w * 32) * 1280;
    }
    if (TYPE < 2) {
      rw0 = mkrsrc(c.wg, D_ * F_ * 2); rw1 = mkrsrc(c.wu, D_ * F_ * 2);
      unsigned key2 = ((l >> 4) & 1) | ((w & 1) << 1);
      vw = (l >> 3) * 3456 + (((l & 7) ^ (key2 << 1)) * 16);
      sw = w * 8 * 3456 + c.cg * 128;
      dw = 64 * 3456;
      if (TYPE == 0) {
        rs = mkrsrc((w & 2) ? c.bim : c.bre, 640 * 640 * 2);
        vs = (l >> 3) * 1280 + (((l & 7) ^ (l >> 3)) * 16);
        ss = (c.cg * 16 + (w & 1) * 8) * 1280;
      }
    } else {
      rw0 = mkrsrc((w & 1) ? c.bim : c.bre, 640 * 640 * 2);
      vw = (l >> 3) * 1280 + (((l & 7) ^ (l >> 3)) * 16);
      sw = ((20 + 4 * (c.cg - 27) + (w >> 1)) * 16) * 1280;
      dw = 128;
    }
#pragma unroll
    for (int kk = 0; kk < 2; ++kk) {
      unsigned ch = (((kk * 4 + q) ^ (i & 7)) * 16);
#pragma unroll
      for (int pr = 0; pr < 2; ++pr) ax[pr][kk] = OX + (wt * 64 + (pr ^ wn) * 32 + 8 * (i >> 2) + (i & 3)) * 128 + ch;
      if (TYPE == 0) {
#pragma unroll
        for (int a = 0; a < 2; ++a) as[a][kk] = OWS + (a ^ (wt & 1)) * 2048 + i * 128 + ch;
      }
      if (TYPE == 2) {
#pragma unroll
        for (int a = 0; a < 4; ++a) aw[a][kk] = OWB + wn * 8192 + (a ^ wt) * 2048 + i * 128 + ch;
      }
    }
    if (TYPE < 2) {
      const int r = (l >> 2) & 3, cc = l & 3;
      const unsigned key2 = ((r >> 1) & 1) | ((q & 1) << 1);
#pragma unroll
      for (int a = 0; a < 4; ++a) {
        const int act = a ^ wt;
        aw[a][0] = OWB + (act >> 1) * 8192 + (8 * q + r) * 128 + (((2 * wn + (act & 1)) ^ key2) * 32) + cc * 8;
      }
    }
#pragma unroll
    for (int a = 0; a < 4; ++a)
#pragma unroll
      for (int m = 0; m < 4; ++m) acc[a][m] = f32x4{0.f, 0.f, 0.f, 0.f};
#pragma unroll
    for (int a = 0; a < 2; ++a)
#pragma unroll
      for (int m = 0; m < 2; ++m) accs[a][m] = f32x4{0.f, 0.f, 0.f, 0.f};
    cs0 = 0.f; cs1 = 0.f;
  }

  DI void stage(int s, unsigned so) {
    if (TYPE == 0) {
      if (w < 4) glds16(rs, vs, ss + s * 128, so + OWS + w * 1024);
    }
    const unsigned gx = sx + s * 128, lx = so + OX + w * 4096;
    glds16(rx, vx0, gx, lx);
    glds16(rx, vx1, gx + 10240, lx + 1024);
    glds16(rx, vx0, gx + 20480, lx + 2048);
    glds16(rx, vx1, gx + 30720, lx + 3072);
    const unsigned gw = sw + s * dw;
    if (TYPE < 2) {
      glds16(rw0, vw, gw, so + OWB + w * 1024);
      glds16(rw1, vw, gw, so + OWB + 8192 + w * 1024);
    } else {
      glds16(rw0, vw, gw, so + OWB + w * 2048);
      glds16(rw0, vw, gw + 10240, so + OWB + w * 2048 + 1024);
    }
  }

  template <int KK> DI void read(Frags& f, unsigned so) {
#pragma unroll
    for (int m = 0; m < 4; ++m) f.x[m] = ldsr<bf16x8>(ax[m >> 1][KK] + so + (m & 1) * 512);
    if (TYPE < 2) {
#pragma unroll
      for (int a = 0; a < 4; ++a) { unsigned b = aw[a][0] + so; f.w[a] = trread2(b + KK * 4096, b + KK * 4096 + 512); }
    } else {
#pragma unroll
      for (int a = 0; a < 4; ++a) f.w[a] = ldsr<bf16x8>(aw[a][KK] + so);
    }
    if (TYPE == 0) {
#pragma unroll
      for (int a = 0; a < 2; ++a) f.s[a] = ldsr<bf16x8>(as[a][KK] + so);
    }
  }

  DI void mma(const Frags& f) {
#pragma unroll
    for (int a = 0; a < 4; ++a)
#pragma unroll
      for (int m = 0; m < 4; ++m) acc[a][m] = TYPE < 2 ? mfma(f.w[a], f.x[m], acc[a][m]) : mfma(f.x[m], f.w[a], acc[a][m]);
    cs0 = colsum4(f.w[0], cs0);
    if (TYPE == 0) {
#pragma unroll
      for (int a = 0; a < 2; ++a)
#pragma unroll
        for (int m = 0; m < 2; ++m) accs[a][m] = mfma(f.x[m], f.s[a], accs[a][m]);
      cs1 = colsum4(f.s[0], cs1);
    }
  }
};

DI void ssm_consts(float ar, float ai, float ldt, cpx& lam, cpx& coef) {
  const float dt = rbf(expf(ldt));
  const float e = expf(dt * ar);
  float sn, cs;
  sincosf(dt * ai, &sn, &cs);
  lam.r = e * cs; lam.i = e * sn;
  const float nr = lam.r - 1.f, ni = lam.i, inv = 1.f / (ar * ar + ai * ai);
  coef.r = (nr * ar + ni * ai) * inv;
  coef.i = (ni * ar - nr * ai) * inv;
}

DI void st_flag(const Ctx& c, unsigned off) {
  v2u v = {c.T, c.T2};
  __builtin_amdgcn_raw_buffer_store_b64(v, c.rctl, off, 0, 16);
}

DI void seg_pass1(f32x4& x0, f32x4& x1, f32x4& y0, f32x4& y1, const f32x4 (&st)[4], float csx, float csy, float la, float lb, float& gx, float& gy) {
  float mu[8] = {st[0].x, st[0].z, st[1].x, st[1].z, st[2].x, st[2].z, st[3].x, st[3].z};
  float rs[8] = {st[0].y, st[0].w, st[1].y, st[1].w, st[2].y, st[2].w, st[3].y, st[3].w};
#pragma unroll
  for (int r = 0; r < 4; ++r) {
    x0[r] = rs[r] * fmaf(-mu[r], csx, x0[r]); y0[r] = rs[r] * fmaf(-mu[r], csy, y0[r]);
    x1[r] = rs[4 + r] * fmaf(-mu[4 + r], csx, x1[r]); y1[r] = rs[4 + r] * fmaf(-mu[4 + r], csy, y1[r]);
  }
  float hx = x0[0], hy = y0[0];
#pragma unroll
  for (int j = 1; j < 8; ++j) {
    const float vx = j < 4 ? x0[j & 3] : x1[j & 3], vy = j < 4 ? y0[j & 3] : y1[j & 3];
    const float nx = fmaf(la, hx, fmaf(-lb, hy, vx)), ny = fmaf(la, hy, fmaf(lb, hx, vy));
    hx = nx; hy = ny;
  }
  gx = hx; gy = hy;
}
DI void seg_pass2(const f32x4& x0, const f32x4& x1, const f32x4& y0, const f32x4& y1, float la, float lb, float hx, float hy, v4u& px, v4u& py) {
  float ox[8], oy[8];
#pragma unroll
  for (int j = 0; j < 8; ++j) {
    const float vx = j < 4 ? x0[j & 3] : x1[j & 3], vy = j < 4 ? y0[j & 3] : y1[j & 3];
    const float nx = fmaf(la, hx, fmaf(-lb, hy, vx)), ny = fmaf(la, hy, fmaf(lb, hx, vy));
    hx = nx; hy = ny; ox[j] = nx; oy[j] = ny;
  }
  px = v4u{pk2(ox[0], ox[1]), pk2(ox[2], ox[3]), pk2(ox[4], ox[5]), pk2(ox[6], ox[7])};
  py = v4u{pk2(oy[0], oy[1]), pk2(oy[2], oy[3]), pk2(oy[4], oy[5]), pk2(oy[6], oy[7])};
}

template <int TYPE> DI void phase1(Ctx& c, const u16* lre, const u16* lim, const u16* ldt) {
  const int l = c.lane, i = l & 15, q = l >> 4, w = c.w, wt = w & 3, wn = w >> 2;
  const int rb = c.rb, cg = c.cg;
  G1<TYPE> g;
  g.init(c);
  unsigned xr0, xr1, xr2, xr3, xr4, pa, pb, pd, ep;
  const int srow = rb * 256 + cg * 8 + w;
  const int gi = rb * 7 + (cg - 20), si = rb * 5 + (cg - 27);
  {
    int pbase = TYPE == 0 ? cg * 16 : (TYPE == 2 ? (w == 1 ? (si >> 2) * 64 : (20 + 4 * (cg - 27)) * 16) : 0);
    int pidx = pbase + (TYPE == 0 ? i : l);
    const u16* xrow = c.x + (size_t)srow * D_;
    asm volatile(
        "global_load_dword %0, %9, %10\n\t"
        "global_load_dword %1, %9, %10 offset:256\n\t"
        "global_load_dword %2, %9, %10 offset:512\n\t"
        "global_load_dword %3, %9, %10 offset:768\n\t"
        "global_load_dword %4, %9, %10 offset:1024\n\t"
        "global_load_ushort %5, %11, %12\n\t"
        "global_load_ushort %6, %11, %13\n\t"
        "global_load_ushort %7, %11, %14\n\t"
        "global_load_dword %8, %15, %16 sc1"
        : "=&v"(xr0), "=&v"(xr1), "=&v"(xr2), "=&v"(xr3), "=&v"(xr4), "=&v"(pa), "=&v"(pb), "=&v"(pd), "=&v"(ep)
        : "v"(l * 4), "s"(xrow), "v"(pidx * 2), "s"(lre), "s"(lim), "s"(ldt), "v"(0), "s"(c.ws)
        : "memory");
  }
  const bool job = (TYPE == 1 && gi < 54) || TYPE == 2;
  if (TYPE == 1) {
    if (job) {
      const v4u rj = mkrsrc(c.wd, F_ * D_ * 2);
#pragma unroll
      for (int j = 0; j < 5; ++j) {
        const unsigned ch = (w * 5 + j) * 64 + l, f = ch / 80, cp = ch - f * 80, sg = cp >> 1;
        const unsigned vo = f * 1280 + ((sg & ~7u) | ((sg ^ f) & 7u)) * 32 + (cp & 1) * 16;
        glds16(rj, vo, gi * 32 * 1280, L_JOB + (w * 5 + j) * 1024);
      }
    }
  }
  if (TYPE == 2) {
    const v4u rj = mkrsrc(wn ? c.cim : c.cre, 640 * 640 * 2);
#pragma unroll
    for (int j = 0; j < 5; ++j) {
      const unsigned row = (wt * 5 + j) * 8 + (l >> 3);
      const unsigned vo = row * 1280 + (((l & 7) ^ ((row >> 1) & 7)) * 16);
      glds16(rj, vo, (si & 3) * 160 * 1280 + (si >> 2) * 128, L_JOB + (w * 5 + j) * 1024);
    }
  }
  g.stage(0, 0);
  g.stage(1, SLOT);
  if (TYPE == 0) g.stage(2, 2 * SLOT);
  asm volatile("s_waitcnt vmcnt(%9)" : "+v"(xr0), "+v"(xr1), "+v"(xr2), "+v"(xr3), "+v"(xr4), "+v"(pa), "+v"(pb), "+v"(pd), "+v"(ep) : "n"(TYPE == 0 ? 18 : 12) : "memory");
  c.T = __builtin_amdgcn_readfirstlane(ep) + 1u;
  c.T2 = (c.T * 0x9E3779B1u) ^ 0x5bd1e995u;
  STAMP(1);
  {
    float v[10] = {bf2f(xr0), bfhi(xr0), bf2f(xr1), bfhi(xr1), bf2f(xr2), bfhi(xr2), bf2f(xr3), bfhi(xr3), bf2f(xr4), bfhi(xr4)};
    float s = 0.f;
#pragma unroll
    for (int j = 0; j < 10; ++j) s += v[j];
#pragma unroll
    for (int o = 32; o; o >>= 1) s += shx(s, o);
    const float mean = s * (1.f / 640.f);
    float qq = 0.f;
#pragma unroll
    for (int j = 0; j < 10; ++j) { const float d = v[j] - mean; qq = fmaf(d, d, qq); }
#pragma unroll
    for (int o = 32; o; o >>= 1) qq += shx(qq, o);
    const float var = rbf(qq * (1.f / 640.f));
    const float sd = sqrtf(rbf(var + 1e-5f));
    const unsigned pkd = pk2(mean, sd);
    if (l == 0) {
      v4u gr = {pkd, c.T, pkd, c.T2};
      __builtin_amdgcn_raw_buffer_store_b128(gr, c.rctl, WS_STAT + srow * 16, 0, 16);
    }
  }
  if (TYPE != 1) {
    if (w < (TYPE == 2 ? 2 : 1)) {
      cpx lam, coef;
      ssm_consts(bf2f(pa), bf2f(pb), bf2f(pd), lam, coef);
      if (w == 0) ldsw<f32x2>(L_LAM + l * 8, f32x2{lam.r, lam.i});
      else ldsw<f32x2>(L_COEF + l * 8, f32x2{2.f * coef.r, 2.f * coef.i});
    }
  }
  unsigned wflag = 0;
  if (TYPE != 0) {
    if (job) {
      BAR();
      const __amdgpu_buffer_rsrc_t rw2 = __builtin_amdgcn_make_buffer_rsrc(c.ws + WS_W2T, 0, 8 * W2T_CT, 0x00020000);
      if (TYPE == 1) {
        const int r = (l >> 2) & 3, cc = l & 3, f = 4 * q + r;
#pragma unroll
        for (int j = 0; j < 5; ++j) {
          const unsigned dt = w * 5 + j;
          const unsigned a = L_JOB + f * 1280 + ((dt & ~7u) | ((dt ^ f) & 7u)) * 32 + cc * 8;
          const bf16x8 v = trread2(a, a + 16 * 1280);
          __builtin_amdgcn_raw_buffer_store_b128(__builtin_bit_cast(v4u, v), rw2, l * 16 + j * 1024 + (w * W2T_CT + gi * 5120), 0, 16);
        }
        wflag = WS_FLAGW + (w * 8 + (gi & 7)) * 128 + (gi >> 3) * 8;
      } else {
        const f32x4 k0 = ldsr<f32x4>(L_COEF + (16 * wt + 4 * q) * 8), k1 = ldsr<f32x4>(L_COEF + (16 * wt + 4 * q) * 8 + 16);
        const float kr[4] = {k0.x, k0.z, k1.x, k1.z}, ki[4] = {k0.y, k0.w, k1.y, k1.w};
        const int step = 54 + 4 * (si >> 2) + wt, ct = 2 * (si & 3) + wn;
#pragma unroll
        for (int j = 0; j < 5; ++j) {
          const unsigned row = (wn * 5 + j) * 16 + i;
          const unsigned a = L_JOB + row * 128 + (((2 * wt + (q >> 1)) ^ ((row >> 1) & 7)) * 16) + (q & 1) * 8;
          const v2u cr = ldsr<v2u>(a), ci = ldsr<v2u>(a + 20480);
          const unsigned cr0 = cr.x, cr1 = cr.y, ci0 = ci.x, ci1 = ci.y;
          const float re[4] = {bf2f(cr0), bfhi(cr0), bf2f(cr1), bfhi(cr1)}, im[4] = {bf2f(ci0), bfhi(ci0), bf2f(ci1), bfhi(ci1)};
          float a0[4], a1[4];
#pragma unroll
          for (int e = 0; e < 4; ++e) { a0[e] = kr[e] * re[e] - ki[e] * im[e]; a1[e] = -(kr[e] * im[e] + ki[e] * re[e]); }
          const v4u o = {pk2(a0[0], a0[1]), pk2(a0[2], a0[3]), pk2(a1[0], a1[1]), pk2(a1[2], a1[3])};
          __builtin_amdgcn_raw_buffer_store_b128(o, rw2, l * 16 + j * 1024 + (ct * W2T_CT + step * 5120), 0, 16);
        }
        wflag = WS_FLAGW + (ct * 8 + (step & 7)) * 128 + (step >> 3) * 8;
      }
      BAR();
    }
    g.stage(2, 2 * SLOT);
  }
  Frags fa, fb;
  WAITV(12);
  BAR();
  STAMP(2);
  g.template read<0>(fa, 0);
  unsigned so = 0;
  for (int s = 0; s < 7; ++s) {
    const unsigned sn = so == 2 * SLOT ? 0 : so + SLOT;
    g.template read<1>(fb, so);
    g.mma(fa);
    WAITV(6);
    BAR();
    if (TYPE != 0) {
      if (s == 1 && job && l == 0) st_flag(c, wflag);
    }
    g.stage(s + 3, so);
    g.template read<0>(fa, sn);
    g.mma(fb);
    so = sn;
  }
  g.template read<1>(fb, SLOT);
  g.mma(fa);
  WAITV(6);
  BAR();
  v4u sg;
  {
    const GAS char* sp = (const GAS char*)c.ws + WS_STAT + rb * 4096;
    asm volatile("global_load_dwordx4 %0, %1, %2 sc1" : "=&v"(sg) : "v"((w & 3) * 1024 + l * 16), "s"(sp) : "memory");
  }
  g.template read<0>(fa, 2 * SLOT);
  g.mma(fb);
  g.template read<1>(fb, 2 * SLOT);
  g.mma(fa);
  asm volatile("s_waitcnt vmcnt(0)" : "+v"(sg)::"memory");
  BAR();
  g.template read<0>(fa, 0);
  g.mma(fb);
  g.template read<1>(fb, 0);
  g.mma(fa);
  g.mma(fb);
  STAMP(3);
  if (w < 4) {
    _Pragma("nounroll") for (int sp = 0; sp < SPIN_CAP; ++sp) {
      asm volatile("" ::: "memory");
      if (__all(sg.y == c.T && sg.w == c.T2)) break;
      __builtin_amdgcn_s_sleep(1);
      sg = __builtin_amdgcn_raw_buffer_load_b128(c.rctl, WS_STAT + rb * 4096 + w * 1024 + l * 16, 0, 16);
    }
    const unsigned pkd = sg.x;
    ldsw<f32x2>(L_ST + (w * 64 + l) * 8, f32x2{bf2f(pkd & 0xffffu), 1.f / bfhi(pkd)});
  }
  {
    float a = g.cs0;
    a += shx(a, 16); a += shx(a, 32);
    if (l < 16) ldsw<float>(L_CS + ((wn * 4 + wt) * 16 + l) * 4, a);
    if (TYPE == 0) {
      float b = g.cs1;
      b += shx(b, 16); b += shx(b, 32);
      if (l < 16 && w < 2) ldsw<float>(L_CSS + (w * 16 + l) * 4, b);
    }
  }
  BAR();
  const __amdgpu_buffer_rsrc_t rh = __builtin_amdgcn_make_buffer_rsrc(c.ws + WS_H + (rb * 4 + wt) * H_RT, 0, H_RT, 0x00020000);
  const unsigned fbase = WS_FLAGH + (rb * 4 + wt) * 2048;
  const int sw = wt & 1;
  constexpr int NU = TYPE == 0 ? 1 : (TYPE == 2 ? 2 : 0);
  constexpr int NP = TYPE == 0 ? 1 : 2;
  float la[2], lb[2];
  if (TYPE != 1) {
#pragma unroll
    for (int u = 0; u < NU; ++u) {
      const int ul = TYPE == 0 ? 0 : 2 * wn + (u ^ (wt >> 1));
      const f32x2 lm = ldsr<f32x2>(L_LAM + (ul * 16 + i) * 8);
      la[u] = lm.x; lb[u] = sw ? -lm.y : lm.y;
      float csx, csy;
      if (TYPE == 0) { csx = ldsr<float>(L_CSS + (sw * 16 + i) * 4); csy = ldsr<float>(L_CSS + ((sw ^ 1) * 16 + i) * 4); }
      else { csx = ldsr<float>(L_CS + ((ul * 2 + sw) * 16 + i) * 4); csy = ldsr<float>(L_CS + ((ul * 2 + (sw ^ 1)) * 16 + i) * 4); }
#pragma unroll
      for (int pr = 0; pr < NP; ++pr) {
        const int seg = (wt * 2 + (pr ^ wn)) * 4 + q;
        f32x4 st[4];
#pragma unroll
        for (int e = 0; e < 4; ++e) st[e] = ldsr<f32x4>(L_ST + seg * 64 + e * 16);
        float gx, gy;
        if (TYPE == 0) seg_pass1(g.accs[0][0], g.accs[0][1], g.accs[1][0], g.accs[1][1], st, csx, csy, la[u], lb[u], gx, gy);
        else seg_pass1(g.acc[2 * u][2 * pr], g.acc[2 * u][2 * pr + 1], g.acc[2 * u + 1][2 * pr], g.acc[2 * u + 1][2 * pr + 1], st, csx, csy, la[u], lb[u], gx, gy);
        ldsw<f32x2>(L_AGG + ((ul * 32 + seg) * 16 + i) * 8, sw ? f32x2{gy, gx} : f32x2{gx, gy});
      }
    }
    BAR();
  }
  cpx ag[8], L8, L64, pg;
  v4u lk0, lk1;
  cpx wk0, wk1, lg;
  const int unit0 = TYPE == 0 ? cg : 20 + 4 * (cg - 27);
  const bool scanw = TYPE != 1 && w < (TYPE == 0 ? 1 : 4);
  if (TYPE != 1) {
    if (scanw) {
      const f32x2 lm = ldsr<f32x2>(L_LAM + (w * 16 + i) * 8);
#pragma unroll
      for (int j = 0; j < 8; ++j) { const f32x2 t = ldsr<f32x2>(L_AGG + ((w * 32 + q * 8 + j) * 16 + i) * 8); ag[j] = {t.x, t.y}; }
      cpx L = {lm.x, lm.y};
      L = cmul(L, L); L = cmul(L, L); L8 = cmul(L, L);
      L = cmul(L8, L8); L = cmul(L, L); L64 = cmul(L, L);
      const cpx L128 = cmul(L64, L64), L256 = cmul(L128, L128);
      cpx t = ag[0];
#pragma unroll
      for (int j = 1; j < 8; ++j) t = cfma(L8, t, ag[j]);
      cpx inc = t;
      { cpx o = {__shfl_up(inc.r, 16), __shfl_up(inc.i, 16)}; if (q >= 1) inc = cfma(L64, o, inc); }
      { cpx o = {__shfl_up(inc.r, 32), __shfl_up(inc.i, 32)}; if (q >= 2) inc = cfma(L128, o, inc); }
      const unsigned poff = ((unit0 + w) * 16 + i) * 16;
      if (q == 3 && rb < 7) {
        v4u gr = {__float_as_uint(inc.r), c.T, __float_as_uint(inc.i), c.T2};
        __builtin_amdgcn_raw_buffer_store_b128(gr, c.rctl, WS_AGG + rb * 10240 + poff, 0, 16);
      }
      pg = {__shfl_up(inc.r, 16), __shfl_up(inc.i, 16)};
      if (q == 0) pg = {0.f, 0.f};
      wk0 = {1.f, 0.f}; wk1 = {1.f, 0.f}; lg = {1.f, 0.f};
#pragma unroll
      for (int k = 0; k < 6; ++k) { if (k < rb - 1 - q) wk0 = cmul(wk0, L256); if (k < rb - 5 - q) wk1 = cmul(wk1, L256); }
#pragma unroll
      for (int k = 0; k < 3; ++k) if (k < q) lg = cmul(lg, L64);
      lk0 = __builtin_amdgcn_raw_buffer_load_b128(c.rctl, WS_AGG + q * 10240 + poff, 0, 16);
      lk1 = __builtin_amdgcn_raw_buffer_load_b128(c.rctl, WS_AGG + (q + 4) * 10240 + poff, 0, 16);
    }
  }
  if (TYPE < 2) {
    f32x4 cs[4];
#pragma unroll
    for (int a = 0; a < 4; ++a) cs[a] = ldsr<f32x4>(L_CS + ((wn * 4 + (a ^ wt)) * 16 + 4 * q) * 4);
    const int step = 2 * cg + wn;
#pragma unroll
    for (int m = 0; m < 4; ++m) {
      const int ma = m ^ (2 * wn);
      const f32x2 st = ldsr<f32x2>(L_ST + (wt * 64 + (ma >> 1) * 32 + 8 * (i >> 2) + 4 * (m & 1) + (i & 3)) * 8);
      const float r2 = st.y * st.y;
      float h[2][4];
#pragma unroll
      for (int a = 0; a < 2; ++a)
#pragma unroll
        for (int r = 0; r < 4; ++r) h[a][r] = r2 * fmaf(-st.x, cs[a][r], g.acc[a][m][r]) * fmaf(-st.x, cs[a + 2][r], g.acc[a + 2][m][r]);
      const unsigned p0 = pk2(h[0][0], h[0][1]), p1 = pk2(h[0][2], h[0][3]), p2 = pk2(h[1][0], h[1][1]), p3 = pk2(h[1][2], h[1][3]);
      const v4u o = sw ? v4u{p2, p3, p0, p1} : v4u{p0, p1, p2, p3};
      __builtin_amdgcn_raw_buffer_store_b128(o, rh, l * 16 + (step * 4096 + ma * 1024), 0, 16);
    }
    WAITV(0);
    if (l < 2) st_flag(c, fbase + (step & 7) * 256 + (step >> 3) * 16 + l * 8);
  }
  STAMP(4);
  if (TYPE != 1) {
    if (scanw) {
      const unsigned poff = ((unit0 + w) * 16 + i) * 16;
      _Pragma("nounroll") for (int sp = 0; sp < SPIN_CAP; ++sp) {
      asm volatile("" ::: "memory");
        const bool ok = (q >= rb || (lk0.y == c.T && lk0.w == c.T2)) && (q + 4 >= rb || (lk1.y == c.T && lk1.w == c.T2));
        if (__all(ok)) break;
        __builtin_amdgcn_s_sleep(1);
        lk0 = __builtin_amdgcn_raw_buffer_load_b128(c.rctl, WS_AGG + q * 10240 + poff, 0, 16);
        lk1 = __builtin_amdgcn_raw_buffer_load_b128(c.rctl, WS_AGG + (q + 4) * 10240 + poff, 0, 16);
      }
      cpx cy = {0.f, 0.f};
      if (q < rb) cy = cmul(wk0, cpx{__uint_as_float(lk0.x), __uint_as_float(lk0.z)});
      if (q + 4 < rb) cy = cfma(wk1, cpx{__uint_as_float(lk1.x), __uint_as_float(lk1.z)}, cy);
      cy.r += shx(cy.r, 16); cy.i += shx(cy.i, 16);
      cy.r += shx(cy.r, 32); cy.i += shx(cy.i, 32);
      cpx z = cfma(lg, cy, pg);
#pragma unroll
      for (int j = 0; j < 8; ++j) {
        ldsw<f32x2>(L_CIN + ((w * 32 + q * 8 + j) * 16 + i) * 8, f32x2{z.r, z.i});
        z = cfma(L8, z, ag[j]);
      }
    }
    BAR();
    const unsigned tr = L_TR + w * 4096;
    const int r = (l >> 2) & 3, cc = l & 3;
#pragma unroll
    for (int u = 0; u < NU; ++u) {
      const int ul = TYPE == 0 ? 0 : 2 * wn + (u ^ (wt >> 1));
      const int step = 54 + unit0 + ul;
#pragma unroll
      for (int pr = 0; pr < NP; ++pr) {
        const int seg = (wt * 2 + (pr ^ wn)) * 4 + q;
        const f32x2 ci = ldsr<f32x2>(L_CIN + ((ul * 32 + seg) * 16 + i) * 8);
        v4u px, py;
        if (TYPE == 0) seg_pass2(g.accs[0][0], g.accs[0][1], g.accs[1][0], g.accs[1][1], la[u], lb[u], sw ? ci.y : ci.x, sw ? ci.x : ci.y, px, py);
        else seg_pass2(g.acc[2 * u][2 * pr], g.acc[2 * u][2 * pr + 1], g.acc[2 * u + 1][2 * pr], g.acc[2 * u + 1][2 * pr + 1], la[u], lb[u], sw ? ci.y : ci.x, sw ? ci.x : ci.y, px, py);
        ldsw<v4u>(tr + sw * 1280 + i * 80 + q * 16, px);
        ldsw<v4u>(tr + (sw ^ 1) * 1280 + i * 80 + q * 16, py);
        asm volatile("s_waitcnt lgkmcnt(0)" ::: "memory");
#pragma unroll
        for (int mm = 0; mm < 2; ++mm) {
          const unsigned a = tr + (4 * q + r) * 80 + cc * 16 + mm * 8;
          const bf16x8 v = trread2(a, a + 1280);
          __builtin_amdgcn_raw_buffer_store_b128(__builtin_bit_cast(v4u, v), rh, l * 16 + (step * 4096 + ((pr ^ wn) * 2 + mm) * 1024), 0, 16);
        }
        asm volatile("s_waitcnt lgkmcnt(0)" ::: "memory");
      }
    }
    WAITV(0);
    if (TYPE == 0) {
      const int step = 54 + cg;
      if (l == 0) st_flag(c, fbase + (step & 7) * 256 + (step >> 3) * 16 + wn * 8);
    } else {
      const int step = 54 + unit0 + 2 * wn + (l >> 1);
      if (l < 4) st_flag(c, fbase + (step & 7) * 256 + (step >> 3) * 16 + (l & 1) * 8);
    }
  }
  STAMP(5);
  if (TYPE == 1) {
    if (gi == 55) {
      _Pragma("nounroll") for (int sp = 0; sp < SPIN_CAP; ++sp) {
      asm volatile("" ::: "memory");
        bool ok = true;
#pragma unroll
        for (int k = 0; k < 4; ++k) {
          const v4u t = __builtin_amdgcn_raw_buffer_load_b128(c.rctl, WS_STAT + (k * 512 + w * 64 + l) * 16, 0, 16);
          ok = ok && t.y == c.T && t.w == c.T2;
        }
        if (__all(ok)) break;
        __builtin_amdgcn_s_sleep(1);
      }
      BAR();
      if (w == 0 && l == 0) __builtin_amdgcn_raw_buffer_store_b32(c.T, c.rctl, WS_EPOCH, 0, 16);
    }
  }
}

DI void poll_flags(const Ctx& c, unsigned off, bool need) {
  _Pragma("nounroll") for (int sp = 0; sp < SPIN_CAP; ++sp) {
      asm volatile("" ::: "memory");
    const v2u t = __builtin_amdgcn_raw_buffer_load_b64(c.rctl, off, 0, 16);
    if (__all(!need || (t.x == c.T && t.y == c.T2))) break;
    __builtin_amdgcn_s_sleep(1);
  }
  __builtin_amdgcn_fence(__ATOMIC_ACQUIRE, "wavefront");
}

DI void phase2(Ctx& c, const u16* dsk, float* out) {
  const int l = c.lane, i = l & 15, q = l >> 4, w = c.w;
  const int rt = c.cg >> 3, ct = c.cg & 7;
  const int rtile = c.rb * 4 + rt;
  const __amdgpu_buffer_rsrc_t rh = __builtin_amdgcn_make_buffer_rsrc(c.ws + WS_H + rtile * H_RT, 0, H_RT, 0x00020000);
  const __amdgpu_buffer_rsrc_t rw = __builtin_amdgcn_make_buffer_rsrc(c.ws + WS_W2T + ct * W2T_CT, 0, W2T_CT, 0x00020000);
  poll_flags(c, WS_FLAGW + (ct * 8 + w) * 128 + (l & 15) * 8, l < 12 && w + 8 * l < NS2);
  constexpr int PW = 4, PH = 3;
  v4u wf[12][5], hf[12][4];
  auto ldw = [&](int j) {
#pragma unroll
    for (int a = 0; a < 5; ++a) wf[j][a] = j < 11 ? __builtin_amdgcn_raw_buffer_load_b128(rw, l * 16 + a * 1024, (w + 8 * j) * 5120, 16) : __builtin_amdgcn_raw_buffer_load_b128(rw, l * 16 + a * 1024 + (w + 88) * 5120, 0, 16);
  };
  auto ldh = [&](int j) {
#pragma unroll
    for (int m = 0; m < 4; ++m) hf[j][m] = j < 11 ? __builtin_amdgcn_raw_buffer_load_b128(rh, l * 16 + m * 1024, (w + 8 * j) * 4096, 16) : __builtin_amdgcn_raw_buffer_load_b128(rh, l * 16 + m * 1024 + (w + 88) * 4096, 0, 16);
  };
#pragma unroll
  for (int j = 0; j < PW; ++j) ldw(j);
  SB0();
  poll_flags(c, WS_FLAGH + rtile * 2048 + w * 256 + (l & 31) * 8, l < 24 && w + 8 * (l >> 1) < NS2);
  STAMP(6);
#pragma unroll
  for (int j = 0; j < PH; ++j) ldh(j);
  SB0();
  f32x4 acc[5][4];
#pragma unroll
  for (int a = 0; a < 5; ++a)
#pragma unroll
    for (int m = 0; m < 4; ++m) acc[a][m] = f32x4{0.f, 0.f, 0.f, 0.f};
#pragma unroll
  for (int j = 0; j < 12; ++j) {
#pragma unroll
    for (int a = 0; a < 5; ++a)
#pragma unroll
      for (int m = 0; m < 4; ++m) acc[a][m] = mfma(__builtin_bit_cast(bf16x8, wf[j][a]), __builtin_bit_cast(bf16x8, hf[j][m]), acc[a][m]);
    SB0();
    if (j + PH < 12) ldh(j + PH);
    if (j + PW < 12) ldw(j + PW);
    SB0();
  }
  STAMP(7);
  f32x2 st[3]; v2u xv[3], dv[3];
#pragma unroll
  for (int k = 0; k < 3; ++k) {
    const int tile = (w + 8 * k) < 20 ? w + 8 * k : w, dt = tile >> 2, m = tile & 3;
    const int tl = rt * 64 + (m >> 1) * 32 + 8 * (i >> 2) + 4 * (m & 1) + (i & 3), d = ct * 80 + dt * 16 + 4 * q;
    st[k] = ldsr<f32x2>(L_ST + tl * 8);
    xv[k] = *(const v2u*)(c.x + (size_t)(c.rb * 256 + tl) * D_ + d);
    dv[k] = *(const v2u*)(dsk + d);
  }
  BAR();
#pragma unroll
  for (int a = 0; a < 5; ++a)
#pragma unroll
    for (int m = 0; m < 4; ++m) ldsw<f32x4>(w * 20480 + (a * 4 + m) * 1024 + l * 16, acc[a][m]);
  BAR();
#pragma unroll
  for (int k = 0; k < 3; ++k) {
    const int tile = w + 8 * k;
    if (tile < 20) {
      f32x4 s = ldsr<f32x4>(tile * 1024 + l * 16);
#pragma unroll
      for (int v = 1; v < 8; ++v) s += ldsr<f32x4>(v * 20480 + tile * 1024 + l * 16);
      const int dt = tile >> 2, m = tile & 3;
      const int tl = rt * 64 + (m >> 1) * 32 + 8 * (i >> 2) + 4 * (m & 1) + (i & 3), d = ct * 80 + dt * 16 + 4 * q;
      const unsigned x0 = xv[k].x, x1 = xv[k].y, d0 = dv[k].x, d1 = dv[k].y;
      const float xf[4] = {bf2f(x0), bfhi(x0), bf2f(x1), bfhi(x1)}, df[4] = {bf2f(d0), bfhi(d0), bf2f(d1), bfhi(d1)};
      f32x4 o;
#pragma unroll
      for (int e = 0; e < 4; ++e) {
        const float xn = rbf(rbf(xf[e] - st[k].x) * st[k].y);
        o[e] = xf[e] + s[e] + rbf(xn * df[e]);
      }
      *(f32x4*)(out + (size_t)(c.rb * 256 + tl) * D_ + d) = o;
    }
  }
}

__global__ void __launch_bounds__(512, 2)
fused_kernel(const u16* x, const u16* lre, const u16* lim, const u16* bre, const u16* bim, const u16* cre, const u16* cim, const u16* ldt,
             const u16* dsk, const u16* wg, const u16* wu, const u16* wd, float* out, char* ws) {
  extern __shared__ __attribute__((aligned(16))) char smem[];
  Ctx c;
  c.x = x; c.bre = bre; c.bim = bim; c.cre = cre; c.cim = cim; c.wg = wg; c.wu = wu; c.wd = wd; c.ws = ws;
  c.lane = threadIdx.x & 63;
  c.w = __builtin_amdgcn_readfirstlane(threadIdx.x >> 6);
  const int xc = blockIdx.x & 7, ix = blockIdx.x >> 3;
  c.rb = (xc >> 1) * 2 + (ix >> 4); c.cg = (xc & 1) * 16 + (ix & 15);
  c.rctl = __builtin_amdgcn_make_buffer_rsrc(ws, 0, WS_CTL_END, 0x00020000);
  STAMP(0);
  if (c.cg < 20) phase1<0>(c, lre, lim, ldt);
  else if (c.cg < 27) phase1<1>(c, lre, lim, ldt);
  else phase1<2>(c, lre, lim, ldt);
  phase2(c, dsk, out);
}

extern "C" void kernel_launch(void* const* d_in, const int* in_sizes, int n_in, void* d_out, int out_size, void* d_ws, size_t ws_size,
                              hipStream_t stream) {
  (void)in_sizes; (void)n_in; (void)out_size; (void)ws_size;
  hipFuncSetAttribute((const void*)fused_kernel, hipFuncAttributeMaxDynamicSharedMemorySize, LDS_BYTES);
  fused_kernel<<<256, 512, LDS_BYTES, stream>>>((const u16*)d_in[0], (const u16*)d_in[1], (const u16*)d_in[2], (const u16*)d_in[3], (const u16*)d_in[4],
                                                (const u16*)d_in[5], (const u16*)d_in[6], (const u16*)d_in[7], (const u16*)d_in[8], (const u16*)d_in[10],
                                                (const u16*)d_in[11], (const u16*)d_in[12], (float*)d_out, (char*)d_ws);
}
```

```cpp
#include <hip/hip_runtime.h>
#include <stdint.h>

#define GAS __attribute__((address_space(1)))
#define LAS __attribute__((address_space(3)))
#define DI __device__ __forceinline__
typedef unsigned short u16;
typedef short bf16x8 __attribute__((ext_vector_type(8)));
typedef short s16x4 __attribute__((ext_vector_type(4)));
typedef __bf16 bf2 __attribute__((ext_vector_type(2)));
typedef float f32x4 __attribute__((ext_vector_type(4)));
typedef float f32x2 __attribute__((ext_vector_type(2)));
typedef unsigned v4u __attribute__((ext_vector_type(4)));
typedef unsigned v2u __attribute__((ext_vector_type(2)));

#define XMAP 2
#define SCHK 7
#define L2PF 0
#define NWR 4
#define PH 2
#define NWP 7
#define PWD 3

constexpr int L_ = 2048, D_ = 640, F_ = 1728;
constexpr int NS2 = 94;
constexpr unsigned WS_EPOCH = 0;
constexpr unsigned WS_STAT = 4096;
constexpr unsigned WS_AGG = WS_STAT + 2048 * 16;
constexpr unsigned WS_FLAGH = WS_AGG + 8 * 640 * 16;
constexpr unsigned WS_FLAGW = WS_FLAGH + 32 * 8 * 256;
constexpr unsigned WS_CTL_END = WS_FLAGW + 8 * 8 * 128;
constexpr unsigned WS_W2T = 262144;
constexpr unsigned W2T_CT = NS2 * 5 * 1024;
constexpr unsigned WS_H = WS_W2T + 8 * W2T_CT;
constexpr unsigned H_RT = NS2 * 4 * 1024;
constexpr unsigned WS_END = WS_H + 32 * H_RT;
constexpr unsigned OOB = 0x70000000u;
static_assert(WS_CTL_END <= WS_W2T, "ws map");
constexpr unsigned LX(int k) { return k * 32768; }
constexpr unsigned LWB(int k) { return 98304 + ((k + 1) % 3) * 16384; }
constexpr unsigned LWS(int k) { return 147456 + k * 4096; }
constexpr unsigned L_ST = 159744;
constexpr unsigned L_CS = L_ST + 2048;
constexpr unsigned L_CSS = L_CS + 512;
constexpr unsigned L_COEF = L_CSS + 1024;
constexpr unsigned L_POW = LWS(0);
constexpr unsigned L_AGG = LWS(1);
constexpr unsigned L_DUMMY = LWS(2);
constexpr unsigned LDS_BYTES = 163840;
static_assert(L_COEF + 512 <= LDS_BYTES && LWB(2) == LX(2) + 32768, "lds map");
constexpr unsigned tile_base(int ch, int j) {
  return ch < 3 ? (j < 4 ? LX((ch + 1) % 3) + j * 1024 : LWB((ch + 1) % 3)) : (j < 3 ? LWB(j) + 8192 : LWS(0) + (j - 3) * 1024);
}
constexpr unsigned tile_wstride(int ch, int j) { return ch < 3 ? (j < 4 ? 4096 : 1024) : (j < 3 ? 1024 : 2048); }
constexpr int SPIN_CAP = 1 << 15;

DI float bf2f(unsigned v) { return __uint_as_float(v << 16); }
DI float bfhi(unsigned v) { return __uint_as_float(v & 0xffff0000u); }
DI unsigned pk2(float a, float b) { bf2 v = {(__bf16)a, (__bf16)b}; return __builtin_bit_cast(unsigned, v); }
DI float rbf(float a) { return bf2f(pk2(a, 0.f) & 0xffffu); }
DI void mfa(f32x4& c, bf16x8 a, bf16x8 b) { asm volatile("v_mfma_f32_16x16x32_bf16 %0, %1, %2, %0" : "+v"(c) : "v"(a), "v"(b)); }
DI void mfz(f32x4& c, bf16x8 a, bf16x8 b) { asm volatile("v_mfma_f32_16x16x32_bf16 %0, %1, %2, 0" : "=&v"(c) : "v"(a), "v"(b)); }
template <bool Z> DI void mf(f32x4& c, bf16x8 a, bf16x8 b) { if (Z) mfz(c, a, b); else mfa(c, a, b); }
#define MFMA_DRAIN() asm volatile("s_nop 7\n\ts_nop 1" ::: "memory")
DI v4u mkrsrc(const void* p, unsigned bytes) {
  uint64_t a = (uint64_t)p;
  v4u r; r.x = (unsigned)a; r.y = (unsigned)(a >> 32) & 0xffffu; r.z = bytes; r.w = 0x00020000u;
  return r;
}
template <unsigned SI, unsigned LI> DI void glds16(v4u rs, unsigned voff, unsigned sb, unsigned lb) {
  unsigned t;
  asm volatile("s_add_u32 m0, %4, %6\n\ts_add_u32 %0, %3, %5\n\tbuffer_load_dwordx4 %1, %2, %0 offen lds"
               : "=&s"(t) : "v"(voff), "s"(rs), "s"(sb), "s"(lb), "i"(SI), "i"(LI) : "memory", "scc");
}
template <unsigned SI, unsigned LI> DI void glds16sc1(v4u rs, unsigned voff, unsigned lb) {
  unsigned t;
  asm volatile("s_add_u32 m0, %3, %5\n\ts_mov_b32 %0, %4\n\tbuffer_load_dwordx4 %1, %2, %0 offen sc1 lds"
               : "=&s"(t) : "v"(voff), "s"(rs), "s"(lb), "i"(SI), "i"(LI) : "memory", "scc");
}
template <class T> DI T ldsr(unsigned a) { return *(LAS T*)(uintptr_t)a; }
template <class T> DI void ldsw(unsigned a, T v) { *(LAS T*)(uintptr_t)a = v; }
DI s16x4 trread(unsigned a) { return __builtin_amdgcn_ds_read_tr16_b64_v4i16((LAS s16x4*)(uintptr_t)a); }
DI bf16x8 cat(s16x4 lo, s16x4 hi) { return __builtin_shufflevector(lo, hi, 0, 1, 2, 3, 4, 5, 6, 7); }
template <int N> DI void waitv() { asm volatile("s_waitcnt vmcnt(%0)" ::"n"(N) : "memory"); }
#define BAR() asm volatile("s_waitcnt lgkmcnt(0)\n\ts_barrier" ::: "memory")
#define LGKM0() __builtin_amdgcn_s_waitcnt(0xc07f)
#define SB0() __builtin_amdgcn_sched_barrier(0)
#define PIN(x) asm volatile("" : "+v"(x))
#define SPIN_LOOP _Pragma("nounroll") for (int sp = 0; sp < SPIN_CAP; ++sp)
template <int N> DI float ror(float v) { return __builtin_bit_cast(float, __builtin_amdgcn_update_dpp(0, __builtin_bit_cast(int, v), 0x120 + N, 0xf, 0xf, false)); }
DI void sw16(float v, float& e, float& o) { unsigned u = __float_as_uint(v); auto r = __builtin_amdgcn_permlane16_swap(u, u, false, false); e = __uint_as_float(r[0]); o = __uint_as_float(r[1]); }
DI void sw32(float v, float& lo, float& hi) { unsigned u = __float_as_uint(v); auto r = __builtin_amdgcn_permlane32_swap(u, u, false, false); lo = __uint_as_float(r[0]); hi = __uint_as_float(r[1]); }
DI float sumq(float v) { float a, b; sw16(v, a, b); v = a + b; sw32(v, a, b); return a + b; }
DI float sumr(float v) { v += ror<8>(v); v += ror<4>(v); v += ror<2>(v); v += ror<1>(v); return v; }
DI void gatherq(float v, float (&a)[4]) { float e, o; sw16(v, e, o); sw32(e, a[0], a[2]); sw32(o, a[1], a[3]); }
typedef f32x2 cpx;
DI cpx cfma(cpx a, cpx b, cpx c) {
  cpx r;
  asm("v_pk_fma_f32 %0, %1, %2, %3 op_sel:[1,1,0] op_sel_hi:[1,0,1] neg_lo:[1,0,0]\n\tv_pk_fma_f32 %0, %1, %2, %0 op_sel_hi:[0,1,1]" : "=&v"(r) : "v"(a), "v"(b), "v"(c));
  return r;
}
DI cpx cmul(cpx a, cpx b) {
  cpx r;
  asm("v_pk_mul_f32 %0, %1, %2 op_sel:[1,1] op_sel_hi:[1,0] neg_lo:[1,0]\n\tv_pk_fma_f32 %0, %1, %2, %0 op_sel_hi:[0,1,1]" : "=&v"(r) : "v"(a), "v"(b));
  return r;
}
DI cpx csel(bool p, cpx a) { return cpx{p ? a.x : 1.f, p ? a.y : 0.f}; }
DI cpx czero(bool p, cpx a) { return cpx{p ? 0.f : a.x, p ? 0.f : a.y}; }
template <int V> struct IC { static constexpr int v = V; };

#define STAMP(k) do {} while (0)
#define CSTAMP(k) do {} while (0)
struct Ctx {
  const u16 *x, *bre, *bim, *cre, *cim, *wg, *wu, *wd;
  char* ws;
  __amdgpu_buffer_rsrc_t rws;
  v2u tag;
  unsigned T, T2;
  int lane, w, rb, cg, rt, ct, shr;
  unsigned vw2, vh2, vfl;
  v4u wf[NWR][5], hf[2][4];
  v2u fl;
  unsigned vfs;
  bool bump;
};
DI v4u ld128(const Ctx& c, unsigned vo, unsigned so = 0) { return __builtin_amdgcn_raw_buffer_load_b128(c.rws, vo, so, 16); }
DI v2u ld64(const Ctx& c, unsigned vo) { return __builtin_amdgcn_raw_buffer_load_b64(c.rws, vo, 0, 16); }
DI void st128(const Ctx& c, v4u v, unsigned vo) { __builtin_amdgcn_raw_buffer_store_b128(v, c.rws, vo, 0, 16); }
DI void st_flag(const Ctx& c, unsigned vo) { __builtin_amdgcn_raw_buffer_store_b64(c.tag, c.rws, vo, 0, 16); }

DI float fexp(float x) {
  const float t = x * 1.44269502162933349609375f;
  const float r = fmaf(x, 1.925963033500011e-8f, fmaf(x, 1.44269502162933349609375f, -t));
  const float e = __builtin_amdgcn_exp2f(t);
  return fmaf(e, r * 0.693147182464599609375f, e);
}
DI void fsincos(float z, float& sn, float& cs) {
  const float k = rintf(z * 0.63661977236758134f);
  float r = fmaf(k, -1.5707963705062866f, z);
  r = fmaf(k, 4.3711388286737929e-08f, r);
  r = fmaf(k, 1.7151245100058819e-15f, r);
  const float r2 = r * r;
  const float sp = fmaf(r * r2, fmaf(r2, fmaf(r2, -1.9515295891e-4f, 8.3321608736e-3f), -1.6666654611e-1f), r);
  const float cp = fmaf(r2 * r2, fmaf(r2, fmaf(r2, 2.443315711809948e-5f, -1.388731625493765e-3f), 4.166664568298827e-2f), fmaf(r2, -0.5f, 1.f));
  const int n = (int)k;
  sn = (n & 1) ? cp : sp; cs = (n & 1) ? sp : cp;
  if (n & 2) sn = -sn;
  if ((n + 1) & 2) cs = -cs;
}
template <int J> DI void ldw(const Ctx& c, v4u (&d)[5]) {
  const unsigned vo = J == 11 ? (c.w < 6 ? c.vw2 : OOB) : c.vw2;
#pragma unroll
  for (int a = 0; a < 4; ++a) d[a] = ld128(c, vo + a * 1024, J * 40960);
  d[4] = ld128(c, vo, J * 40960 + 4096);
}
template <int J> DI void ldh(const Ctx& c, v4u (&d)[4]) {
  const unsigned vo = J == 11 ? (c.w < 6 ? c.vh2 : OOB) : c.vh2;
#pragma unroll
  for (int m = 0; m < 4; ++m) d[m] = ld128(c, vo + m * 1024, J * 32768);
}

template <int TYPE> struct G1 {
  static constexpr int NU = TYPE == 0 ? 1 : 3;
  static constexpr int NG = TYPE == 0 ? 10 : 6;
  static constexpr int NR = TYPE == 0 ? 14 : 8;
  static constexpr int NP = TYPE == 0 ? 7 : 6;
  struct Frags { bf16x8 x[TYPE == 0 ? 4 : 2]; s16x4 wl[4], wh[4]; bf16x8 s[2 * NU]; };
  v4u rx, rw0, rw1, rs;
  unsigned vx0, vx1, vw, vs;
  unsigned sx, sw, ss, ss2;
  unsigned ax[2][2][2], aw[4], as[NU][2];
  f32x4 acc[4][4];
  f32x4 accs[2 * NU][2];
  f32x4 cs0, cs1, cs2;
  bf16x8 ones;
  int w;
  unsigned w1k, w4k, w1k6, vs6;

  DI void init_stage(const Ctx& c, int unit0) {
    const int l = c.lane;
    w = c.w; w1k = w * 1024; w4k = w * 4096;
    rx = mkrsrc(c.x, L_ * D_ * 2);
    const unsigned ch = (l & 7) ^ ((l >> 3) & 3);
    vx0 = (l >> 3) * 1280 + ch * 16;
    vx1 = (l >> 3) * 1280 + (ch ^ 4) * 16;
    sx = (c.rb * 256 + w * 32) * 1280;
    rs = mkrsrc((w & 2) ? c.bim : c.bre, 640 * 640 * 2);
    vs = (l >> 3) * 1280 + (((l & 7) ^ (l >> 3)) * 16);
    vs6 = w < 4 ? vs : OOB; w1k6 = w < 4 ? w1k : 65536;
    if (TYPE == 0) {
      rw0 = mkrsrc(c.wg, D_ * F_ * 2); rw1 = mkrsrc(c.wu, D_ * F_ * 2);
      const unsigned key2 = ((l >> 4) & 1) | ((w & 1) << 1);
      vw = (l >> 3) * 3456 + (((l & 7) ^ (key2 << 1)) * 16);
      sw = w * 8 * 3456 + c.cg * 128;
      ss2 = (unit0 * 16 + (w & 1) * 8) * 1280;
    } else {
      ss = (min(unit0 + (w >> 2), 39) * 16 + (w & 1) * 8) * 1280;
      ss2 = (min(unit0 + 2, 39) * 16 + (w & 1) * 8) * 1280;
    }
  }
  DI void init_read(const Ctx& c) {
    const int l = c.lane, i = l & 15, q = l >> 4;
    const int wt = w & 3, wn = w >> 2;
#pragma unroll
    for (int kk = 0; kk < 2; ++kk) {
      const unsigned ch = (((kk * 4 + q) ^ (i & 7)) * 16);
#pragma unroll
      for (int pr = 0; pr < (TYPE == 0 ? 2 : 1); ++pr) {
        ax[0][pr][kk] = (wt * 64 + (pr ^ wn) * 32 + 8 * (i >> 2) + (i & 3)) * 128 + ch;
        ax[1][pr][kk] = ax[0][pr][kk] + 65536;
        PIN(ax[0][pr][kk]); PIN(ax[1][pr][kk]);
      }
#pragma unroll
      for (int u = 0; u < NU; ++u) {
        as[u][kk] = (TYPE == 0 ? LWS(0) : LWB(2) + ((u + (w >> 1)) % 3) * 4096) + i * 128 + ch;
        PIN(as[u][kk]);
      }
    }
    if (TYPE == 0) {
      const int r = (l >> 2) & 3, cc = l & 3;
      const unsigned key2 = ((r >> 1) & 1) | ((q & 1) << 1);
#pragma unroll
      for (int a = 0; a < 4; ++a) {
        const int act = a ^ wt;
        aw[a] = LWB(2) + (act >> 1) * 8192 + (8 * q + r) * 128 + (((2 * wn + (act & 1)) ^ key2) * 32) + cc * 8;
        PIN(aw[a]);
      }
      cs1 = f32x4{0.f, 0.f, 0.f, 0.f}; cs2 = f32x4{0.f, 0.f, 0.f, 0.f};
      PIN(cs1); PIN(cs2);
    }
    ones = bf16x8{0x3f80, 0x3f80, 0x3f80, 0x3f80, 0x3f80, 0x3f80, 0x3f80, 0x3f80};
    PIN(ones);
  }
  template <int P, int S> DI void piece() {
    constexpr int K = S % 3;
    if constexpr (P < 4) glds16<S * 128 + P * 10240, LX(K) + P * 1024>(rx, (P & 1) ? vx1 : vx0, sx, w4k);
    else if constexpr (P < 6) {
      if constexpr (TYPE == 0) glds16<S * (64 * 3456), LWB(K) + (P - 4) * 8192>(P == 4 ? rw0 : rw1, vw, sw, w1k);
      else if constexpr (P == 4) glds16<S * 128, LWB(K)>(rs, vs, ss, w1k);
    } else {
      glds16<S * 128, (TYPE == 0 ? LWS(K) : LWB(K) + 8192)>(rs, vs6, ss2, w1k6);
    }
  }
  template <int S> DI void half0() { piece<6, S>(); piece<0, S>(); piece<4, S>(); piece<1, S>(); }
  template <int S> DI void half1() { piece<2, S>(); piece<5, S>(); piece<3, S>(); }

  template <int K, int KK, int R> DI void rd(Frags& f) {
    constexpr unsigned ow = LWB(K) - LWB(2), ox = K == 1 ? 32768 : 0;
    if constexpr (TYPE == 0) {
      if (R == 0) f.wl[0] = trread(aw[0] + (ow + KK * 4096));
      else if (R == 1) f.wh[0] = trread(aw[0] + (ow + KK * 4096 + 512));
      else if (R < 6) f.x[(R - 2) & 3] = ldsr<bf16x8>(ax[K == 2][((R - 2) >> 1) & 1][KK] + (ox + ((R - 2) & 1) * 512));
      else if (R < 12) {
        constexpr int a = ((R - 4) >> 1) & 3;
        if (R & 1) f.wh[a] = trread(aw[a] + (ow + KK * 4096 + 512)); else f.wl[a] = trread(aw[a] + (ow + KK * 4096));
      } else f.s[R & 1] = ldsr<bf16x8>(as[0][KK] + (K * 4096 + (R & 1) * 2048));
    } else {
      if (R == 1 || R == 2) f.x[(R - 1) & 1] = ldsr<bf16x8>(ax[K == 2][0][KK] + (ox + ((R - 1) & 1) * 512));
      else { constexpr int a = R == 0 ? 0 : R - 2; f.s[a] = ldsr<bf16x8>(as[a >> 1][KK] + (ow + (a & 1) * 2048)); }
    }
  }
  template <bool Z, int M> DI void mm(const Frags& f) {
    if constexpr (TYPE == 0 && M < 16) mf<Z>(acc[M >> 2][M & 3], cat(f.wl[M >> 2], f.wh[M >> 2]), f.x[M & 3]);
    else { constexpr int a = (M - (TYPE == 0 ? 16 : 0)) >> 1; mf<Z>(accs[a][M & 1], f.x[M & 1], f.s[a]); }
  }
  template <int SUB, int G, class H> DI void group(const Frags& cur, Frags& nxt, H& hook) {
    constexpr int N = SUB + 1, K = (N >> 1) % 3, KK = N & 1;
    constexpr bool RD = N < 20;
    mm<SUB == 0, 2 * G>(cur);
    if constexpr (RD && 2 * G < NR) rd<K, KK, 2 * G>(nxt);
    mm<SUB == 0, 2 * G + 1>(cur);
    if constexpr (RD && 2 * G + 1 < NR) rd<K, KK, 2 * G + 1>(nxt);
    if constexpr (TYPE == 0) {
      if (G == 0) mf<SUB == 0>(cs0, ones, cat(cur.wl[0], cur.wh[0]));
      if (G == 2) { if (__builtin_expect(w == (SUB & 7), 0)) { mfa(cs1, ones, cur.s[0]); mfa(cs2, ones, cur.s[1]); } }
    } else {
      if (G == 0) { mf<SUB == 0>(cs1, ones, cur.s[0]); mf<SUB == 0>(cs2, ones, cur.s[1]); }
    }
    hook(IC<G>{});
    SB0();
    if constexpr (G + 1 < NG) group<SUB, G + 1>(cur, nxt, hook);
  }
  template <int SUB, class H> DI void part(const Frags& cur, Frags& nxt, H hook) { group<SUB, 0>(cur, nxt, hook); }
  template <int R> DI void rdall(Frags& f) { rd<0, 0, R>(f); if constexpr (R + 1 < NR) rdall<R + 1>(f); }
};

template <int NU> struct Side {
  unsigned xr[5], pa, pb, pd;
  float v[10], s, dt, e, sn, cs;
  cpx lam[NU], L8[NU], L32[NU], u0[NU], u1[NU], f1[NU], W0[NU], W1[NU], wk0[NU], wk1[NU], t0, t1, t2;
  float u2;
  unsigned a_mu, a_cs, v_hg, v_fg, a_st, a_css[NU], a_aw[NU], a_ar[NU], v_pub[NU], v_lk0[NU], v_lk1[NU], v_hs[NU], v_fs, a_tw, a_tr;
  static constexpr int PERU = 16;
  static constexpr int NCH = 8 + PERU * NU + 5;
  template <int N> DI void tick(Ctx& c, int srow, int unit0) {
    const int l = c.lane, i = l & 15, q = l >> 4, w = c.w, wt = w & 3, wn = w >> 2, grp = wt * 2 + wn;
    if constexpr (N == 0) {
#pragma unroll
      for (int j = 0; j < 5; ++j) { v[2 * j] = bf2f(xr[j]); v[2 * j + 1] = bfhi(xr[j]); PIN(v[2 * j]); PIN(v[2 * j + 1]); }
    } else if constexpr (N == 1) {
      s = ((v[0] + v[1]) + (v[2] + v[3])) + ((v[4] + v[5]) + (v[6] + v[7])) + (v[8] + v[9]); PIN(s);
    } else if constexpr (N == 2) { s = sumr(s); PIN(s); }
    else if constexpr (N == 3) { s = sumq(s) * (1.f / 640.f); PIN(s); }
    else if constexpr (N == 4) {
#pragma unroll
      for (int j = 0; j < 10; ++j) { v[j] -= s; PIN(v[j]); }
    } else if constexpr (N == 5) {
      e = v[0] * v[0];
#pragma unroll
      for (int j = 1; j < 10; ++j) e = fmaf(v[j], v[j], e);
      PIN(e);
    } else if constexpr (N == 6) { e = sumr(e); PIN(e); }
    else if constexpr (N == 7) {
      const float var = rbf(sumq(e) * (1.f / 640.f));
      const unsigned pkd = pk2(s, __builtin_amdgcn_sqrtf(rbf(var + 1e-5f)));
      st128(c, v4u{pkd, c.T, pkd, c.T2}, l == 0 ? WS_STAT + srow * 16 : OOB);
    } else if constexpr (N < 8 + PERU * NU) {
      constexpr int u = (N - 8) / PERU, k = (N - 8) % PERU;
      const int ul = NU == 1 ? 0 : (u + (w >> 1)) % 3, ua = unit0 + ul;
      const int e1 = grp - 2 - 2 * q, k0 = c.rb - 1 - q;
      const unsigned pw = L_POW + (ul * 16 + i) * 8;
      if constexpr (k == 0) { if (NU == 1) { dt = rbf(fexp(bf2f(pd))); PIN(dt); } }
      else if constexpr (k == 1) { if (NU == 1) { e = fexp(dt * bf2f(pa)); PIN(e); } }
      else if constexpr (k == 2) { if (NU == 1) { fsincos(dt * bf2f(pb), sn, cs); PIN(sn); PIN(cs); } }
      else if constexpr (k == 3) {
        if (NU == 1) { lam[u] = cpx{e * cs, e * sn}; t0 = cmul(lam[u], lam[u]); t0 = cmul(t0, t0); L8[u] = cmul(t0, t0); t0 = cmul(L8[u], L8[u]); L32[u] = cmul(t0, t0); }
        else { lam[u] = ldsr<cpx>(pw); L8[u] = ldsr<cpx>(pw + 512); t0 = ldsr<cpx>(pw + 1024); L32[u] = ldsr<cpx>(pw + 1536); }
      }
      else if constexpr (k == 4) { f1[u] = cmul(csel(q & 1, L8[u]), csel(q & 2, t0)); PIN(lam[u]); PIN(L8[u]); PIN(t0); PIN(L32[u]); PIN(f1[u]); }
      else if constexpr (k == 5) {
        u1[u] = czero(q < 2, csel(q == 3, L8[u])); PIN(u1[u]);
        u2 = q == 3 ? 1.f : 0.f; PIN(u2);
      } else if constexpr (k == 6) { u0[u] = czero(q == 0, q == 3 ? t0 : csel(q == 2, L8[u])); PIN(u0[u]); }
      else if constexpr (k == 7) {
        if (NU == 1) { t0 = cmul(L32[u], L32[u]); t1 = cmul(t0, t0); PIN(t0); PIN(t1); } else { t0 = ldsr<cpx>(pw + 2048); t1 = ldsr<cpx>(pw + 2560); }
      }
      else if constexpr (k == 8) { W1[u] = cmul(csel(e1 & 1, L32[u]), csel(e1 & 2, t0)); t2 = cmul(csel(grp & 1, L32[u]), csel(grp & 2, t0)); PIN(W1[u]); PIN(t2); }
      else if constexpr (k == 9) { W1[u] = cmul(W1[u], csel(e1 & 4, t1)); t2 = cmul(t2, csel(grp & 4, t1)); PIN(W1[u]); PIN(t2); }
      else if constexpr (k == 10) { W0[u] = czero(e1 < -1, csel(e1 >= 0, cmul(W1[u], L32[u]))); W1[u] = czero(e1 < 0, W1[u]); PIN(W0[u]); PIN(W1[u]); }
      else if constexpr (k == 11) { t0 = cmul(t1, t1); t1 = cmul(t0, t0); wk1[u] = cmul(t2, csel(k0 & 1, t0)); PIN(t1); PIN(wk1[u]); }
      else if constexpr (k == 12) { wk1[u] = cmul(wk1[u], csel(k0 & 2, t1)); t1 = cmul(t1, t1); PIN(t1); PIN(wk1[u]); }
      else if constexpr (k == 13) { wk0[u] = czero(k0 < 0, cmul(wk1[u], csel(k0 & 4, t1))); wk1[u] = czero(k0 < 4, wk1[u]); PIN(wk0[u]); PIN(wk1[u]); }
      else if constexpr (k == 14) {
        a_css[u] = L_CSS + (NU == 1 ? q * 128 : ul * 128) + i * 4;
        a_ar[u] = L_AGG + (ul * 16 + i) * 64 + q * 16;
        a_aw[u] = q == 3 ? L_AGG + (ul * 16 + i) * 64 + grp * 8 : L_DUMMY + l * 8;
        v_hs[u] = ua < 40 ? WS_H + (c.rb * 4 + wt) * H_RT + (54 + ua) * 4096 + wn * 2048 + l * 16 : OOB;
        PIN(a_css[u]); PIN(a_ar[u]); PIN(a_aw[u]); PIN(v_hs[u]);
      } else {
        const unsigned po = WS_AGG + (ua * 16 + i) * 16;
        v_pub[u] = (q == 3 && ua < 40) ? po + c.rb * 10240 : OOB;
        v_lk0[u] = (k0 >= 0 && ua < 40) ? po + q * 10240 : OOB;
        v_lk1[u] = (k0 >= 4 && ua < 40) ? po + (q + 4) * 10240 : OOB;
        PIN(v_pub[u]); PIN(v_lk0[u]); PIN(v_lk1[u]);
      }
    } else {
      constexpr int k = N - 8 - PERU * NU;
      const unsigned fbase = WS_FLAGH + (c.rb * 4 + wt) * 2048;
      if constexpr (k == 0) {
        if (NU == 1) {
          const int step = 2 * c.cg + wn;
          a_mu = L_ST + (wt * 64 + wn * 32 + 8 * (i >> 2) + (i & 3)) * 8;
          a_cs = L_CS + wn * 256 + wt * 64 + q * 16;
          v_hg = WS_H + (c.rb * 4 + wt) * H_RT + step * 4096 + wn * 2048 + l * 16;
          v_fg = l < 2 ? fbase + (step & 7) * 256 + (step >> 3) * 16 + l * 8 : OOB;
          PIN(a_mu); PIN(a_cs); PIN(v_hg); PIN(v_fg);
        }
      } else if constexpr (k == 1) {
        const int step = 54 + unit0 + l;
        v_fs = (l < NU && step < NS2) ? fbase + (step & 7) * 256 + (step >> 3) * 16 + wn * 8 : OOB;
        a_st = L_ST + (grp * 4 + q) * 64;
        PIN(v_fs); PIN(a_st);
      } else if constexpr (k == 2) {
        const unsigned tr = (wn ? LWB(2) : LWB(1)) + 8192 + wt * 2048;
        const int pr = 4 * q + ((l >> 2) & 3);
        a_tw = tr + i * 64 + ((q ^ (i >> 1)) & 3) * 16;
        a_tr = tr + pr * 64 + ((l ^ (pr >> 1)) & 3) * 16;
        PIN(a_tw); PIN(a_tr);
      } else if constexpr (k == 3) {
        c.vh2 = WS_H + (c.rb * 4 + c.rt) * H_RT + w * 4096 + l * 16;
        c.vfl = WS_FLAGH + (c.rb * 4 + c.rt) * 2048 + w * 256 + (l & 31) * 8;
        PIN(c.vh2); PIN(c.vfl);
      } else {
        c.vw2 = WS_W2T + c.ct * W2T_CT + w * 5120 + l * 16;
        PIN(c.vw2);
      }
    }
  }
};

DI unsigned jaddr1(unsigned o) { return o < 32768 ? LX(1) + o : LWB(1) - 32768 + o; }
DI void job_glu_load(const Ctx& c, int step) {
  const v4u rj = mkrsrc(c.wd, F_ * D_ * 2);
#pragma unroll
  for (int j = 0; j < 5; ++j) {
    const unsigned ch = (c.w * 5 + j) * 64 + c.lane, f = ch / 80, cp = ch - f * 80, sg = cp >> 1;
    const unsigned vo = f * 1280 + ((sg & ~7u) | ((sg ^ f) & 7u)) * 32 + (cp & 1) * 16;
    glds16<0, LX(2)>(rj, vo, step * 32 * 1280, (c.w * 5 + j) * 1024);
  }
}
DI unsigned job_glu_run(const Ctx& c, int step) {
  const int l = c.lane, w = c.w, r = (l >> 2) & 3, cc = l & 3, f = 4 * (l >> 4) + r;
#pragma unroll
  for (int j = 0; j < 5; ++j) {
    const unsigned dt = w * 5 + j;
    const unsigned a = LX(2) + f * 1280 + ((dt & ~7u) | ((dt ^ f) & 7u)) * 32 + cc * 8;
    const bf16x8 v = cat(trread(a), trread(a + 16 * 1280));
    st128(c, __builtin_bit_cast(v4u, v), l * 16 + j * 1024 + (WS_W2T + w * W2T_CT + step * 5120));
  }
  return WS_FLAGW + (w * 8 + (step & 7)) * 128 + (step >> 3) * 8;
}

template <int TYPE> DI void phase1(Ctx& c, const u16* lre, const u16* lim, const u16* ldt) {
  const int l = c.lane, i = l & 15, q = l >> 4, w = c.w, wt = w & 3, wn = w >> 2;
  const int rb = c.rb, cg = c.cg;
  constexpr int NU = G1<TYPE>::NU, NP = G1<TYPE>::NP;
  G1<TYPE> g;
  Side<NU> sd;
  const int srow = rb * 256 + cg * 8 + w;
  const int si = rb * 5 + (cg - 27), sj = si;
  const int unit0 = TYPE == 0 ? cg : 27 + 3 * (cg - 27);
  g.init_stage(c, unit0);
  if (TYPE == 0) { g.template half0<0>(); g.template half1<0>(); }
  unsigned ep;
  {
    const u16* xrow = c.x + (size_t)srow * D_;
    asm volatile(
        "global_load_dword %5, %8, %9 sc1\n\t"
        "global_load_dword %0, %6, %7\n\t"
        "global_load_dword %1, %6, %7 offset:256\n\t"
        "global_load_dword %2, %6, %7 offset:512\n\t"
        "global_load_dword %3, %6, %7 offset:768\n\t"
        "global_load_dword %4, %6, %7 offset:1024"
        : "=&v"(sd.xr[0]), "=&v"(sd.xr[1]), "=&v"(sd.xr[2]), "=&v"(sd.xr[3]), "=&v"(sd.xr[4]), "=&v"(ep)
        : "v"(l * 4), "s"(xrow), "v"(0), "s"(c.ws)
        : "memory");
    const int pidx = TYPE == 0 ? unit0 * 16 + i : (w == 0 ? (sj >> 2) * 64 + l : min(unit0 * 16 + l, 639));
    asm volatile("global_load_ushort %0, %3, %4\n\tglobal_load_ushort %1, %3, %5\n\tglobal_load_ushort %2, %3, %6"
                 : "=&v"(sd.pa), "=&v"(sd.pb), "=&v"(sd.pd) : "v"(pidx * 2), "s"(lre), "s"(lim), "s"(ldt) : "memory");
  }
  if (TYPE == 1) {
    job_glu_load(c, si);
    const v4u rj = mkrsrc(wn ? c.cim : c.cre, 640 * 640 * 2);
#pragma unroll
    for (int j = 0; j < 5; ++j) {
      const unsigned row = (wt * 5 + j) * 8 + (l >> 3);
      const unsigned vo = row * 1280 + (((l & 7) ^ ((row >> 1) & 7)) * 16);
      glds16<0, 0>(rj, vo, (sj & 3) * 160 * 1280 + (sj >> 2) * 128, jaddr1((w * 5 + j) * 1024));
    }
    g.template half0<0>(); g.template half1<0>();
  }
  g.init_read(c);
  if (TYPE == 0) {
    __builtin_amdgcn_s_barrier();
    g.template half0<1>(); g.template half1<1>();
  }
  asm volatile("s_waitcnt vmcnt(%6)" : "+v"(sd.xr[0]), "+v"(sd.xr[1]), "+v"(sd.xr[2]), "+v"(sd.xr[3]), "+v"(sd.xr[4]), "+v"(ep) : "n"(TYPE == 0 ? 7 : 16) : "memory");
  asm volatile("" : "+v"(sd.pa), "+v"(sd.pb), "+v"(sd.pd));
  STAMP(1);
  c.T = __builtin_amdgcn_readfirstlane(ep) + 1u;
  c.T2 = (c.T * 0x9E3779B1u) ^ 0x5bd1e995u;
  c.tag = v2u{c.T, c.T2};
  PIN(c.tag);
  typename G1<TYPE>::Frags fa, fb;
  unsigned wflag = OOB;
  if (TYPE == 0) {
    BAR();
    g.template rdall<0>(fa);
    g.template half0<2>();
  } else {
    if (w < 2) {
      const float ar = bf2f(sd.pa), ai = bf2f(sd.pb), dt = rbf(fexp(bf2f(sd.pd))), e = fexp(dt * ar);
      float sn, cs;
      fsincos(dt * ai, sn, cs);
      cpx p = {e * cs, e * sn};
      if (w == 0) {
        const float inv = 2.f / (ar * ar + ai * ai);
        ldsw<f32x2>(L_COEF + l * 8, cmul(cpx{p.x - 1.f, p.y}, cpx{ar * inv, -ai * inv}));
      } else {
        ldsw<f32x2>(L_POW + l * 8, p);
        p = cmul(p, p); p = cmul(p, p);
#pragma unroll
        for (int k = 1; k < 6; ++k) { p = cmul(p, p); ldsw<f32x2>(L_POW + k * 512 + l * 8, p); }
      }
    }
    waitv<11>();
    BAR();
    const unsigned f0 = job_glu_run(c, si);
    waitv<11>();
    BAR();
    if (si >= 26) job_glu_load(c, si + 14);
    const f32x4 k0 = ldsr<f32x4>(L_COEF + (16 * wt + 4 * q) * 8), k1 = ldsr<f32x4>(L_COEF + (16 * wt + 4 * q) * 8 + 16);
    const cpx kc[4] = {{k0.x, k0.y}, {k0.z, k0.w}, {k1.x, k1.y}, {k1.z, k1.w}};
    const int step = 54 + 4 * (sj >> 2) + wt, ct = 2 * (sj & 3) + wn;
#pragma unroll
    for (int j = 0; j < 5; ++j) {
      const unsigned row = (wn * 5 + j) * 16 + i;
      const unsigned a = row * 128 + (((2 * wt + (q >> 1)) ^ ((row >> 1) & 7)) * 16) + (q & 1) * 8;
      const v2u cr = ldsr<v2u>(LX(1) + a), ci = ldsr<v2u>(jaddr1(a + 20480));
      const unsigned cr0 = cr.x, cr1 = cr.y, ci0 = ci.x, ci1 = ci.y;
      const cpx p0 = cmul(kc[0], cpx{bf2f(cr0), bf2f(ci0)}), p1 = cmul(kc[1], cpx{bfhi(cr0), bfhi(ci0)});
      const cpx p2 = cmul(kc[2], cpx{bf2f(cr1), bf2f(ci1)}), p3 = cmul(kc[3], cpx{bfhi(cr1), bfhi(ci1)});
      const v4u o = {pk2(p0.x, p1.x), pk2(p2.x, p3.x), pk2(-p0.y, -p1.y), pk2(-p2.y, -p3.y)};
      st128(c, o, l * 16 + j * 1024 + (WS_W2T + ct * W2T_CT + step * 5120));
    }
    const unsigned f1 = WS_FLAGW + (ct * 8 + (step & 7)) * 128 + (step >> 3) * 8;
    SB0();
    waitv<5>();
    BAR();
    g.template half0<1>(); g.template half1<1>();
    g.template rdall<0>(fa);
    unsigned f2 = OOB;
    if (si >= 26) {
      f2 = job_glu_run(c, si + 14);
      BAR();
    }
    g.template half0<2>();
    wflag = l == 0 ? f0 : (l == 1 ? f1 : (l == 2 ? f2 : OOB));
  }
  STAMP(2); CSTAMP(0);
  SB0();
  v4u sg; v2u fw; unsigned pfd = 0;
  const v4u rwsv = mkrsrc(c.ws, WS_END);
  auto w2l = [&](auto CH, auto A) { glds16sc1<CH.v * 40960 + A.v * 1024, tile_base(CH.v, A.v)>(rwsv, c.vw2, A.v < 4 ? g.w4k : g.w1k); };
  auto side = [&](auto SUB, auto G) {
    constexpr int k = TYPE == 0 ? (G.v == 3 ? 0 : (G.v == 7 ? 1 : (G.v == 9 ? 2 : -1))) : G.v - 1;
    constexpr int n = SUB.v * (TYPE == 0 ? 3 : 5) + k;
    if constexpr (k >= 0 && n < Side<NU>::NCH) sd.template tick<n>(c, srow, unit0);
  };
  static_assert(Side<NU>::NCH <= 14 * (TYPE == 0 ? 3 : 5), "side work must be done before step 7");
  constexpr int GA = TYPE == 0 ? 4 : 2, GB = TYPE == 0 ? 6 : 4;
  auto step = [&](auto SS) {
    constexpr int S = SS.v;
    LGKM0();
    g.template part<2 * S>(fa, fb, [&](auto G) {
      if constexpr (S + 2 < 10) {
        if (G.v == 1) g.template piece<2, S + 2>();
        if (G.v == GA) g.template piece<5, S + 2>();
        if (G.v == GB) g.template piece<3, S + 2>();
      } else {
        if (G.v == 1) w2l(IC<S - 8>{}, IC<3>{});
        if (G.v == GB) w2l(IC<S - 8>{}, IC<4>{});
      }
      side(IC<2 * S>{}, G);
    });
    if constexpr (S < 9) {
      if (S == SCHK) asm volatile("s_waitcnt vmcnt(%2)" : "+v"(sg), "+v"(fw) : "n"(NP) : "memory");
      else if (S == 8) waitv<5>();
      else waitv<NP>();
      LGKM0();
      asm volatile("s_barrier" ::: "memory");
    } else LGKM0();
    if (TYPE == 1 && S == 0) st_flag(c, wflag);
    if (S == SCHK - 1) {
      const GAS char* sp = (const GAS char*)c.ws + WS_STAT + rb * 4096;
      const GAS char* fp = (const GAS char*)c.ws + WS_FLAGW + (c.ct * 8 + w) * 128;
      asm volatile("global_load_dwordx4 %0, %2, %3 sc1\n\tglobal_load_dwordx2 %1, %4, %5 sc1"
                   : "=&v"(sg), "=&v"(fw) : "v"(wt * 1024 + l * 16), "s"(sp), "v"(i * 8), "s"(fp) : "memory");
    }
    if (S == SCHK) {
      const bool need = i < 12 && w + 8 * i < NS2;
      SPIN_LOOP {
        if (__all((!need || (fw.x == c.T && fw.y == c.T2)) && sg.y == c.T && sg.w == c.T2)) break;
        __builtin_amdgcn_s_sleep(1);
        asm volatile("" ::: "memory");
        sg = ld128(c, WS_STAT + rb * 4096 + wt * 1024 + l * 16);
        fw = ld64(c, WS_FLAGW + (c.ct * 8 + w) * 128 + i * 8);
      }
      const unsigned pkd = sg.x;
      ldsw<f32x2>(L_ST + (wt * 64 + l) * 8, f32x2{bf2f(pkd & 0xffffu), 1.f / bfhi(pkd)});
    }
    SB0();
    g.template part<2 * S + 1>(fb, fa, [&](auto G) {
      if constexpr (S + 3 < 10) {
        if (G.v == 0) g.template piece<6, S + 3>();
        if (G.v == (TYPE == 0 ? 2 : 1)) g.template piece<0, S + 3>();
        if (G.v == GA) g.template piece<4, S + 3>();
        if (G.v == GB) g.template piece<1, S + 3>();
      } else if constexpr (S < 9) {
        if (G.v == 1) w2l(IC<S - 7>{}, IC<0>{});
        if (G.v == GA) w2l(IC<S - 7>{}, IC<1>{});
        if (G.v == GB) w2l(IC<S - 7>{}, IC<2>{});
      } else {
        if (G.v == G1<TYPE>::NG - 1) {
          if (TYPE == 0) ldsw<float>(L_CS + ((wn * 4 + wt) * 16 + i) * 4, g.cs0.x);
          const unsigned ca = L_CSS + (TYPE == 0 ? w : (w >> 1) % 3) * 128 + i * 4;
          ldsw<float>(ca, g.cs1.x); ldsw<float>(ca + 64, g.cs2.x);
        }
      }
      side(IC<2 * S + 1>{}, G);
    });
  };
  step(IC<0>{}); step(IC<1>{}); step(IC<2>{}); step(IC<3>{}); step(IC<4>{}); step(IC<5>{}); step(IC<6>{});
  CSTAMP(1); STAMP(9);
  step(IC<7>{}); step(IC<8>{}); step(IC<9>{});
  asm volatile("" ::"v"(pfd));
  MFMA_DRAIN();
  BAR();
  SB0();
  STAMP(3);
  w2l(IC<2>{}, IC<0>{}); w2l(IC<2>{}, IC<1>{}); w2l(IC<2>{}, IC<2>{}); w2l(IC<2>{}, IC<3>{}); w2l(IC<2>{}, IC<4>{});
  if (TYPE == 0) {
    f32x4 cs[4];
    float mu[4];
#pragma unroll
    for (int a = 0; a < 4; ++a) cs[a] = ldsr<f32x4>(sd.a_cs ^ (a * 64));
#pragma unroll
    for (int m = 0; m < 4; ++m) mu[m] = -ldsr<float>((m < 2 ? sd.a_mu : (sd.a_mu ^ 256)) + (m & 1) * 32);
    auto glu = [&](auto ODD) {
#pragma unroll
      for (int m = 0; m < 4; ++m) {
        const f32x4 mv = {mu[m], mu[m], mu[m], mu[m]};
        const f32x4 h0 = __builtin_elementwise_fma(mv, cs[ODD.v], g.acc[ODD.v][m]) * __builtin_elementwise_fma(mv, cs[2 + ODD.v], g.acc[2 + ODD.v][m]);
        const f32x4 h1 = __builtin_elementwise_fma(mv, cs[1 - ODD.v], g.acc[1 - ODD.v][m]) * __builtin_elementwise_fma(mv, cs[3 - ODD.v], g.acc[3 - ODD.v][m]);
        st128(c, v4u{pk2(h0[0], h0[1]), pk2(h0[2], h0[3]), pk2(h1[0], h1[1]), pk2(h1[2], h1[3])}, (m < 2 ? sd.v_hg : (sd.v_hg ^ 2048)) + (m & 1) * 1024);
        asm volatile("" ::: "memory");
      }
    };
    if (w & 1) glu(IC<1>{}); else glu(IC<0>{});
    SB0();
  }
  if (TYPE == 0) { ldw<3>(c, c.wf[0]); ldw<4>(c, c.wf[1]); SB0(); }
  cpx v[NU][8], eq[NU], tot[NU];
  {
    f32x4 st[4];
#pragma unroll
    for (int e = 0; e < 4; ++e) st[e] = ldsr<f32x4>(sd.a_st + e * 16);
#pragma unroll
    for (int u = 0; u < NU; ++u) {
      float csx = ldsr<float>(sd.a_css[u]), csy = ldsr<float>(sd.a_css[u] + 64);
      if (TYPE == 0) { csx = sumq(csx + ldsr<float>(sd.a_css[u] + 512)); csy = sumq(csy + ldsr<float>(sd.a_css[u] + 576)); }
#pragma unroll
      for (int j = 0; j < 8; ++j) {
        const float mu = (j & 1) ? st[j >> 1].z : st[j >> 1].x, rs = (j & 1) ? st[j >> 1].w : st[j >> 1].y;
        float a = fmaf(-mu, csx, g.accs[2 * u][j >> 2][j & 3]), b = fmaf(-mu, csy, g.accs[2 * u + 1][j >> 2][j & 3]);
        asm("" : "+v"(a), "+v"(b));
        v[u][j] = cpx{a * rs, b * rs};
        asm("" : "+v"(v[u][j]));
      }
      cpx h = v[u][0];
#pragma unroll
      for (int j = 1; j < 8; ++j) h = cfma(sd.lam[u], h, v[u][j]);
      float ax[4], ay[4];
      gatherq(h.x, ax); gatherq(h.y, ay);
      eq[u] = cfma(sd.u0[u], cpx{ax[0], ay[0]}, cfma(sd.u1[u], cpx{ax[1], ay[1]}, cpx{sd.u2 * ax[2], sd.u2 * ay[2]}));
      tot[u] = cfma(sd.L8[u], eq[u], h);
      ldsw<f32x2>(sd.a_aw[u], tot[u]);
    }
  }
  SB0();
  BAR();
  cpx pm[NU];
#pragma unroll
  for (int u = 0; u < NU; ++u) {
    const f32x4 t = ldsr<f32x4>(sd.a_ar[u]);
    pm[u] = cfma(sd.W0[u], cpx{t.x, t.y}, cmul(sd.W1[u], cpx{t.z, t.w}));
  }
  if (w == 7) {
#pragma unroll
    for (int u = 0; u < NU; ++u) {
      const cpx P = cfma(sd.L32[u], cpx{sumq(pm[u].x), sumq(pm[u].y)}, tot[u]);
      st128(c, v4u{__float_as_uint(P.x), c.T, __float_as_uint(P.y), c.T2}, sd.v_pub[u]);
    }
  }
  SB0();
  if (TYPE == 0) {
    if (w == 7) waitv<11>(); else waitv<10>();
    st_flag(c, sd.v_fg);
  }
  STAMP(4);
  v4u lk0[NU], lk1[NU];
#pragma unroll
  for (int u = 0; u < NU; ++u) { lk0[u] = ld128(c, sd.v_lk0[u]); lk1[u] = ld128(c, sd.v_lk1[u]); }
  if (TYPE == 0) {
    c.fl = ld64(c, c.vfl);
    ldw<5>(c, c.wf[2]);
    SB0();
    SPIN_LOOP {
      if (__all(l >= 12 || (c.fl.x == c.T && c.fl.y == c.T2))) break;
      asm volatile("" ::: "memory");
      c.fl = ld64(c, c.vfl);
    }
    __builtin_amdgcn_fence(__ATOMIC_ACQUIRE, "wavefront");
  } else ldw<3>(c, c.wf[0]);
  SB0();
  SPIN_LOOP {
    bool ok = true;
#pragma unroll
    for (int u = 0; u < NU; ++u)
      ok = ok && (sd.v_lk0[u] == OOB || (lk0[u].y == c.T && lk0[u].w == c.T2)) && (sd.v_lk1[u] == OOB || (lk1[u].y == c.T && lk1[u].w == c.T2));
    if (__all(ok)) break;
    asm volatile("" ::: "memory");
#pragma unroll
    for (int u = 0; u < NU; ++u) { lk0[u] = ld128(c, sd.v_lk0[u]); lk1[u] = ld128(c, sd.v_lk1[u]); }
  }
  SB0();
  if (TYPE == 0) { ldh<0>(c, c.hf[0]); ldh<1>(c, c.hf[1]); if (NWR > 3) ldw<6>(c, c.wf[NWR - 1]); }
  SB0();
  STAMP(5);
#pragma unroll
  for (int u = 0; u < NU; ++u) {
    const unsigned x0 = lk0[u].x, y0 = lk0[u].z, x1 = lk1[u].x, y1 = lk1[u].z;
    const cpx cy = cfma(sd.wk1[u], cpx{__uint_as_float(x1), __uint_as_float(y1)}, cfma(sd.wk0[u], cpx{__uint_as_float(x0), __uint_as_float(y0)}, pm[u]));
    cpx h = cfma(sd.f1[u], cpx{sumq(cy.x), sumq(cy.y)}, eq[u]);
#pragma unroll
    for (int j = 0; j < 8; ++j) { h = cfma(sd.lam[u], h, v[u][j]); v[u][j] = h; }
    ldsw<v4u>(sd.a_tw, v4u{pk2(v[u][0].x, v[u][1].x), pk2(v[u][2].x, v[u][3].x), pk2(v[u][4].x, v[u][5].x), pk2(v[u][6].x, v[u][7].x)});
    ldsw<v4u>(sd.a_tw + 1024, v4u{pk2(v[u][0].y, v[u][1].y), pk2(v[u][2].y, v[u][3].y), pk2(v[u][4].y, v[u][5].y), pk2(v[u][6].y, v[u][7].y)});
    asm volatile("s_waitcnt lgkmcnt(0)" ::: "memory");
#pragma unroll
    for (int mm = 0; mm < 2; ++mm) st128(c, __builtin_bit_cast(v4u, cat(trread(sd.a_tr + mm * 8), trread(sd.a_tr + mm * 8 + 1024))), sd.v_hs[u] + mm * 1024);
    if (u + 1 < NU) asm volatile("s_waitcnt lgkmcnt(0)" ::: "memory");
  }
  SB0();
  c.vfs = sd.v_fs;
  if (TYPE == 1) {
    ldw<4>(c, c.wf[1]);
    SB0();
    waitv<5>();
    st_flag(c, sd.v_fs);
    c.vfs = OOB;
    c.fl = ld64(c, c.vfl);
    ldw<5>(c, c.wf[2]);
    SB0();
    SPIN_LOOP {
      if (__all(l >= 12 || (c.fl.x == c.T && c.fl.y == c.T2))) break;
      asm volatile("" ::: "memory");
      c.fl = ld64(c, c.vfl);
    }
    __builtin_amdgcn_fence(__ATOMIC_ACQUIRE, "wavefront");
    ldh<0>(c, c.hf[0]); ldh<1>(c, c.hf[1]); if (NWR > 3) ldw<6>(c, c.wf[NWR - 1]);
  }
  if (TYPE == 1) {
    if (si == 39) {
      SPIN_LOOP {
        bool ok = true;
        asm volatile("" ::: "memory");
#pragma unroll
        for (int k = 0; k < 4; ++k) {
          const v4u t = ld128(c, WS_STAT + (k * 512 + w * 64 + l) * 16);
          ok = ok && t.y == c.T && t.w == c.T2;
        }
        if (__all(ok)) break;
        __builtin_amdgcn_s_sleep(1);
      }
    }
  }
  c.bump = TYPE == 1 && si == 39;
  SB0();
}

template <int J> struct P2 {};
DI void phase2(Ctx& c, const u16* dsk, float* out) {
  const int l = c.lane, i = l & 15, q = l >> 4, w = c.w, rt = c.rt, ct = c.ct;
  v4u wf[12][5], hf[12][4];
  f32x4 acc[4][5];
  v2u fl = c.fl;
  STAMP(6); CSTAMP(2);
  const unsigned a4 = w * 4096 + l * 16, a1 = w * 1024 + l * 16;
  float r2[4];
  f32x2 st[3]; v2u xv[3], dv[3];
  f32x4 base[3];
  unsigned oo[3], ra[3], rs[3], b4, b4h, b1, b2;
  auto body = [&](auto JJ) {
    constexpr int j = JJ.v;
    if constexpr (j < 3) {
#pragma unroll
      for (int a = 0; a < 5; ++a) wf[j][a] = ldsr<v4u>(tile_base(j, a) + (a < 4 ? a4 : a1));
    } else if constexpr (j < 3 + NWR) {
#pragma unroll
      for (int a = 0; a < 5; ++a) wf[j][a] = c.wf[j - 3][a];
    }
    if constexpr (j < 2) {
#pragma unroll
      for (int m = 0; m < 4; ++m) hf[j][m] = c.hf[j][m];
    }
    if constexpr (j == 4) {
      const bool need = l >= 12 && l < 24 && w + 8 * (l >> 1) < NS2;
      SPIN_LOOP {
        if (__all(!need || (fl.x == c.T && fl.y == c.T2))) break;
        asm volatile("" ::: "memory");
        fl = ld64(c, c.vfl);
      }
      __builtin_amdgcn_fence(__ATOMIC_ACQUIRE, "wavefront");
    }
    if constexpr (j == 3) fl = ld64(c, c.vfl);
    if constexpr (j + 2 < 12) ldh<j + 2>(c, hf[j + 2]);
    if constexpr (j + 3 < 12 && j + 3 >= 3 + NWR) ldw<j + 3>(c, wf[j + 3]);
    if constexpr (j == 1) {
#pragma unroll
      for (int m = 0; m < 4; ++m) { const float r = ldsr<float>(L_ST + (rt * 64 + (m >> 1) * 32 + 8 * (i >> 2) + 4 * (m & 1) + (i & 3)) * 8 + 4); r2[m] = r * r; PIN(r2[m]); }
#pragma unroll
      for (int k = 0; k < 3; ++k) {
        const int tile = (w + 8 * k) < 20 ? w + 8 * k : w, m = tile / 5;
        st[k] = ldsr<f32x2>(L_ST + (rt * 64 + (m >> 1) * 32 + 8 * (i >> 2) + 4 * (m & 1) + (i & 3)) * 8);
        PIN(st[k]);
      }
    }
    if constexpr (j == 9) {
      int l = threadIdx.x & 63; PIN(l);
      const int i = l & 15, q = l >> 4;
#pragma unroll
      for (int k = 0; k < 3; ++k) {
        const int tile = (w + 8 * k) < 20 ? w + 8 * k : w, m = tile / 5, dt = tile - m * 5;
        const int tl = rt * 64 + (m >> 1) * 32 + 8 * (i >> 2) + 4 * (m & 1) + (i & 3), d = ct * 80 + dt * 16 + 4 * q;
        oo[k] = (c.rb * 256 + tl) * D_ + d;
        xv[k] = *(const v2u*)(c.x + oo[k]);
        dv[k] = *(const v2u*)(dsk + d);
        ra[k] = tile_base(m, dt) + l * 16; rs[k] = tile_wstride(m, dt);
        PIN(oo[k]); PIN(ra[k]);
      }
    }
    if constexpr (j == 10) {
      int l = threadIdx.x & 63; PIN(l);
      b4 = w * 4096 + l * 16; b4h = b4 + 65536; b1 = 98304 + w * 1024 + l * 16; b2 = 147456 + w * 2048 + l * 16;
      PIN(b4); PIN(b4h); PIN(b1); PIN(b2);
    }
    if constexpr (j == 11) {
#pragma unroll
      for (int k = 0; k < 3; ++k) {
        const unsigned x0 = xv[k].x, x1 = xv[k].y, d0 = dv[k].x, d1 = dv[k].y;
        const float xf[4] = {bf2f(x0), bfhi(x0), bf2f(x1), bfhi(x1)}, df[4] = {bf2f(d0), bfhi(d0), bf2f(d1), bfhi(d1)};
#pragma unroll
        for (int e = 0; e < 4; ++e) {
          float xn = rbf(rbf(xf[e] - st[k].x) * st[k].y);
          asm("" : "+v"(xn));
          base[k][e] = xf[e] + rbf(xn * df[e]);
        }
        PIN(base[k]);
      }
    }
    if constexpr (j == 6 || j == 7) {
      if ((w < 6) == (j == 7)) {
        MFMA_DRAIN();
#pragma unroll
        for (int m = 0; m < 4; ++m)
#pragma unroll
          for (int a = 0; a < 5; ++a) {
#pragma unroll
            for (int e = 0; e < 4; ++e) asm volatile("v_mul_f32 %0, %1, %0" : "+v"(acc[m][a][e]) : "v"(r2[m]));
          }
        asm volatile("s_nop 1");
      }
    }
    SB0();
#pragma unroll
    for (int a = 0; a < 5; ++a)
#pragma unroll
      for (int m = 0; m < 4; ++m) mf<j == 0>(acc[m][a], __builtin_bit_cast(bf16x8, wf[j][a]), __builtin_bit_cast(bf16x8, hf[j][m]));
    if constexpr (j == 0) { waitv<4>(); st_flag(c, c.vfs); }
    if constexpr (j == 2) {
      BAR();
      __builtin_amdgcn_raw_buffer_store_b32(c.T, c.rws, (c.bump && w == 0 && l == 0) ? WS_EPOCH : OOB, 0, 16);
    }
    if (j == 1) STAMP(10);
    if (j == 3) STAMP(11);
    if (j == 4) STAMP(12);
    if (j == 7) STAMP(13);
    SB0();
  };
  body(IC<0>{}); body(IC<1>{}); body(IC<2>{}); body(IC<3>{}); body(IC<4>{}); body(IC<5>{});
  body(IC<6>{}); body(IC<7>{}); body(IC<8>{}); body(IC<9>{}); body(IC<10>{}); body(IC<11>{});
  MFMA_DRAIN();
  SB0();
  STAMP(7);
#pragma unroll
  for (int m = 0; m < 4; ++m)
#pragma unroll
    for (int a = 0; a < 5; ++a) {
      constexpr unsigned dummy = 0; (void)dummy;
      const unsigned tb = tile_base(m, a), ts = tile_wstride(m, a);
      ldsw<f32x4>(ts == 4096 ? (tb >= 65536 ? b4h + (tb - 65536) : b4 + tb) : (ts == 1024 ? b1 + (tb - 98304) : b2 + (tb - 147456)), acc[m][a]);
    }
  BAR();
#pragma unroll
  for (int k = 0; k < 3; ++k) {
    if (k < 2 || w < 4) {
      f32x4 s = base[k];
#pragma unroll
      for (int v = 0; v < 8; ++v) s += ldsr<f32x4>(ra[k] + v * rs[k]);
      *(f32x4*)(out + oo[k]) = s;
    }
  }
}

__global__ void __launch_bounds__(512, 2)
fused_kernel(const u16* x, const u16* lre, const u16* lim, const u16* bre, const u16* bim, const u16* cre, const u16* cim, const u16* ldt,
             const u16* dsk, const u16* wg, const u16* wu, const u16* wd, float* out, char* ws) {
  Ctx c;
  c.x = x; c.bre = bre; c.bim = bim; c.cre = cre; c.cim = cim; c.wg = wg; c.wu = wu; c.wd = wd; c.ws = ws;
  c.lane = threadIdx.x & 63;
  c.w = __builtin_amdgcn_readfirstlane(threadIdx.x >> 6);
  const int xc = blockIdx.x & 7, ix = blockIdx.x >> 3;
  c.rb = (xc >> 1) * 2 + (ix >> 4); c.cg = (xc & 1) * 16 + (ix & 15);
  c.rt = c.cg >> 3; c.ct = c.cg & 7;
  c.shr = ix >> 3;
  c.rws = __builtin_amdgcn_make_buffer_rsrc(ws, 0, WS_END, 0x00020000);
  STAMP(0);
  if (c.cg < 27) phase1<0>(c, lre, lim, ldt);
  else phase1<1>(c, lre, lim, ldt);
  phase2(c, dsk, out);
}

extern "C" void kernel_launch(void* const* d_in, const int* in_sizes, int n_in, void* d_out, int out_size, void* d_ws, size_t ws_size,
                              hipStream_t stream) {
  (void)in_sizes; (void)n_in; (void)out_size; (void)ws_size;
  (void)hipFuncSetAttribute((const void*)fused_kernel, hipFuncAttributeMaxDynamicSharedMemorySize, LDS_BYTES);
  fused_kernel<<<256, 512, LDS_BYTES, stream>>>((const u16*)d_in[0], (const u16*)d_in[1], (const u16*)d_in[2], (const u16*)d_in[3], (const u16*)d_in[4],
                                                (const u16*)d_in[5], (const u16*)d_in[6], (const u16*)d_in[7], (const u16*)d_in[8], (const u16*)d_in[10],
                                                (const u16*)d_in[11], (const u16*)d_in[12], (float*)d_out, (char*)d_ws);
}
```

```cpp
#include <hip/hip_runtime.h>
#include <stdint.h>

#define GAS __attribute__((address_space(1)))
#define LAS __attribute__((address_space(3)))
#define DI __device__ __forceinline__
typedef unsigned short u16;
typedef short bf16x8 __attribute__((ext_vector_type(8)));
typedef short s16x4 __attribute__((ext_vector_type(4)));
typedef __bf16 bf2 __attribute__((ext_vector_type(2)));
typedef float f32x4 __attribute__((ext_vector_type(4)));
typedef float f32x2 __attribute__((ext_vector_type(2)));
typedef unsigned v4u __attribute__((ext_vector_type(4)));
typedef unsigned v2u __attribute__((ext_vector_type(2)));

#define XMAP 2
#define SCHK 7
#define L2PF 0
#define NWR 4
#define PH 2
#define NWP 7
#define PWD 3

constexpr int L_ = 2048, D_ = 640, F_ = 1728;
constexpr int NS2 = 94;
constexpr unsigned WS_EPOCH = 0;
constexpr unsigned WS_STAT = 4096;
constexpr unsigned WS_AGG = WS_STAT + 2048 * 16;
constexpr unsigned WS_FLAGH = WS_AGG + 8 * 640 * 16;
constexpr unsigned WS_FLAGW = WS_FLAGH + 32 * 8 * 256;
constexpr unsigned WS_CTL_END = WS_FLAGW + 8 * 8 * 128;
constexpr unsigned WS_W2T = 262144;
constexpr unsigned W2T_CT = NS2 * 5 * 1024;
constexpr unsigned WS_H = WS_W2T + 8 * W2T_CT;
constexpr unsigned H_RT = NS2 * 4 * 1024;
constexpr unsigned WS_END = WS_H + 32 * H_RT;
constexpr unsigned OOB = 0x70000000u;
static_assert(WS_CTL_END <= WS_W2T, "ws map");
constexpr unsigned LX(int k) { return k * 32768; }
constexpr unsigned LWB(int k) { return 98304 + ((k + 1) % 3) * 16384; }
constexpr unsigned LWS(int k) { return 147456 + k * 4096; }
constexpr unsigned L_ST = 159744;
constexpr unsigned L_CS = L_ST + 2048;
constexpr unsigned L_CSS = L_CS + 512;
constexpr unsigned L_COEF = L_CSS + 1024;
constexpr unsigned L_POW = LWS(0);
constexpr unsigned L_AGG = LWS(1);
constexpr unsigned L_DUMMY = LWS(2);
constexpr unsigned LDS_BYTES = 163840;
static_assert(L_COEF + 512 <= LDS_BYTES && LWB(2) == LX(2) + 32768, "lds map");
constexpr unsigned tile_base(int ch, int j) {
  return ch < 3 ? (j < 4 ? LX((ch + 1) % 3) + j * 1024 : LWB((ch + 1) % 3)) : (j < 3 ? LWB(j) + 8192 : LWS(0) + (j - 3) * 1024);
}
constexpr unsigned tile_wstride(int ch, int j) { return ch < 3 ? (j < 4 ? 4096 : 1024) : (j < 3 ? 1024 : 2048); }
constexpr int SPIN_CAP = 1 << 15;

DI float bf2f(unsigned v) { return __uint_as_float(v << 16); }
DI float bfhi(unsigned v) { return __uint_as_float(v & 0xffff0000u); }
DI unsigned pk2(float a, float b) { bf2 v = {(__bf16)a, (__bf16)b}; return __builtin_bit_cast(unsigned, v); }
DI float rbf(float a) { return bf2f(pk2(a, 0.f) & 0xffffu); }
DI void mfa(f32x4& c, bf16x8 a, bf16x8 b) { asm volatile("v_mfma_f32_16x16x32_bf16 %0, %1, %2, %0" : "+v"(c) : "v"(a), "v"(b)); }
DI void mfz(f32x4& c, bf16x8 a, bf16x8 b) { asm volatile("v_mfma_f32_16x16x32_bf16 %0, %1, %2, 0" : "=&v"(c) : "v"(a), "v"(b)); }
template <bool Z> DI void mf(f32x4& c, bf16x8 a, bf16x8 b) { if (Z) mfz(c, a, b); else mfa(c, a, b); }
#define MFMA_DRAIN() asm volatile("s_nop 7\n\ts_nop 1" ::: "memory")
DI v4u mkrsrc(const void* p, unsigned bytes) {
  uint64_t a = (uint64_t)p;
  v4u r; r.x = (unsigned)a; r.y = (unsigned)(a >> 32) & 0xffffu; r.z = bytes; r.w = 0x00020000u;
  return r;
}
template <unsigned SI, unsigned LI> DI void glds16(v4u rs, unsigned voff, unsigned sb, unsigned lb) {
  unsigned t;
  asm volatile("s_add_u32 m0, %4, %6\n\ts_add_u32 %0, %3, %5\n\tbuffer_load_dwordx4 %1, %2, %0 offen lds"
               : "=&s"(t) : "v"(voff), "s"(rs), "s"(sb), "s"(lb), "i"(SI), "i"(LI) : "memory", "scc");
}
template <unsigned SI, unsigned LI> DI void glds16sc1(v4u rs, unsigned voff, unsigned lb) {
  unsigned t;
  asm volatile("s_add_u32 m0, %3, %5\n\ts_mov_b32 %0, %4\n\tbuffer_load_dwordx4 %1, %2, %0 offen sc1 lds"
               : "=&s"(t) : "v"(voff), "s"(rs), "s"(lb), "i"(SI), "i"(LI) : "memory", "scc");
}
template <class T> DI T ldsr(unsigned a) { return *(LAS T*)(uintptr_t)a; }
template <class T> DI void ldsw(unsigned a, T v) { *(LAS T*)(uintptr_t)a = v; }
DI s16x4 trread(unsigned a) { return __builtin_amdgcn_ds_read_tr16_b64_v4i16((LAS s16x4*)(uintptr_t)a); }
DI bf16x8 cat(s16x4 lo, s16x4 hi) { return __builtin_shufflevector(lo, hi, 0, 1, 2, 3, 4, 5, 6, 7); }
template <int N> DI void waitv() { asm volatile("s_waitcnt vmcnt(%0)" ::"n"(N) : "memory"); }
#define BAR() asm volatile("s_waitcnt lgkmcnt(0)\n\ts_barrier" ::: "memory")
#define LGKM0() __builtin_amdgcn_s_waitcnt(0xc07f)
#define SB0() __builtin_amdgcn_sched_barrier(0)
#define PIN(x) asm volatile("" : "+v"(x))
#define SLPN 0
#define SLP() do { if (SLPN) __builtin_amdgcn_s_sleep(SLPN); } while (0)
#define SPIN_LOOP _Pragma("nounroll") for (int sp = 0; sp < SPIN_CAP; ++sp)
template <int N> DI float ror(float v) { return __builtin_bit_cast(float, __builtin_amdgcn_update_dpp(0, __builtin_bit_cast(int, v), 0x120 + N, 0xf, 0xf, false)); }
DI void sw16(float v, float& e, float& o) { unsigned u = __float_as_uint(v); auto r = __builtin_amdgcn_permlane16_swap(u, u, false, false); e = __uint_as_float(r[0]); o = __uint_as_float(r[1]); }
DI void sw32(float v, float& lo, float& hi) { unsigned u = __float_as_uint(v); auto r = __builtin_amdgcn_permlane32_swap(u, u, false, false); lo = __uint_as_float(r[0]); hi = __uint_as_float(r[1]); }
DI float sumq(float v) { float a, b; sw16(v, a, b); v = a + b; sw32(v, a, b); return a + b; }
DI float sumr(float v) { v += ror<8>(v); v += ror<4>(v); v += ror<2>(v); v += ror<1>(v); return v; }
DI void gatherq(float v, float (&a)[4]) { float e, o; sw16(v, e, o); sw32(e, a[0], a[2]); sw32(o, a[1], a[3]); }
typedef f32x2 cpx;
DI cpx cfma(cpx a, cpx b, cpx c) {
  cpx r;
  asm("v_pk_fma_f32 %0, %1, %2, %3 op_sel:[1,1,0] op_sel_hi:[1,0,1] neg_lo:[1,0,0]\n\tv_pk_fma_f32 %0, %1, %2, %0 op_sel_hi:[0,1,1]" : "=&v"(r) : "v"(a), "v"(b), "v"(c));
  return r;
}
DI cpx cmul(cpx a, cpx b) {
  cpx r;
  asm("v_pk_mul_f32 %0, %1, %2 op_sel:[1,1] op_sel_hi:[1,0] neg_lo:[1,0]\n\tv_pk_fma_f32 %0, %1, %2, %0 op_sel_hi:[0,1,1]" : "=&v"(r) : "v"(a), "v"(b));
  return r;
}
DI cpx csel(bool p, cpx a) { return cpx{p ? a.x : 1.f, p ? a.y : 0.f}; }
DI cpx czero(bool p, cpx a) { return cpx{p ? 0.f : a.x, p ? 0.f : a.y}; }
template <int V> struct IC { static constexpr int v = V; };

#define STAMP(k) do {} while (0)
#define CSTAMP(k) do {} while (0)
struct Ctx {
  const u16 *x, *bre, *bim, *cre, *cim, *wg, *wu, *wd;
  char* ws;
  __amdgpu_buffer_rsrc_t rws;
  v2u tag;
  unsigned T, T2;
  int lane, w, rb, cg, rt, ct, shr;
  unsigned vw2, vh2, vfl;
  v4u wf[NWR][5], hf[2][4];
  v2u fl;
  unsigned vfs;
  bool bump;
};
DI v4u ld128(const Ctx& c, unsigned vo, unsigned so = 0) { return __builtin_amdgcn_raw_buffer_load_b128(c.rws, vo, so, 16); }
DI v2u ld64(const Ctx& c, unsigned vo) { return __builtin_amdgcn_raw_buffer_load_b64(c.rws, vo, 0, 16); }
DI void st128(const Ctx& c, v4u v, unsigned vo) { __builtin_amdgcn_raw_buffer_store_b128(v, c.rws, vo, 0, 16); }
DI void st_flag(const Ctx& c, unsigned vo) { __builtin_amdgcn_raw_buffer_store_b64(c.tag, c.rws, vo, 0, 16); }

DI float fexp(float x) {
  const float t = x * 1.44269502162933349609375f;
  const float r = fmaf(x, 1.925963033500011e-8f, fmaf(x, 1.44269502162933349609375f, -t));
  const float e = __builtin_amdgcn_exp2f(t);
  return fmaf(e, r * 0.693147182464599609375f, e);
}
DI void fsincos(float z, float& sn, float& cs) {
  const float k = rintf(z * 0.63661977236758134f);
  float r = fmaf(k, -1.5707963705062866f, z);
  r = fmaf(k, 4.3711388286737929e-08f, r);
  r = fmaf(k, 1.7151245100058819e-15f, r);
  const float r2 = r * r;
  const float sp = fmaf(r * r2, fmaf(r2, fmaf(r2, -1.9515295891e-4f, 8.3321608736e-3f), -1.6666654611e-1f), r);
  const float cp = fmaf(r2 * r2, fmaf(r2, fmaf(r2, 2.443315711809948e-5f, -1.388731625493765e-3f), 4.166664568298827e-2f), fmaf(r2, -0.5f, 1.f));
  const int n = (int)k;
  sn = (n & 1) ? cp : sp; cs = (n & 1) ? sp : cp;
  if (n & 2) sn = -sn;
  if ((n + 1) & 2) cs = -cs;
}
template <int J> DI void ldw(const Ctx& c, v4u (&d)[5]) {
  const unsigned vo = J == 11 ? (c.w < 6 ? c.vw2 : OOB) : c.vw2;
#pragma unroll
  for (int a = 0; a < 4; ++a) d[a] = ld128(c, vo + a * 1024, J * 40960);
  d[4] = ld128(c, vo, J * 40960 + 4096);
}
template <int J> DI void ldh(const Ctx& c, v4u (&d)[4]) {
  const unsigned vo = J == 11 ? (c.w < 6 ? c.vh2 : OOB) : c.vh2;
#pragma unroll
  for (int m = 0; m < 4; ++m) d[m] = ld128(c, vo + m * 1024, J * 32768);
}

template <int TYPE> struct G1 {
  static constexpr int NU = TYPE == 0 ? 1 : 3;
  static constexpr int NG = TYPE == 0 ? 10 : 6;
  static constexpr int NR = TYPE == 0 ? 14 : 8;
  static constexpr int NP = TYPE == 0 ? 7 : 6;
  struct Frags { bf16x8 x[TYPE == 0 ? 4 : 2]; s16x4 wl[4], wh[4]; bf16x8 s[2 * NU]; };
  v4u rx, rw0, rw1, rs;
  unsigned vx0, vx1, vw, vs;
  unsigned sx, sw, ss, ss2;
  unsigned ax[2][2][2], aw[4], as[NU][2];
  f32x4 acc[4][4];
  f32x4 accs[2 * NU][2];
  f32x4 cs0, cs1, cs2;
  bf16x8 ones;
  int w;
  unsigned w1k, w4k, w1k6, vs6;

  DI void init_stage(const Ctx& c, int unit0) {
    const int l = c.lane;
    w = c.w; w1k = w * 1024; w4k = w * 4096;
    rx = mkrsrc(c.x, L_ * D_ * 2);
    const unsigned ch = (l & 7) ^ ((l >> 3) & 3);
    vx0 = (l >> 3) * 1280 + ch * 16;
    vx1 = (l >> 3) * 1280 + (ch ^ 4) * 16;
    sx = (c.rb * 256 + w * 32) * 1280;
    rs = mkrsrc((w & 2) ? c.bim : c.bre, 640 * 640 * 2);
    vs = (l >> 3) * 1280 + (((l & 7) ^ (l >> 3)) * 16);
    vs6 = w < 4 ? vs : OOB; w1k6 = w < 4 ? w1k : 65536;
    if (TYPE == 0) {
      rw0 = mkrsrc(c.wg, D_ * F_ * 2); rw1 = mkrsrc(c.wu, D_ * F_ * 2);
      const unsigned key2 = ((l >> 4) & 1) | ((w & 1) << 1);
      vw = (l >> 3) * 3456 + (((l & 7) ^ (key2 << 1)) * 16);
      sw = w * 8 * 3456 + c.cg * 128;
      ss2 = (unit0 * 16 + (w & 1) * 8) * 1280;
    } else {
      ss = (min(unit0 + (w >> 2), 39) * 16 + (w & 1) * 8) * 1280;
      ss2 = (min(unit0 + 2, 39) * 16 + (w & 1) * 8) * 1280;
    }
  }
  DI void init_read(const Ctx& c) {
    const int l = c.lane, i = l & 15, q = l >> 4;
    const int wt = w & 3, wn = w >> 2;
#pragma unroll
    for (int kk = 0; kk < 2; ++kk) {
      const unsigned ch = (((kk * 4 + q) ^ (i & 7)) * 16);
#pragma unroll
      for (int pr = 0; pr < (TYPE == 0 ? 2 : 1); ++pr) {
        ax[0][pr][kk] = (wt * 64 + (pr ^ wn) * 32 + 8 * (i >> 2) + (i & 3)) * 128 + ch;
        ax[1][pr][kk] = ax[0][pr][kk] + 65536;
        PIN(ax[0][pr][kk]); PIN(ax[1][pr][kk]);
      }
#pragma unroll
      for (int u = 0; u < NU; ++u) {
        as[u][kk] = (TYPE == 0 ? LWS(0) : LWB(2) + ((u + (w >> 1)) % 3) * 4096) + i * 128 + ch;
        PIN(as[u][kk]);
      }
    }
    if (TYPE == 0) {
      const int r = (l >> 2) & 3, cc = l & 3;
      const unsigned key2 = ((r >> 1) & 1) | ((q & 1) << 1);
#pragma unroll
      for (int a = 0; a < 4; ++a) {
        const int act = a ^ wt;
        aw[a] = LWB(2) + (act >> 1) * 8192 + (8 * q + r) * 128 + (((2 * wn + (act & 1)) ^ key2) * 32) + cc * 8;
        PIN(aw[a]);
      }
      cs1 = f32x4{0.f, 0.f, 0.f, 0.f}; cs2 = f32x4{0.f, 0.f, 0.f, 0.f};
      PIN(cs1); PIN(cs2);
    }
    ones = bf16x8{0x3f80, 0x3f80, 0x3f80, 0x3f80, 0x3f80, 0x3f80, 0x3f80, 0x3f80};
    PIN(ones);
  }
  template <int P, int S> DI void piece() {
    constexpr int K = S % 3;
    if constexpr (P < 4) glds16<S * 128 + P * 10240, LX(K) + P * 1024>(rx, (P & 1) ? vx1 : vx0, sx, w4k);
    else if constexpr (P < 6) {
      if constexpr (TYPE == 0) glds16<S * (64 * 3456), LWB(K) + (P - 4) * 8192>(P == 4 ? rw0 : rw1, vw, sw, w1k);
      else if constexpr (P == 4) glds16<S * 128, LWB(K)>(rs, vs, ss, w1k);
    } else {
      glds16<S * 128, (TYPE == 0 ? LWS(K) : LWB(K) + 8192)>(rs, vs6, ss2, w1k6);
    }
  }
  template <int S> DI void half0() { piece<6, S>(); piece<0, S>(); piece<4, S>(); piece<1, S>(); }
  template <int S> DI void half1() { piece<2, S>(); piece<5, S>(); piece<3, S>(); }

  template <int K, int KK, int R> DI void rd(Frags& f) {
    constexpr unsigned ow = LWB(K) - LWB(2), ox = K == 1 ? 32768 : 0;
    if constexpr (TYPE == 0) {
      if (R == 0) f.wl[0] = trread(aw[0] + (ow + KK * 4096));
      else if (R == 1) f.wh[0] = trread(aw[0] + (ow + KK * 4096 + 512));
      else if (R < 6) f.x[(R - 2) & 3] = ldsr<bf16x8>(ax[K == 2][((R - 2) >> 1) & 1][KK] + (ox + ((R - 2) & 1) * 512));
      else if (R < 12) {
        constexpr int a = ((R - 4) >> 1) & 3;
        if (R & 1) f.wh[a] = trread(aw[a] + (ow + KK * 4096 + 512)); else f.wl[a] = trread(aw[a] + (ow + KK * 4096));
      } else f.s[R & 1] = ldsr<bf16x8>(as[0][KK] + (K * 4096 + (R & 1) * 2048));
    } else {
      if (R == 1 || R == 2) f.x[(R - 1) & 1] = ldsr<bf16x8>(ax[K == 2][0][KK] + (ox + ((R - 1) & 1) * 512));
      else { constexpr int a = R == 0 ? 0 : R - 2; f.s[a] = ldsr<bf16x8>(as[a >> 1][KK] + (ow + (a & 1) * 2048)); }
    }
  }
  template <bool Z, int M> DI void mm(const Frags& f) {
    if constexpr (TYPE == 0 && M < 16) mf<Z>(acc[M >> 2][M & 3], cat(f.wl[M >> 2], f.wh[M >> 2]), f.x[M & 3]);
    else { constexpr int a = (M - (TYPE == 0 ? 16 : 0)) >> 1; mf<Z>(accs[a][M & 1], f.x[M & 1], f.s[a]); }
  }
  template <int SUB, int G, class H> DI void group(const Frags& cur, Frags& nxt, H& hook) {
    constexpr int N = SUB + 1, K = (N >> 1) % 3, KK = N & 1;
    constexpr bool RD = N < 20;
    mm<SUB == 0, 2 * G>(cur);
    if constexpr (RD && 2 * G < NR) rd<K, KK, 2 * G>(nxt);
    mm<SUB == 0, 2 * G + 1>(cur);
    if constexpr (RD && 2 * G + 1 < NR) rd<K, KK, 2 * G + 1>(nxt);
    if constexpr (TYPE == 0) {
      if (G == 0) mf<SUB == 0>(cs0, ones, cat(cur.wl[0], cur.wh[0]));
      if (G == 2) { if (__builtin_expect(w == (SUB & 7), 0)) { mfa(cs1, ones, cur.s[0]); mfa(cs2, ones, cur.s[1]); } }
    } else {
      if (G == 0) { mf<SUB == 0>(cs1, ones, cur.s[0]); mf<SUB == 0>(cs2, ones, cur.s[1]); }
    }
    hook(IC<G>{});
    SB0();
    if constexpr (G + 1 < NG) group<SUB, G + 1>(cur, nxt, hook);
  }
  template <int SUB, class H> DI void part(const Frags& cur, Frags& nxt, H hook) { group<SUB, 0>(cur, nxt, hook); }
  template <int R> DI void rdall(Frags& f) { rd<0, 0, R>(f); if constexpr (R + 1 < NR) rdall<R + 1>(f); }
};

template <int NU> struct Side {
  unsigned xr[5], pa, pb, pd;
  float v[10], s, dt, e, sn, cs;
  cpx lam[NU], L8[NU], L32[NU], u0[NU], u1[NU], f1[NU], W0[NU], W1[NU], wk0[NU], wk1[NU], t0, t1, t2;
  float u2;
  unsigned a_mu, a_cs, v_hg, v_fg, a_st, a_css[NU], a_aw[NU], a_ar[NU], v_pub[NU], v_lk0[NU], v_lk1[NU], v_hs[NU], v_fs, a_tw, a_tr;
  static constexpr int PERU = 16;
  static constexpr int NCH = 8 + PERU * NU + 5;
  template <int N> DI void tick(Ctx& c, int srow, int unit0) {
    const int l = c.lane, i = l & 15, q = l >> 4, w = c.w, wt = w & 3, wn = w >> 2, grp = wt * 2 + wn;
    if constexpr (N == 0) {
#pragma unroll
      for (int j = 0; j < 5; ++j) { v[2 * j] = bf2f(xr[j]); v[2 * j + 1] = bfhi(xr[j]); PIN(v[2 * j]); PIN(v[2 * j + 1]); }
    } else if constexpr (N == 1) {
      s = ((v[0] + v[1]) + (v[2] + v[3])) + ((v[4] + v[5]) + (v[6] + v[7])) + (v[8] + v[9]); PIN(s);
    } else if constexpr (N == 2) { s = sumr(s); PIN(s); }
    else if constexpr (N == 3) { s = sumq(s) * (1.f / 640.f); PIN(s); }
    else if constexpr (N == 4) {
#pragma unroll
      for (int j = 0; j < 10; ++j) { v[j] -= s; PIN(v[j]); }
    } else if constexpr (N == 5) {
      e = v[0] * v[0];
#pragma unroll
      for (int j = 1; j < 10; ++j) e = fmaf(v[j], v[j], e);
      PIN(e);
    } else if constexpr (N == 6) { e = sumr(e); PIN(e); }
    else if constexpr (N == 7) {
      const float var = rbf(sumq(e) * (1.f / 640.f));
      const unsigned pkd = pk2(s, __builtin_amdgcn_sqrtf(rbf(var + 1e-5f)));
      st128(c, v4u{pkd, c.T, pkd, c.T2}, l == 0 ? WS_STAT + srow * 16 : OOB);
    } else if constexpr (N < 8 + PERU * NU) {
      constexpr int u = (N - 8) / PERU, k = (N - 8) % PERU;
      const int ul = NU == 1 ? 0 : (u + (w >> 1)) % 3, ua = unit0 + ul;
      const int e1 = grp - 2 - 2 * q, k0 = c.rb - 1 - q;
      const unsigned pw = L_POW + (ul * 16 + i) * 8;
      if constexpr (k == 0) { if (NU == 1) { dt = rbf(fexp(bf2f(pd))); PIN(dt); } }
      else if constexpr (k == 1) { if (NU == 1) { e = fexp(dt * bf2f(pa)); PIN(e); } }
      else if constexpr (k == 2) { if (NU == 1) { fsincos(dt * bf2f(pb), sn, cs); PIN(sn); PIN(cs); } }
      else if constexpr (k == 3) {
        if (NU == 1) { lam[u] = cpx{e * cs, e * sn}; t0 = cmul(lam[u], lam[u]); t0 = cmul(t0, t0); L8[u] = cmul(t0, t0); t0 = cmul(L8[u], L8[u]); L32[u] = cmul(t0, t0); }
        else { lam[u] = ldsr<cpx>(pw); L8[u] = ldsr<cpx>(pw + 512); t0 = ldsr<cpx>(pw + 1024); L32[u] = ldsr<cpx>(pw + 1536); }
      }
      else if constexpr (k == 4) { f1[u] = cmul(csel(q & 1, L8[u]), csel(q & 2, t0)); PIN(lam[u]); PIN(L8[u]); PIN(t0); PIN(L32[u]); PIN(f1[u]); }
      else if constexpr (k == 5) {
        u1[u] = czero(q < 2, csel(q == 3, L8[u])); PIN(u1[u]);
        u2 = q == 3 ? 1.f : 0.f; PIN(u2);
      } else if constexpr (k == 6) { u0[u] = czero(q == 0, q == 3 ? t0 : csel(q == 2, L8[u])); PIN(u0[u]); }
      else if constexpr (k == 7) {
        if (NU == 1) { t0 = cmul(L32[u], L32[u]); t1 = cmul(t0, t0); PIN(t0); PIN(t1); } else { t0 = ldsr<cpx>(pw + 2048); t1 = ldsr<cpx>(pw + 2560); }
      }
      else if constexpr (k == 8) { W1[u] = cmul(csel(e1 & 1, L32[u]), csel(e1 & 2, t0)); t2 = cmul(csel(grp & 1, L32[u]), csel(grp & 2, t0)); PIN(W1[u]); PIN(t2); }
      else if constexpr (k == 9) { W1[u] = cmul(W1[u], csel(e1 & 4, t1)); t2 = cmul(t2, csel(grp & 4, t1)); PIN(W1[u]); PIN(t2); }
      else if constexpr (k == 10) { W0[u] = czero(e1 < -1, csel(e1 >= 0, cmul(W1[u], L32[u]))); W1[u] = czero(e1 < 0, W1[u]); PIN(W0[u]); PIN(W1[u]); }
      else if constexpr (k == 11) { t0 = cmul(t1, t1); t1 = cmul(t0, t0); wk1[u] = cmul(t2, csel(k0 & 1, t0)); PIN(t1); PIN(wk1[u]); }
      else if constexpr (k == 12) { wk1[u] = cmul(wk1[u], csel(k0 & 2, t1)); t1 = cmul(t1, t1); PIN(t1); PIN(wk1[u]); }
      else if constexpr (k == 13) { wk0[u] = czero(k0 < 0, cmul(wk1[u], csel(k0 & 4, t1))); wk1[u] = czero(k0 < 4, wk1[u]); PIN(wk0[u]); PIN(wk1[u]); }
      else if constexpr (k == 14) {
        a_css[u] = L_CSS + (NU == 1 ? q * 128 : ul * 128) + i * 4;
        a_ar[u] = L_AGG + (ul * 16 + i) * 64 + q * 16;
        a_aw[u] = q == 3 ? L_AGG + (ul * 16 + i) * 64 + grp * 8 : L_DUMMY + l * 8;
        v_hs[u] = ua < 40 ? WS_H + (c.rb * 4 + wt) * H_RT + (54 + ua) * 4096 + wn * 2048 + l * 16 : OOB;
        PIN(a_css[u]); PIN(a_ar[u]); PIN(a_aw[u]); PIN(v_hs[u]);
      } else {
        const unsigned po = WS_AGG + (ua * 16 + i) * 16;
        v_pub[u] = (q == 3 && ua < 40) ? po + c.rb * 10240 : OOB;
        v_lk0[u] = (k0 >= 0 && ua < 40) ? po + q * 10240 : OOB;
        v_lk1[u] = (k0 >= 4 && ua < 40) ? po + (q + 4) * 10240 : OOB;
        PIN(v_pub[u]); PIN(v_lk0[u]); PIN(v_lk1[u]);
      }
    } else {
      constexpr int k = N - 8 - PERU * NU;
      const unsigned fbase = WS_FLAGH + (c.rb * 4 + wt) * 2048;
      if constexpr (k == 0) {
        if (NU == 1) {
          const int step = 2 * c.cg + wn;
          a_mu = L_ST + (wt * 64 + wn * 32 + 8 * (i >> 2) + (i & 3)) * 8;
          a_cs = L_CS + wn * 256 + wt * 64 + q * 16;
          v_hg = WS_H + (c.rb * 4 + wt) * H_RT + step * 4096 + wn * 2048 + l * 16;
          v_fg = l < 2 ? fbase + (step & 7) * 256 + (step >> 3) * 16 + l * 8 : OOB;
          PIN(a_mu); PIN(a_cs); PIN(v_hg); PIN(v_fg);
        }
      } else if constexpr (k == 1) {
        const int step = 54 + unit0 + l;
        v_fs = (l < NU && step < NS2) ? fbase + (step & 7) * 256 + (step >> 3) * 16 + wn * 8 : OOB;
        a_st = L_ST + (grp * 4 + q) * 64;
        PIN(v_fs); PIN(a_st);
      } else if constexpr (k == 2) {
        const unsigned tr = (wn ? LWB(2) : LWB(1)) + 8192 + wt * 2048;
        const int pr = 4 * q + ((l >> 2) & 3);
        a_tw = tr + i * 64 + ((q ^ (i >> 1)) & 3) * 16;
        a_tr = tr + pr * 64 + ((l ^ (pr >> 1)) & 3) * 16;
        PIN(a_tw); PIN(a_tr);
      } else if constexpr (k == 3) {
        c.vh2 = WS_H + (c.rb * 4 + c.rt) * H_RT + w * 4096 + l * 16;
        c.vfl = WS_FLAGH + (c.rb * 4 + c.rt) * 2048 + w * 256 + (l & 31) * 8;
        PIN(c.vh2); PIN(c.vfl);
      } else {
        c.vw2 = WS_W2T + c.ct * W2T_CT + w * 5120 + l * 16;
        PIN(c.vw2);
      }
    }
  }
};

DI unsigned jaddr1(unsigned o) { return o < 32768 ? LX(1) + o : LWB(1) - 32768 + o; }
DI void job_glu_load(const Ctx& c, int step) {
  const v4u rj = mkrsrc(c.wd, F_ * D_ * 2);
#pragma unroll
  for (int j = 0; j < 5; ++j) {
    const unsigned ch = (c.w * 5 + j) * 64 + c.lane, f = ch / 80, cp = ch - f * 80, sg = cp >> 1;
    const unsigned vo = f * 1280 + ((sg & ~7u) | ((sg ^ f) & 7u)) * 32 + (cp & 1) * 16;
    glds16<0, LX(2)>(rj, vo, step * 32 * 1280, (c.w * 5 + j) * 1024);
  }
}
DI unsigned job_glu_run(const Ctx& c, int step) {
  const int l = c.lane, w = c.w, r = (l >> 2) & 3, cc = l & 3, f = 4 * (l >> 4) + r;
#pragma unroll
  for (int j = 0; j < 5; ++j) {
    const unsigned dt = w * 5 + j;
    const unsigned a = LX(2) + f * 1280 + ((dt & ~7u) | ((dt ^ f) & 7u)) * 32 + cc * 8;
    const bf16x8 v = cat(trread(a), trread(a + 16 * 1280));
    st128(c, __builtin_bit_cast(v4u, v), l * 16 + j * 1024 + (WS_W2T + w * W2T_CT + step * 5120));
  }
  return WS_FLAGW + (w * 8 + (step & 7)) * 128 + (step >> 3) * 8;
}

template <int TYPE> DI void phase1(Ctx& c, const u16* lre, const u16* lim, const u16* ldt) {
  const int l = c.lane, i = l & 15, q = l >> 4, w = c.w, wt = w & 3, wn = w >> 2;
  const int rb = c.rb, cg = c.cg;
  constexpr int NU = G1<TYPE>::NU, NP = G1<TYPE>::NP;
  G1<TYPE> g;
  Side<NU> sd;
  const int srow = rb * 256 + cg * 8 + w;
  const int si = rb * 5 + (cg - 27), sj = si;
  const int unit0 = TYPE == 0 ? cg : 27 + 3 * (cg - 27);
  g.init_stage(c, unit0);
  if (TYPE == 0) { g.template half0<0>(); g.template half1<0>(); }
  unsigned ep;
  {
    const u16* xrow = c.x + (size_t)srow * D_;
    asm volatile(
        "global_load_dword %5, %8, %9 sc1\n\t"
        "global_load_dword %0, %6, %7\n\t"
        "global_load_dword %1, %6, %7 offset:256\n\t"
        "global_load_dword %2, %6, %7 offset:512\n\t"
        "global_load_dword %3, %6, %7 offset:768\n\t"
        "global_load_dword %4, %6, %7 offset:1024"
        : "=&v"(sd.xr[0]), "=&v"(sd.xr[1]), "=&v"(sd.xr[2]), "=&v"(sd.xr[3]), "=&v"(sd.xr[4]), "=&v"(ep)
        : "v"(l * 4), "s"(xrow), "v"(0), "s"(c.ws)
        : "memory");
    const int pidx = TYPE == 0 ? unit0 * 16 + i : (w == 0 ? (sj >> 2) * 64 + l : min(unit0 * 16 + l, 639));
    asm volatile("global_load_ushort %0, %3, %4\n\tglobal_load_ushort %1, %3, %5\n\tglobal_load_ushort %2, %3, %6"
                 : "=&v"(sd.pa), "=&v"(sd.pb), "=&v"(sd.pd) : "v"(pidx * 2), "s"(lre), "s"(lim), "s"(ldt) : "memory");
  }
  if (TYPE == 1) {
    job_glu_load(c, si);
    const v4u rj = mkrsrc(wn ? c.cim : c.cre, 640 * 640 * 2);
#pragma unroll
    for (int j = 0; j < 5; ++j) {
      const unsigned row = (wt * 5 + j) * 8 + (l >> 3);
      const unsigned vo = row * 1280 + (((l & 7) ^ ((row >> 1) & 7)) * 16);
      glds16<0, 0>(rj, vo, (sj & 3) * 160 * 1280 + (sj >> 2) * 128, jaddr1((w * 5 + j) * 1024));
    }
    g.template half0<0>(); g.template half1<0>();
  }
  g.init_read(c);
  if (TYPE == 0) {
    __builtin_amdgcn_s_barrier();
    g.template half0<1>(); g.template half1<1>();
  }
  asm volatile("s_waitcnt vmcnt(%6)" : "+v"(sd.xr[0]), "+v"(sd.xr[1]), "+v"(sd.xr[2]), "+v"(sd.xr[3]), "+v"(sd.xr[4]), "+v"(ep) : "n"(TYPE == 0 ? 7 : 16) : "memory");
  asm volatile("" : "+v"(sd.pa), "+v"(sd.pb), "+v"(sd.pd));
  STAMP(1);
  c.T = __builtin_amdgcn_readfirstlane(ep) + 1u;
  c.T2 = (c.T * 0x9E3779B1u) ^ 0x5bd1e995u;
  c.tag = v2u{c.T, c.T2};
  PIN(c.tag);
  typename G1<TYPE>::Frags fa, fb;
  unsigned wflag = OOB;
  if (TYPE == 0) {
    BAR();
    g.template rdall<0>(fa);
    g.template half0<2>();
  } else {
    if (w < 2) {
      const float ar = bf2f(sd.pa), ai = bf2f(sd.pb), dt = rbf(fexp(bf2f(sd.pd))), e = fexp(dt * ar);
      float sn, cs;
      fsincos(dt * ai, sn, cs);
      cpx p = {e * cs, e * sn};
      if (w == 0) {
        const float inv = 2.f / (ar * ar + ai * ai);
        ldsw<f32x2>(L_COEF + l * 8, cmul(cpx{p.x - 1.f, p.y}, cpx{ar * inv, -ai * inv}));
      } else {
        ldsw<f32x2>(L_POW + l * 8, p);
        p = cmul(p, p); p = cmul(p, p);
#pragma unroll
        for (int k = 1; k < 6; ++k) { p = cmul(p, p); ldsw<f32x2>(L_POW + k * 512 + l * 8, p); }
      }
    }
    waitv<11>();
    BAR();
    const unsigned f0 = job_glu_run(c, si);
    waitv<11>();
    BAR();
    if (si >= 26) job_glu_load(c, si + 14);
    const f32x4 k0 = ldsr<f32x4>(L_COEF + (16 * wt + 4 * q) * 8), k1 = ldsr<f32x4>(L_COEF + (16 * wt + 4 * q) * 8 + 16);
    const cpx kc[4] = {{k0.x, k0.y}, {k0.z, k0.w}, {k1.x, k1.y}, {k1.z, k1.w}};
    const int step = 54 + 4 * (sj >> 2) + wt, ct = 2 * (sj & 3) + wn;
#pragma unroll
    for (int j = 0; j < 5; ++j) {
      const unsigned row = (wn * 5 + j) * 16 + i;
      const unsigned a = row * 128 + (((2 * wt + (q >> 1)) ^ ((row >> 1) & 7)) * 16) + (q & 1) * 8;
      const v2u cr = ldsr<v2u>(LX(1) + a), ci = ldsr<v2u>(jaddr1(a + 20480));
      const unsigned cr0 = cr.x, cr1 = cr.y, ci0 = ci.x, ci1 = ci.y;
      const cpx p0 = cmul(kc[0], cpx{bf2f(cr0), bf2f(ci0)}), p1 = cmul(kc[1], cpx{bfhi(cr0), bfhi(ci0)});
      const cpx p2 = cmul(kc[2], cpx{bf2f(cr1), bf2f(ci1)}), p3 = cmul(kc[3], cpx{bfhi(cr1), bfhi(ci1)});
      const v4u o = {pk2(p0.x, p1.x), pk2(p2.x, p3.x), pk2(-p0.y, -p1.y), pk2(-p2.y, -p3.y)};
      st128(c, o, l * 16 + j * 1024 + (WS_W2T + ct * W2T_CT + step * 5120));
    }
    const unsigned f1 = WS_FLAGW + (ct * 8 + (step & 7)) * 128 + (step >> 3) * 8;
    SB0();
    waitv<5>();
    BAR();
    g.template half0<1>(); g.template half1<1>();
    g.template rdall<0>(fa);
    unsigned f2 = OOB;
    if (si >= 26) {
      f2 = job_glu_run(c, si + 14);
      BAR();
    }
    g.template half0<2>();
    wflag = l == 0 ? f0 : (l == 1 ? f1 : (l == 2 ? f2 : OOB));
  }
  STAMP(2); CSTAMP(0);
  SB0();
  v4u sg; v2u fw; unsigned pfd = 0;
  const v4u rwsv = mkrsrc(c.ws, WS_END);
  auto w2l = [&](auto CH, auto A) { glds16sc1<CH.v * 40960 + A.v * 1024, tile_base(CH.v, A.v)>(rwsv, c.vw2, A.v < 4 ? g.w4k : g.w1k); };
  auto side = [&](auto SUB, auto G) {
    constexpr int k = TYPE == 0 ? (G.v == 3 ? 0 : (G.v == 7 ? 1 : (G.v == 9 ? 2 : -1))) : G.v - 1;
    constexpr int n = SUB.v * (TYPE == 0 ? 3 : 5) + k;
    if constexpr (k >= 0 && n < Side<NU>::NCH) sd.template tick<n>(c, srow, unit0);
  };
  static_assert(Side<NU>::NCH <= 14 * (TYPE == 0 ? 3 : 5), "side work must be done before step 7");
  constexpr int GA = TYPE == 0 ? 4 : 2, GB = TYPE == 0 ? 6 : 4;
  auto step = [&](auto SS) {
    constexpr int S = SS.v;
    LGKM0();
    g.template part<2 * S>(fa, fb, [&](auto G) {
      if constexpr (S + 2 < 10) {
        if (G.v == 1) g.template piece<2, S + 2>();
        if (G.v == GA) g.template piece<5, S + 2>();
        if (G.v == GB) g.template piece<3, S + 2>();
      } else {
        if (G.v == 1) w2l(IC<S - 8>{}, IC<3>{});
        if (G.v == GB) w2l(IC<S - 8>{}, IC<4>{});
      }
      side(IC<2 * S>{}, G);
    });
    if constexpr (S < 9) {
      if (S == SCHK) asm volatile("s_waitcnt vmcnt(%2)" : "+v"(sg), "+v"(fw) : "n"(NP) : "memory");
      else if (S == 8) waitv<5>();
      else waitv<NP>();
      LGKM0();
      asm volatile("s_barrier" ::: "memory");
    } else LGKM0();
    if (TYPE == 1 && S == 0) st_flag(c, wflag);
    if (S == SCHK - 1) {
      const GAS char* sp = (const GAS char*)c.ws + WS_STAT + rb * 4096;
      const GAS char* fp = (const GAS char*)c.ws + WS_FLAGW + (c.ct * 8 + w) * 128;
      asm volatile("global_load_dwordx4 %0, %2, %3 sc1\n\tglobal_load_dwordx2 %1, %4, %5 sc1"
                   : "=&v"(sg), "=&v"(fw) : "v"(wt * 1024 + l * 16), "s"(sp), "v"(i * 8), "s"(fp) : "memory");
    }
    if (S == SCHK) {
      const bool need = i < 12 && w + 8 * i < NS2;
      SPIN_LOOP {
        if (__all((!need || (fw.x == c.T && fw.y == c.T2)) && sg.y == c.T && sg.w == c.T2)) break;
        __builtin_amdgcn_s_sleep(1);
        asm volatile("" ::: "memory");
        sg = ld128(c, WS_STAT + rb * 4096 + wt * 1024 + l * 16);
        fw = ld64(c, WS_FLAGW + (c.ct * 8 + w) * 128 + i * 8);
      }
      const unsigned pkd = sg.x;
      ldsw<f32x2>(L_ST + (wt * 64 + l) * 8, f32x2{bf2f(pkd & 0xffffu), 1.f / bfhi(pkd)});
    }
    SB0();
    g.template part<2 * S + 1>(fb, fa, [&](auto G) {
      if constexpr (S + 3 < 10) {
        if (G.v == 0) g.template piece<6, S + 3>();
        if (G.v == (TYPE == 0 ? 2 : 1)) g.template piece<0, S + 3>();
        if (G.v == GA) g.template piece<4, S + 3>();
        if (G.v == GB) g.template piece<1, S + 3>();
      } else if constexpr (S < 9) {
        if (G.v == 1) w2l(IC<S - 7>{}, IC<0>{});
        if (G.v == GA) w2l(IC<S - 7>{}, IC<1>{});
        if (G.v == GB) w2l(IC<S - 7>{}, IC<2>{});
      } else {
        if (G.v == G1<TYPE>::NG - 1) {
          if (TYPE == 0) ldsw<float>(L_CS + ((wn * 4 + wt) * 16 + i) * 4, g.cs0.x);
          const unsigned ca = L_CSS + (TYPE == 0 ? w : (w >> 1) % 3) * 128 + i * 4;
          ldsw<float>(ca, g.cs1.x); ldsw<float>(ca + 64, g.cs2.x);
        }
      }
      side(IC<2 * S + 1>{}, G);
    });
  };
  step(IC<0>{}); step(IC<1>{}); step(IC<2>{}); step(IC<3>{}); step(IC<4>{}); step(IC<5>{}); step(IC<6>{});
  CSTAMP(1); STAMP(9);
  step(IC<7>{}); step(IC<8>{}); step(IC<9>{});
  asm volatile("" ::"v"(pfd));
  MFMA_DRAIN();
  BAR();
  SB0();
  STAMP(3);
  w2l(IC<2>{}, IC<0>{}); w2l(IC<2>{}, IC<1>{}); w2l(IC<2>{}, IC<2>{}); w2l(IC<2>{}, IC<3>{}); w2l(IC<2>{}, IC<4>{});
  if (TYPE == 0) {
    f32x4 cs[4];
    float mu[4];
#pragma unroll
    for (int a = 0; a < 4; ++a) cs[a] = ldsr<f32x4>(sd.a_cs ^ (a * 64));
#pragma unroll
    for (int m = 0; m < 4; ++m) mu[m] = -ldsr<float>((m < 2 ? sd.a_mu : (sd.a_mu ^ 256)) + (m & 1) * 32);
    auto glu = [&](auto ODD) {
#pragma unroll
      for (int m = 0; m < 4; ++m) {
        const f32x4 mv = {mu[m], mu[m], mu[m], mu[m]};
        const f32x4 h0 = __builtin_elementwise_fma(mv, cs[ODD.v], g.acc[ODD.v][m]) * __builtin_elementwise_fma(mv, cs[2 + ODD.v], g.acc[2 + ODD.v][m]);
        const f32x4 h1 = __builtin_elementwise_fma(mv, cs[1 - ODD.v], g.acc[1 - ODD.v][m]) * __builtin_elementwise_fma(mv, cs[3 - ODD.v], g.acc[3 - ODD.v][m]);
        st128(c, v4u{pk2(h0[0], h0[1]), pk2(h0[2], h0[3]), pk2(h1[0], h1[1]), pk2(h1[2], h1[3])}, (m < 2 ? sd.v_hg : (sd.v_hg ^ 2048)) + (m & 1) * 1024);
        asm volatile("" ::: "memory");
      }
    };
    if (w & 1) glu(IC<1>{}); else glu(IC<0>{});
    SB0();
  }
  if (TYPE == 0) { ldw<3>(c, c.wf[0]); ldw<4>(c, c.wf[1]); SB0(); }
  cpx v[NU][8], eq[NU], tot[NU];
  {
    f32x4 st[4];
#pragma unroll
    for (int e = 0; e < 4; ++e) st[e] = ldsr<f32x4>(sd.a_st + e * 16);
#pragma unroll
    for (int u = 0; u < NU; ++u) {
      float csx = ldsr<float>(sd.a_css[u]), csy = ldsr<float>(sd.a_css[u] + 64);
      if (TYPE == 0) { csx = sumq(csx + ldsr<float>(sd.a_css[u] + 512)); csy = sumq(csy + ldsr<float>(sd.a_css[u] + 576)); }
#pragma unroll
      for (int j = 0; j < 8; ++j) {
        const float mu = (j & 1) ? st[j >> 1].z : st[j >> 1].x, rs = (j & 1) ? st[j >> 1].w : st[j >> 1].y;
        float a = fmaf(-mu, csx, g.accs[2 * u][j >> 2][j & 3]), b = fmaf(-mu, csy, g.accs[2 * u + 1][j >> 2][j & 3]);
        asm("" : "+v"(a), "+v"(b));
        v[u][j] = cpx{a * rs, b * rs};
        asm("" : "+v"(v[u][j]));
      }
      cpx h = v[u][0];
#pragma unroll
      for (int j = 1; j < 8; ++j) h = cfma(sd.lam[u], h, v[u][j]);
      float ax[4], ay[4];
      gatherq(h.x, ax); gatherq(h.y, ay);
      eq[u] = cfma(sd.u0[u], cpx{ax[0], ay[0]}, cfma(sd.u1[u], cpx{ax[1], ay[1]}, cpx{sd.u2 * ax[2], sd.u2 * ay[2]}));
      tot[u] = cfma(sd.L8[u], eq[u], h);
      ldsw<f32x2>(sd.a_aw[u], tot[u]);
    }
  }
  SB0();
  BAR();
  cpx pm[NU];
#pragma unroll
  for (int u = 0; u < NU; ++u) {
    const f32x4 t = ldsr<f32x4>(sd.a_ar[u]);
    pm[u] = cfma(sd.W0[u], cpx{t.x, t.y}, cmul(sd.W1[u], cpx{t.z, t.w}));
  }
  if (w == 7) {
#pragma unroll
    for (int u = 0; u < NU; ++u) {
      const cpx P = cfma(sd.L32[u], cpx{sumq(pm[u].x), sumq(pm[u].y)}, tot[u]);
      st128(c, v4u{__float_as_uint(P.x), c.T, __float_as_uint(P.y), c.T2}, sd.v_pub[u]);
    }
  }
  SB0();
  if (TYPE == 0) {
    if (w == 7) waitv<11>(); else waitv<10>();
    st_flag(c, sd.v_fg);
  }
  STAMP(4);
  v4u lk0[NU], lk1[NU];
#pragma unroll
  for (int u = 0; u < NU; ++u) { lk0[u] = ld128(c, sd.v_lk0[u]); lk1[u] = ld128(c, sd.v_lk1[u]); }
  if (TYPE == 0) {
    c.fl = ld64(c, c.vfl);
    ldw<5>(c, c.wf[2]);
    SB0();
    SPIN_LOOP {
      if (__all(l >= 12 || (c.fl.x == c.T && c.fl.y == c.T2))) break;
      asm volatile("" ::: "memory"); SLP();
      c.fl = ld64(c, c.vfl);
    }
    __builtin_amdgcn_fence(__ATOMIC_ACQUIRE, "wavefront");
  } else ldw<3>(c, c.wf[0]);
  SB0();
  SPIN_LOOP {
    bool ok = true;
#pragma unroll
    for (int u = 0; u < NU; ++u)
      ok = ok && (sd.v_lk0[u] == OOB || (lk0[u].y == c.T && lk0[u].w == c.T2)) && (sd.v_lk1[u] == OOB || (lk1[u].y == c.T && lk1[u].w == c.T2));
    if (__all(ok)) break;
    asm volatile("" ::: "memory"); SLP();
#pragma unroll
    for (int u = 0; u < NU; ++u) { lk0[u] = ld128(c, sd.v_lk0[u]); lk1[u] = ld128(c, sd.v_lk1[u]); }
  }
  SB0();
  if (TYPE == 0) { ldh<0>(c, c.hf[0]); ldh<1>(c, c.hf[1]); if (NWR > 3) ldw<6>(c, c.wf[NWR - 1]); }
  SB0();
  STAMP(5);
#pragma unroll
  for (int u = 0; u < NU; ++u) {
    const unsigned x0 = lk0[u].x, y0 = lk0[u].z, x1 = lk1[u].x, y1 = lk1[u].z;
    const cpx cy = cfma(sd.wk1[u], cpx{__uint_as_float(x1), __uint_as_float(y1)}, cfma(sd.wk0[u], cpx{__uint_as_float(x0), __uint_as_float(y0)}, pm[u]));
    cpx h = cfma(sd.f1[u], cpx{sumq(cy.x), sumq(cy.y)}, eq[u]);
#pragma unroll
    for (int j = 0; j < 8; ++j) { h = cfma(sd.lam[u], h, v[u][j]); v[u][j] = h; }
    ldsw<v4u>(sd.a_tw, v4u{pk2(v[u][0].x, v[u][1].x), pk2(v[u][2].x, v[u][3].x), pk2(v[u][4].x, v[u][5].x), pk2(v[u][6].x, v[u][7].x)});
    ldsw<v4u>(sd.a_tw + 1024, v4u{pk2(v[u][0].y, v[u][1].y), pk2(v[u][2].y, v[u][3].y), pk2(v[u][4].y, v[u][5].y), pk2(v[u][6].y, v[u][7].y)});
    asm volatile("s_waitcnt lgkmcnt(0)" ::: "memory");
#pragma unroll
    for (int mm = 0; mm < 2; ++mm) st128(c, __builtin_bit_cast(v4u, cat(trread(sd.a_tr + mm * 8), trread(sd.a_tr + mm * 8 + 1024))), sd.v_hs[u] + mm * 1024);
    if (u + 1 < NU) asm volatile("s_waitcnt lgkmcnt(0)" ::: "memory");
  }
  SB0();
  c.vfs = sd.v_fs;
  if (TYPE == 1) {
    ldw<4>(c, c.wf[1]);
    SB0();
    waitv<5>();
    st_flag(c, sd.v_fs);
    c.vfs = OOB;
    c.fl = ld64(c, c.vfl);
    ldw<5>(c, c.wf[2]);
    SB0();
    SPIN_LOOP {
      if (__all(l >= 12 || (c.fl.x == c.T && c.fl.y == c.T2))) break;
      asm volatile("" ::: "memory"); SLP();
      c.fl = ld64(c, c.vfl);
    }
    __builtin_amdgcn_fence(__ATOMIC_ACQUIRE, "wavefront");
    ldh<0>(c, c.hf[0]); ldh<1>(c, c.hf[1]); if (NWR > 3) ldw<6>(c, c.wf[NWR - 1]);
  }
  if (TYPE == 1) {
    if (si == 39) {
      SPIN_LOOP {
        bool ok = true;
        asm volatile("" ::: "memory");
#pragma unroll
        for (int k = 0; k < 4; ++k) {
          const v4u t = ld128(c, WS_STAT + (k * 512 + w * 64 + l) * 16);
          ok = ok && t.y == c.T && t.w == c.T2;
        }
        if (__all(ok)) break;
        __builtin_amdgcn_s_sleep(1);
      }
    }
  }
  c.bump = TYPE == 1 && si == 39;
  SB0();
}

template <int J> struct P2 {};
DI void phase2(Ctx& c, const u16* dsk, float* out) {
  const int l = c.lane, i = l & 15, q = l >> 4, w = c.w, rt = c.rt, ct = c.ct;
  v4u wf[12][5], hf[12][4];
  f32x4 acc[4][5];
  v2u fl = c.fl;
  STAMP(6); CSTAMP(2);
  const unsigned a4 = w * 4096 + l * 16, a1 = w * 1024 + l * 16;
  float r2[4];
  f32x2 st[3]; v2u xv[3], dv[3];
  f32x4 base[3];
  unsigned oo[3], ra[3], rs[3], b4, b4h, b1, b2;
  auto body = [&](auto JJ) {
    constexpr int j = JJ.v;
    if constexpr (j < 3) {
#pragma unroll
      for (int a = 0; a < 5; ++a) wf[j][a] = ldsr<v4u>(tile_base(j, a) + (a < 4 ? a4 : a1));
    } else if constexpr (j < 3 + NWR) {
#pragma unroll
      for (int a = 0; a < 5; ++a) wf[j][a] = c.wf[j - 3][a];
    }
    if constexpr (j < 2) {
#pragma unroll
      for (int m = 0; m < 4; ++m) hf[j][m] = c.hf[j][m];
    }
    if constexpr (j == 4) {
      const bool need = l >= 12 && l < 24 && w + 8 * (l >> 1) < NS2;
      SPIN_LOOP {
        if (__all(!need || (fl.x == c.T && fl.y == c.T2))) break;
        asm volatile("" ::: "memory"); SLP();
        fl = ld64(c, c.vfl);
      }
      __builtin_amdgcn_fence(__ATOMIC_ACQUIRE, "wavefront");
    }
    if constexpr (j == 3) fl = ld64(c, c.vfl);
    if constexpr (j + 2 < 12) ldh<j + 2>(c, hf[j + 2]);
    if constexpr (j + 3 < 12 && j + 3 >= 3 + NWR) ldw<j + 3>(c, wf[j + 3]);
    if constexpr (j == 1) {
#pragma unroll
      for (int m = 0; m < 4; ++m) { const float r = ldsr<float>(L_ST + (rt * 64 + (m >> 1) * 32 + 8 * (i >> 2) + 4 * (m & 1) + (i & 3)) * 8 + 4); r2[m] = r * r; PIN(r2[m]); }
#pragma unroll
      for (int k = 0; k < 3; ++k) {
        const int tile = (w + 8 * k) < 20 ? w + 8 * k : w, m = tile / 5;
        st[k] = ldsr<f32x2>(L_ST + (rt * 64 + (m >> 1) * 32 + 8 * (i >> 2) + 4 * (m & 1) + (i & 3)) * 8);
        PIN(st[k]);
      }
    }
    if constexpr (j == 9) {
      int l = threadIdx.x & 63; PIN(l);
      const int i = l & 15, q = l >> 4;
#pragma unroll
      for (int k = 0; k < 3; ++k) {
        const int tile = (w + 8 * k) < 20 ? w + 8 * k : w, m = tile / 5, dt = tile - m * 5;
        const int tl = rt * 64 + (m >> 1) * 32 + 8 * (i >> 2) + 4 * (m & 1) + (i & 3), d = ct * 80 + dt * 16 + 4 * q;
        oo[k] = (c.rb * 256 + tl) * D_ + d;
        xv[k] = *(const v2u*)(c.x + oo[k]);
        dv[k] = *(const v2u*)(dsk + d);
        ra[k] = tile_base(m, dt) + l * 16; rs[k] = tile_wstride(m, dt);
        PIN(oo[k]); PIN(ra[k]);
      }
    }
    if constexpr (j == 10) {
      int l = threadIdx.x & 63; PIN(l);
      b4 = w * 4096 + l * 16; b4h = b4 + 65536; b1 = 98304 + w * 1024 + l * 16; b2 = 147456 + w * 2048 + l * 16;
      PIN(b4); PIN(b4h); PIN(b1); PIN(b2);
    }
    if constexpr (j == 11) {
#pragma unroll
      for (int k = 0; k < 3; ++k) {
        const unsigned x0 = xv[k].x, x1 = xv[k].y, d0 = dv[k].x, d1 = dv[k].y;
        const float xf[4] = {bf2f(x0), bfhi(x0), bf2f(x1), bfhi(x1)}, df[4] = {bf2f(d0), bfhi(d0), bf2f(d1), bfhi(d1)};
#pragma unroll
        for (int e = 0; e < 4; ++e) {
          float xn = rbf(rbf(xf[e] - st[k].x) * st[k].y);
          asm("" : "+v"(xn));
          base[k][e] = xf[e] + rbf(xn * df[e]);
        }
        PIN(base[k]);
      }
    }
    if constexpr (j == 6 || j == 7) {
      if ((w < 6) == (j == 7)) {
        MFMA_DRAIN();
#pragma unroll
        for (int m = 0; m < 4; ++m)
#pragma unroll
          for (int a = 0; a < 5; ++a) {
#pragma unroll
            for (int e = 0; e < 4; ++e) asm volatile("v_mul_f32 %0, %1, %0" : "+v"(acc[m][a][e]) : "v"(r2[m]));
          }
        asm volatile("s_nop 1");
      }
    }
    SB0();
#pragma unroll
    for (int a = 0; a < 5; ++a)
#pragma unroll
      for (int m = 0; m < 4; ++m) mf<j == 0>(acc[m][a], __builtin_bit_cast(bf16x8, wf[j][a]), __builtin_bit_cast(bf16x8, hf[j][m]));
    if constexpr (j == 0) { waitv<4>(); st_flag(c, c.vfs); }
    if constexpr (j == 2) {
      BAR();
      __builtin_amdgcn_raw_buffer_store_b32(c.T, c.rws, (c.bump && w == 0 && l == 0) ? WS_EPOCH : OOB, 0, 16);
    }
    if (j == 1) STAMP(10);
    if (j == 3) STAMP(11);
    if (j == 4) STAMP(12);
    if (j == 7) STAMP(13);
    SB0();
  };
  body(IC<0>{}); body(IC<1>{}); body(IC<2>{}); body(IC<3>{}); body(IC<4>{}); body(IC<5>{});
  body(IC<6>{}); body(IC<7>{}); body(IC<8>{}); body(IC<9>{}); body(IC<10>{}); body(IC<11>{});
  MFMA_DRAIN();
  SB0();
  STAMP(7);
#pragma unroll
  for (int m = 0; m < 4; ++m)
#pragma unroll
    for (int a = 0; a < 5; ++a) {
      constexpr unsigned dummy = 0; (void)dummy;
      const unsigned tb = tile_base(m, a), ts = tile_wstride(m, a);
      ldsw<f32x4>(ts == 4096 ? (tb >= 65536 ? b4h + (tb - 65536) : b4 + tb) : (ts == 1024 ? b1 + (tb - 98304) : b2 + (tb - 147456)), acc[m][a]);
    }
  BAR();
#pragma unroll
  for (int k = 0; k < 3; ++k) {
    if (k < 2 || w < 4) {
      f32x4 s = base[k];
#pragma unroll
      for (int v = 0; v < 8; ++v) s += ldsr<f32x4>(ra[k] + v * rs[k]);
      *(f32x4*)(out + oo[k]) = s;
    }
  }
}

__global__ void __launch_bounds__(512, 2)
fused_kernel(const u16* x, const u16* lre, const u16* lim, const u16* bre, const u16* bim, const u16* cre, const u16* cim, const u16* ldt,
             const u16* dsk, const u16* wg, const u16* wu, const u16* wd, float* out, char* ws) {
  Ctx c;
  c.x = x; c.bre = bre; c.bim = bim; c.cre = cre; c.cim = cim; c.wg = wg; c.wu = wu; c.wd = wd; c.ws = ws;
  c.lane = threadIdx.x & 63;
  c.w = __builtin_amdgcn_readfirstlane(threadIdx.x >> 6);
  const int xc = blockIdx.x & 7, ix = blockIdx.x >> 3;
  c.rb = (xc >> 1) * 2 + (ix >> 4); c.cg = (xc & 1) * 16 + (ix & 15);
  c.rt = c.cg >> 3; c.ct = c.cg & 7;
  c.shr = ix >> 3;
  c.rws = __builtin_amdgcn_make_buffer_rsrc(ws, 0, WS_END, 0x00020000);
  STAMP(0);
  if (c.cg < 27) phase1<0>(c, lre, lim, ldt);
  else phase1<1>(c, lre, lim, ldt);
  phase2(c, dsk, out);
}

extern "C" void kernel_launch(void* const* d_in, const int* in_sizes, int n_in, void* d_out, int out_size, void* d_ws, size_t ws_size,
                              hipStream_t stream) {
  (void)in_sizes; (void)n_in; (void)out_size; (void)ws_size;
  (void)hipFuncSetAttribute((const void*)fused_kernel, hipFuncAttributeMaxDynamicSharedMemorySize, LDS_BYTES);
  fused_kernel<<<256, 512, LDS_BYTES, stream>>>((const u16*)d_in[0], (const u16*)d_in[1], (const u16*)d_in[2], (const u16*)d_in[3], (const u16*)d_in[4],
                                                (const u16*)d_in[5], (const u16*)d_in[6], (const u16*)d_in[7], (const u16*)d_in[8], (const u16*)d_in[10],
                                                (const u16*)d_in[11], (const u16*)d_in[12], (float*)d_out, (char*)d_ws);
}
```

```cpp
#include <hip/hip_runtime.h>
#include <stdint.h>

#define GAS __attribute__((address_space(1)))
#define LAS __attribute__((address_space(3)))
#define DI __device__ __forceinline__
typedef unsigned short u16;
typedef short bf16x8 __attribute__((ext_vector_type(8)));
typedef short s16x4 __attribute__((ext_vector_type(4)));
typedef __bf16 bf2 __attribute__((ext_vector_type(2)));
typedef float f32x4 __attribute__((ext_vector_type(4)));
typedef float f32x2 __attribute__((ext_vector_type(2)));
typedef unsigned v4u __attribute__((ext_vector_type(4)));
typedef unsigned v2u __attribute__((ext_vector_type(2)));

#define XMAP 2
#define SCHK 7
#define L2PF 0
#define PRE 12
#define ROTC 0
#define ORD 0
#define UO 1
#define JC 6
#define JE 8
#define L2P 0
#define NSHR (XMAP == 1 ? 8 : 4)
#define JF 0
#define P6BR
#define P6D 1
#define JS 2
#define HF 0
#define HM (HF ? 0xfull : 0xfffull)
#define NA 2
#define NB 2
#define PRIO 2
#define JB 3
#define NWR 4
#define PH 2
#define NWP 7
#define PWD 3

constexpr int L_ = 2048, D_ = 640, F_ = 1728;
constexpr int ustep(int u) { return UO ? (u >= 27 ? 27 + u : 67 + u) : 54 + u; }
constexpr int NS2 = 94;
constexpr unsigned WS_EPOCH = 0;
constexpr unsigned WS_STAT = 4096;
constexpr unsigned WS_AGG = WS_STAT + 2048 * 16;
constexpr unsigned WS_FLAGH = WS_AGG + 8 * 640 * 16;
constexpr unsigned WS_FLAGW = WS_FLAGH + 32 * 8 * 256;
constexpr unsigned WS_CTL_END = WS_FLAGW + 8 * 8 * 128;
constexpr unsigned WS_W2T = 262144;
constexpr unsigned W2T_CT = NS2 * 5 * 1024;
constexpr unsigned WS_H = WS_W2T + 8 * W2T_CT;
constexpr unsigned H_RT = NS2 * 4 * 1024;
constexpr unsigned WS_END = WS_H + 32 * H_RT;
constexpr unsigned OOB = 0x70000000u;
static_assert(WS_CTL_END <= WS_W2T, "ws map");
constexpr unsigned LX(int k) { return k * 32768; }
constexpr unsigned LWB(int k) { return 98304 + ((k + 1) % 3) * 16384; }
constexpr unsigned LWS(int k) { return 147456 + k * 4096; }
constexpr unsigned L_ST = 159744;
constexpr unsigned L_CS = L_ST + 2048;
constexpr unsigned L_CSS = L_CS + 512;
constexpr unsigned L_COEF = L_CSS + 1024;
constexpr unsigned L_POW = LWS(0);
constexpr unsigned L_AGG = LWS(1);
constexpr unsigned L_DUMMY = LWS(2);
constexpr unsigned L_XT0 = LWB(0) + 8192;
constexpr unsigned L_XT1 = LWS(0);
constexpr unsigned LDS_BYTES = 163840;
static_assert(L_COEF + 512 <= LDS_BYTES && LWB(2) == LX(2) + 32768, "lds map");
constexpr unsigned tile_base(int ch, int j) {
  return ch < 3 ? (j < 4 ? LX((ch + 1) % 3) + j * 1024 : LWB((ch + 1) % 3)) : (j < 3 ? LWB(j) + 8192 : LWS(0) + (j - 3) * 1024);
}
constexpr unsigned tile_wstride(int ch, int j) { return ch < 3 ? (j < 4 ? 4096 : 1024) : (j < 3 ? 1024 : 2048); }
constexpr int SPIN_CAP = 1 << 15;

DI float bf2f(unsigned v) { return __uint_as_float(v << 16); }
DI float bfhi(unsigned v) { return __uint_as_float(v & 0xffff0000u); }
DI unsigned pk2(float a, float b) { bf2 v = {(__bf16)a, (__bf16)b}; return __builtin_bit_cast(unsigned, v); }
DI float rbf(float a) { return bf2f(pk2(a, 0.f) & 0xffffu); }
DI void mfa(f32x4& c, bf16x8 a, bf16x8 b) { asm volatile("v_mfma_f32_16x16x32_bf16 %0, %1, %2, %0" : "+v"(c) : "v"(a), "v"(b)); }
DI void mfz(f32x4& c, bf16x8 a, bf16x8 b) { asm volatile("v_mfma_f32_16x16x32_bf16 %0, %1, %2, 0" : "=&v"(c) : "v"(a), "v"(b)); }
template <bool Z> DI void mf(f32x4& c, bf16x8 a, bf16x8 b) { if (Z) mfz(c, a, b); else mfa(c, a, b); }
#define MFMA_DRAIN() asm volatile("s_nop 7\n\ts_nop 1" ::: "memory")
DI v4u mkrsrc(const void* p, unsigned bytes) {
  uint64_t a = (uint64_t)p;
  v4u r; r.x = (unsigned)a; r.y = (unsigned)(a >> 32) & 0xffffu; r.z = bytes; r.w = 0x00020000u;
  return r;
}
template <unsigned SI, unsigned LI> DI void glds16(v4u rs, unsigned voff, unsigned sb, unsigned lb) {
  unsigned t;
  asm volatile("s_add_u32 m0, %4, %6\n\ts_add_u32 %0, %3, %5\n\tbuffer_load_dwordx4 %1, %2, %0 offen lds"
               : "=&s"(t) : "v"(voff), "s"(rs), "s"(sb), "s"(lb), "i"(SI), "i"(LI) : "memory", "scc");
}
template <unsigned SI, unsigned LI> DI void glds16sc1(v4u rs, unsigned voff, unsigned lb) {
  unsigned t;
  asm volatile("s_add_u32 m0, %3, %5\n\ts_mov_b32 %0, %4\n\tbuffer_load_dwordx4 %1, %2, %0 offen sc1 lds"
               : "=&s"(t) : "v"(voff), "s"(rs), "s"(lb), "i"(SI), "i"(LI) : "memory", "scc");
}
template <class T> DI T ldsr(unsigned a) { return *(LAS T*)(uintptr_t)a; }
template <class T> DI void ldsw(unsigned a, T v) { *(LAS T*)(uintptr_t)a = v; }
DI s16x4 trread(unsigned a) { return __builtin_amdgcn_ds_read_tr16_b64_v4i16((LAS s16x4*)(uintptr_t)a); }
DI bf16x8 cat(s16x4 lo, s16x4 hi) { return __builtin_shufflevector(lo, hi, 0, 1, 2, 3, 4, 5, 6, 7); }
template <int N> DI void waitv() { asm volatile("s_waitcnt vmcnt(%0)" ::"n"(N) : "memory"); }
#define BAR() asm volatile("s_waitcnt lgkmcnt(0)\n\ts_barrier" ::: "memory")
#define LGKM0() __builtin_amdgcn_s_waitcnt(0xc07f)
#define PRI(n) do { if (PRIO == 2) { SB0(); __builtin_amdgcn_s_setprio(n); SB0(); } } while (0)
#define SB0() __builtin_amdgcn_sched_barrier(0)
#define PIN(x) asm volatile("" : "+v"(x))
#define SLPN 0
#define SLP() do { if (SLPN) __builtin_amdgcn_s_sleep(SLPN); } while (0)
#define SPIN_LOOP _Pragma("nounroll") for (int sp = 0; sp < SPIN_CAP; ++sp)
template <int N> DI float ror(float v) { return __builtin_bit_cast(float, __builtin_amdgcn_update_dpp(0, __builtin_bit_cast(int, v), 0x120 + N, 0xf, 0xf, false)); }
DI void sw16(float v, float& e, float& o) { unsigned u = __float_as_uint(v); auto r = __builtin_amdgcn_permlane16_swap(u, u, false, false); e = __uint_as_float(r[0]); o = __uint_as_float(r[1]); }
DI void sw32(float v, float& lo, float& hi) { unsigned u = __float_as_uint(v); auto r = __builtin_amdgcn_permlane32_swap(u, u, false, false); lo = __uint_as_float(r[0]); hi = __uint_as_float(r[1]); }
DI float sumq(float v) { float a, b; sw16(v, a, b); v = a + b; sw32(v, a, b); return a + b; }
DI float sumr(float v) { v += ror<8>(v); v += ror<4>(v); v += ror<2>(v); v += ror<1>(v); return v; }
DI void gatherq(float v, float (&a)[4]) { float e, o; sw16(v, e, o); sw32(e, a[0], a[2]); sw32(o, a[1], a[3]); }
typedef f32x2 cpx;
DI cpx cfma(cpx a, cpx b, cpx c) {
  cpx r;
  asm("v_pk_fma_f32 %0, %1, %2, %3 op_sel:[1,1,0] op_sel_hi:[1,0,1] neg_lo:[1,0,0]\n\tv_pk_fma_f32 %0, %1, %2, %0 op_sel_hi:[0,1,1]" : "=&v"(r) : "v"(a), "v"(b), "v"(c));
  return r;
}
DI cpx cmul(cpx a, cpx b) {
  cpx r;
  asm("v_pk_mul_f32 %0, %1, %2 op_sel:[1,1] op_sel_hi:[1,0] neg_lo:[1,0]\n\tv_pk_fma_f32 %0, %1, %2, %0 op_sel_hi:[0,1,1]" : "=&v"(r) : "v"(a), "v"(b));
  return r;
}
DI cpx csel(bool p, cpx a) { return cpx{p ? a.x : 1.f, p ? a.y : 0.f}; }
DI cpx czero(bool p, cpx a) { return cpx{p ? 0.f : a.x, p ? 0.f : a.y}; }
template <int V> struct IC { static constexpr int v = V; };

#define STAMP(k) do {} while (0)
#define CSTAMP(k) do {} while (0)
struct Ctx {
  const u16 *dsk, *x, *bre, *bim, *cre, *cim, *wg, *wu, *wd;
  char* ws;
  __amdgpu_buffer_rsrc_t rws;
  v2u tag;
  unsigned T, T2;
  int lane, w, wc, rb, cg, rt, ct, shr;
  unsigned vw2, vh2, vfl;
  v4u wf[NWR][5], hf[2][4];
  v2u fl;
  unsigned vfs;
  bool bump;
};
DI v4u ld128(const Ctx& c, unsigned vo, unsigned so = 0) { return __builtin_amdgcn_raw_buffer_load_b128(c.rws, vo, so, 16); }
DI v2u ld64(const Ctx& c, unsigned vo) { return __builtin_amdgcn_raw_buffer_load_b64(c.rws, vo, 0, 16); }
DI void st128(const Ctx& c, v4u v, unsigned vo) { __builtin_amdgcn_raw_buffer_store_b128(v, c.rws, vo, 0, 16); }
DI void st_flag(const Ctx& c, unsigned vo) { __builtin_amdgcn_raw_buffer_store_b64(c.tag, c.rws, vo, 0, 16); }

DI float fexp(float x) {
  const float t = x * 1.44269502162933349609375f;
  const float r = fmaf(x, 1.925963033500011e-8f, fmaf(x, 1.44269502162933349609375f, -t));
  const float e = __builtin_amdgcn_exp2f(t);
  return fmaf(e, r * 0.693147182464599609375f, e);
}
DI void fsincos(float z, float& sn, float& cs) {
  const float k = rintf(z * 0.63661977236758134f);
  float r = fmaf(k, -1.5707963705062866f, z);
  r = fmaf(k, 4.3711388286737929e-08f, r);
  r = fmaf(k, 1.7151245100058819e-15f, r);
  const float r2 = r * r;
  const float sp = fmaf(r * r2, fmaf(r2, fmaf(r2, -1.9515295891e-4f, 8.3321608736e-3f), -1.6666654611e-1f), r);
  const float cp = fmaf(r2 * r2, fmaf(r2, fmaf(r2, 2.443315711809948e-5f, -1.388731625493765e-3f), 4.166664568298827e-2f), fmaf(r2, -0.5f, 1.f));
  const int n = (int)k;
  sn = (n & 1) ? cp : sp; cs = (n & 1) ? sp : cp;
  if (n & 2) sn = -sn;
  if ((n + 1) & 2) cs = -cs;
}
template <int J> DI void ldw(const Ctx& c, v4u (&d)[5]) {
  const unsigned vo = J == 11 ? (c.wc < 6 ? c.vw2 : OOB) : c.vw2;
#pragma unroll
  for (int a = 0; a < 4; ++a) d[a] = ld128(c, vo + a * 1024, J * 40960);
  d[4] = ld128(c, vo, J * 40960 + 4096);
}
template <int J> DI void ldh(const Ctx& c, v4u (&d)[4]) {
  const unsigned vo = J == 11 ? (c.wc < 6 ? c.vh2 : OOB) : c.vh2;
#pragma unroll
  for (int m = 0; m < 4; ++m) d[m] = ld128(c, vo + m * 1024, J * 32768);
}

template <int TYPE> struct G1 {
  static constexpr int NU = TYPE == 0 ? 1 : 3;
  static constexpr int NG = TYPE == 0 ? 10 : 6;
  static constexpr int NR = TYPE == 0 ? 14 : 8;
  static constexpr int NP = (TYPE == 0 ? 7 : 6) - P6D;
  struct Frags { bf16x8 x[TYPE == 0 ? 4 : 2]; s16x4 wl[4], wh[4]; bf16x8 s[2 * NU]; };
  v4u rx, rw0, rw1, rs;
  unsigned vx0, vx1, vw, vs;
  unsigned sx, sw, ss, ss2;
  unsigned ax[2][2][2], aw[4], as[NU][2];
  f32x4 acc[4][4];
  f32x4 accs[2 * NU][2];
  f32x4 cs0, cs1, cs2;
  bf16x8 ones;
  int w;
  unsigned w1k, w4k, w1k6, vs6;

  DI void init_stage(const Ctx& c, int unit0) {
    const int l = c.lane;
    w = c.w; w1k = w * 1024; w4k = w * 4096;
    rx = mkrsrc(c.x, L_ * D_ * 2);
    const unsigned ch = (l & 7) ^ ((l >> 3) & 3);
    vx0 = (l >> 3) * 1280 + ch * 16;
    vx1 = (l >> 3) * 1280 + (ch ^ 4) * 16;
    sx = (c.rb * 256 + w * 32) * 1280;
    rs = mkrsrc((w & 2) ? c.bim : c.bre, 640 * 640 * 2);
    vs = (l >> 3) * 1280 + (((l & 7) ^ (l >> 3)) * 16);
    vs6 = w < 4 ? vs : OOB; w1k6 = w < 4 ? w1k : 65536;
    if (TYPE == 0) {
      rw0 = mkrsrc(c.wg, D_ * F_ * 2); rw1 = mkrsrc(c.wu, D_ * F_ * 2);
      const unsigned key2 = ((l >> 4) & 1) | ((w & 1) << 1);
      vw = (l >> 3) * 3456 + (((l & 7) ^ (key2 << 1)) * 16);
      sw = w * 8 * 3456 + c.cg * 128;
      ss2 = (unit0 * 16 + (w & 1) * 8) * 1280;
    } else {
      ss = (min(unit0 + (w >> 2), 39) * 16 + (w & 1) * 8) * 1280;
      ss2 = (min(unit0 + 2, 39) * 16 + (w & 1) * 8) * 1280;
    }
  }
  DI void init_read(const Ctx& c) {
    const int l = c.lane, i = l & 15, q = l >> 4;
    const int wt = w & 3, wn = w >> 2;
#pragma unroll
    for (int kk = 0; kk < 2; ++kk) {
      const unsigned ch = (((kk * 4 + q) ^ (i & 7)) * 16);
#pragma unroll
      for (int pr = 0; pr < (TYPE == 0 ? 2 : 1); ++pr) {
        ax[0][pr][kk] = (wt * 64 + (pr ^ wn) * 32 + 8 * (i >> 2) + (i & 3)) * 128 + ch;
        ax[1][pr][kk] = ax[0][pr][kk] + 65536;
        PIN(ax[0][pr][kk]); PIN(ax[1][pr][kk]);
      }
#pragma unroll
      for (int u = 0; u < NU; ++u) {
        as[u][kk] = (TYPE == 0 ? LWS(0) : LWB(2) + ((u + (w >> 1)) % 3) * 4096) + i * 128 + ch;
        PIN(as[u][kk]);
      }
    }
    if (TYPE == 0) {
      const int r = (l >> 2) & 3, cc = l & 3;
      const unsigned key2 = ((r >> 1) & 1) | ((q & 1) << 1);
#pragma unroll
      for (int a = 0; a < 4; ++a) {
        const int act = a ^ wt;
        aw[a] = LWB(2) + (act >> 1) * 8192 + (8 * q + r) * 128 + (((2 * wn + (act & 1)) ^ key2) * 32) + cc * 8;
        PIN(aw[a]);
      }
      cs1 = f32x4{0.f, 0.f, 0.f, 0.f}; cs2 = f32x4{0.f, 0.f, 0.f, 0.f};
      PIN(cs1); PIN(cs2);
    }
    ones = bf16x8{0x3f80, 0x3f80, 0x3f80, 0x3f80, 0x3f80, 0x3f80, 0x3f80, 0x3f80};
    PIN(ones);
  }
  template <int P, int S> DI void piece() {
    constexpr int K = S % 3;
    if constexpr (P < 4) glds16<S * 128 + P * 10240, LX(K) + P * 1024>(rx, (P & 1) ? vx1 : vx0, sx, w4k);
    else if constexpr (P < 6) {
      if constexpr (TYPE == 0) glds16<S * (64 * 3456), LWB(K) + (P - 4) * 8192>(P == 4 ? rw0 : rw1, vw, sw, w1k);
      else if constexpr (P == 4) glds16<S * 128, LWB(K)>(rs, vs, ss, w1k);
    } else {
      if (w < 4)
      glds16<S * 128, (TYPE == 0 ? LWS(K) : LWB(K) + 8192)>(rs, vs6, ss2, w1k6);
    }
  }
  template <int S> DI void half0() { piece<6, S>(); piece<0, S>(); piece<4, S>(); piece<1, S>(); }
  template <int S> DI void half1() { piece<2, S>(); piece<5, S>(); piece<3, S>(); }

  template <int K, int KK, int R> DI void rd(Frags& f) {
    constexpr unsigned ow = LWB(K) - LWB(2), ox = K == 1 ? 32768 : 0;
    if constexpr (TYPE == 0) {
      if (R == 0) f.wl[0] = trread(aw[0] + (ow + KK * 4096));
      else if (R == 1) f.wh[0] = trread(aw[0] + (ow + KK * 4096 + 512));
      else if (R < 6) f.x[(R - 2) & 3] = ldsr<bf16x8>(ax[K == 2][((R - 2) >> 1) & 1][KK] + (ox + ((R - 2) & 1) * 512));
      else if (R < 12) {
        constexpr int a = ((R - 4) >> 1) & 3;
        if (R & 1) f.wh[a] = trread(aw[a] + (ow + KK * 4096 + 512)); else f.wl[a] = trread(aw[a] + (ow + KK * 4096));
      } else f.s[R & 1] = ldsr<bf16x8>(as[0][KK] + (K * 4096 + (R & 1) * 2048));
    } else {
      if (R == 1 || R == 2) f.x[(R - 1) & 1] = ldsr<bf16x8>(ax[K == 2][0][KK] + (ox + ((R - 1) & 1) * 512));
      else { constexpr int a = R == 0 ? 0 : R - 2; f.s[a] = ldsr<bf16x8>(as[a >> 1][KK] + (ow + (a & 1) * 2048)); }
    }
  }
  template <bool Z, int M> DI void mm(const Frags& f) {
    if constexpr (TYPE == 0 && M < 16) mf<Z>(acc[M >> 2][M & 3], cat(f.wl[M >> 2], f.wh[M >> 2]), f.x[M & 3]);
    else { constexpr int a = (M - (TYPE == 0 ? 16 : 0)) >> 1; mf<Z>(accs[a][M & 1], f.x[M & 1], f.s[a]); }
  }
  template <int SUB, int G, class H> DI void group(const Frags& cur, Frags& nxt, H& hook) {
    constexpr int N = SUB + 1, K = (N >> 1) % 3, KK = N & 1;
    constexpr bool RD = N < 20;
    mm<SUB == 0, 2 * G>(cur);
    if constexpr (RD && 2 * G < NR) rd<K, KK, 2 * G>(nxt);
    mm<SUB == 0, 2 * G + 1>(cur);
    if constexpr (RD && 2 * G + 1 < NR) rd<K, KK, 2 * G + 1>(nxt);
    if constexpr (TYPE == 0) {
      if (G == 0) mf<SUB == 0>(cs0, ones, cat(cur.wl[0], cur.wh[0]));
      if (G == 2) { if (__builtin_expect(w == (SUB & 7), 0)) { mfa(cs1, ones, cur.s[0]); mfa(cs2, ones, cur.s[1]); } }
    } else {
      if (G == 0) { mf<SUB == 0>(cs1, ones, cur.s[0]); mf<SUB == 0>(cs2, ones, cur.s[1]); }
    }
    hook(IC<G>{});
    SB0();
    if constexpr (G + 1 < NG) group<SUB, G + 1>(cur, nxt, hook);
  }
  template <int SUB, class H> DI void part(const Frags& cur, Frags& nxt, H hook) { group<SUB, 0>(cur, nxt, hook); }
  template <int R> DI void rdall(Frags& f) { rd<0, 0, R>(f); if constexpr (R + 1 < NR) rdall<R + 1>(f); }
};

template <int NU> struct Side {
  unsigned xr[5], pa, pb, pd;
  float v[10], s, dt, e, sn, cs;
  cpx lam[NU], L8[NU], L32[NU], u0[NU], u1[NU], f1[NU], W0[NU], W1[NU], wk0[NU], wk1[NU], t0, t1, t2;
  float u2;
  unsigned a_mu, a_cs, v_hg, v_fg, a_st, a_css[NU], a_aw[NU], a_ar[NU], v_pub[NU], v_lk0[NU], v_lk1[NU], v_hs[NU], v_fs, a_tw, a_tr, v_x0, v_x1;
  static constexpr int PERU = 16;
  static constexpr int NCH = 8 + PERU * NU + 5;
  template <int N> DI void tick(Ctx& c, int srow, int unit0) {
    const int l = c.lane, i = l & 15, q = l >> 4, w = c.w, wt = w & 3, wn = w >> 2, grp = wt * 2 + wn;
    if constexpr (N == 0) {
#pragma unroll
      for (int j = 0; j < 5; ++j) { v[2 * j] = bf2f(xr[j]); v[2 * j + 1] = bfhi(xr[j]); PIN(v[2 * j]); PIN(v[2 * j + 1]); }
    } else if constexpr (N == 1) {
      s = ((v[0] + v[1]) + (v[2] + v[3])) + ((v[4] + v[5]) + (v[6] + v[7])) + (v[8] + v[9]); PIN(s);
    } else if constexpr (N == 2) { s = sumr(s); PIN(s); }
    else if constexpr (N == 3) { s = sumq(s) * (1.f / 640.f); PIN(s); }
    else if constexpr (N == 4) {
#pragma unroll
      for (int j = 0; j < 10; ++j) { v[j] -= s; PIN(v[j]); }
    } else if constexpr (N == 5) {
      e = v[0] * v[0];
#pragma unroll
      for (int j = 1; j < 10; ++j) e = fmaf(v[j], v[j], e);
      PIN(e);
    } else if constexpr (N == 6) { e = sumr(e); PIN(e); }
    else if constexpr (N == 7) {
      const float var = rbf(sumq(e) * (1.f / 640.f));
      const float sd = rbf(__builtin_amdgcn_sqrtf(rbf(var + 1e-5f)));
      float rc = __builtin_amdgcn_rcpf(sd);
      rc = fmaf(fmaf(-sd, rc, 1.f), rc, rc);
      st128(c, v4u{__float_as_uint(rbf(s)), c.T, __float_as_uint(rc), c.T2}, l == 0 ? WS_STAT + srow * 16 : OOB);
    } else if constexpr (N < 8 + PERU * NU) {
      constexpr int u = (N - 8) / PERU, k = (N - 8) % PERU;
      const int ul = NU == 1 ? 0 : (u + (w >> 1)) % 3, ua = unit0 + ul;
      const int e1 = grp - 2 - 2 * q, k0 = c.rb - 1 - q;
      const unsigned pw = L_POW + (ul * 16 + i) * 8;
      if constexpr (k == 0) { if (NU == 1) { dt = rbf(fexp(bf2f(pd))); PIN(dt); } }
      else if constexpr (k == 1) { if (NU == 1) { e = fexp(dt * bf2f(pa)); PIN(e); } }
      else if constexpr (k == 2) { if (NU == 1) { fsincos(dt * bf2f(pb), sn, cs); PIN(sn); PIN(cs); } }
      else if constexpr (k == 3) {
        if (NU == 1) { lam[u] = cpx{e * cs, e * sn}; t0 = cmul(lam[u], lam[u]); t0 = cmul(t0, t0); L8[u] = cmul(t0, t0); t0 = cmul(L8[u], L8[u]); L32[u] = cmul(t0, t0); }
        else { lam[u] = ldsr<cpx>(pw); L8[u] = ldsr<cpx>(pw + 512); t0 = ldsr<cpx>(pw + 1024); L32[u] = ldsr<cpx>(pw + 1536); }
      }
      else if constexpr (k == 4) { f1[u] = cmul(csel(q & 1, L8[u]), csel(q & 2, t0)); PIN(lam[u]); PIN(L8[u]); PIN(t0); PIN(L32[u]); PIN(f1[u]); }
      else if constexpr (k == 5) {
        u1[u] = czero(q < 2, csel(q == 3, L8[u])); PIN(u1[u]);
        u2 = q == 3 ? 1.f : 0.f; PIN(u2);
      } else if constexpr (k == 6) { u0[u] = czero(q == 0, q == 3 ? t0 : csel(q == 2, L8[u])); PIN(u0[u]); }
      else if constexpr (k == 7) {
        if (NU == 1) { t0 = cmul(L32[u], L32[u]); t1 = cmul(t0, t0); PIN(t0); PIN(t1); } else { t0 = ldsr<cpx>(pw + 2048); t1 = ldsr<cpx>(pw + 2560); }
      }
      else if constexpr (k == 8) { W1[u] = cmul(csel(e1 & 1, L32[u]), csel(e1 & 2, t0)); t2 = cmul(csel(grp & 1, L32[u]), csel(grp & 2, t0)); PIN(W1[u]); PIN(t2); }
      else if constexpr (k == 9) { W1[u] = cmul(W1[u], csel(e1 & 4, t1)); t2 = cmul(t2, csel(grp & 4, t1)); PIN(W1[u]); PIN(t2); }
      else if constexpr (k == 10) { W0[u] = czero(e1 < -1, csel(e1 >= 0, cmul(W1[u], L32[u]))); W1[u] = czero(e1 < 0, W1[u]); PIN(W0[u]); PIN(W1[u]); }
      else if constexpr (k == 11) { t0 = cmul(t1, t1); t1 = cmul(t0, t0); wk1[u] = cmul(t2, csel(k0 & 1, t0)); PIN(t1); PIN(wk1[u]); }
      else if constexpr (k == 12) { wk1[u] = cmul(wk1[u], csel(k0 & 2, t1)); t1 = cmul(t1, t1); PIN(t1); PIN(wk1[u]); }
      else if constexpr (k == 13) { wk0[u] = czero(k0 < 0, cmul(wk1[u], csel(k0 & 4, t1))); wk1[u] = czero(k0 < 4, wk1[u]); PIN(wk0[u]); PIN(wk1[u]); }
      else if constexpr (k == 14) {
        a_css[u] = L_CSS + (NU == 1 ? q * 128 : ul * 128) + i * 4;
        a_ar[u] = L_AGG + (ul * 16 + i) * 64 + q * 16;
        a_aw[u] = q == 3 ? L_AGG + (ul * 16 + i) * 64 + grp * 8 : L_DUMMY + l * 8;
        v_hs[u] = ua < 40 ? WS_H + (c.rb * 4 + wt) * H_RT + ustep(ua) * 4096 + wn * 2048 + l * 16 : OOB;
        PIN(a_css[u]); PIN(a_ar[u]); PIN(a_aw[u]); PIN(v_hs[u]);
      } else {
        const unsigned po = WS_AGG + (ua * 16 + i) * 16;
        v_pub[u] = (q == 3 && ua < 40) ? po + c.rb * 10240 : OOB;
        v_lk0[u] = (k0 >= 0 && ua < 40) ? po + q * 10240 : OOB;
        v_lk1[u] = (k0 >= 4 && ua < 40) ? po + (q + 4) * 10240 : OOB;
        PIN(v_pub[u]); PIN(v_lk0[u]); PIN(v_lk1[u]);
      }
    } else {
      constexpr int k = N - 8 - PERU * NU;
      const unsigned fbase = WS_FLAGH + (c.rb * 4 + wt) * 2048;
      if constexpr (k == 0) {
        if (NU == 1) {
          const int step = 2 * c.cg + wn;
          a_mu = L_ST + (wt * 64 + wn * 32 + 8 * (i >> 2) + (i & 3)) * 8;
          a_cs = L_CS + wn * 256 + wt * 64 + q * 16;
          v_hg = WS_H + (c.rb * 4 + wt) * H_RT + step * 4096 + wn * 2048 + l * 16;
          v_fg = l < 2 ? fbase + (step & 7) * 256 + (step >> 3) * 16 + l * 8 : OOB;
          PIN(a_mu); PIN(a_cs); PIN(v_hg); PIN(v_fg);
        }
      } else if constexpr (k == 1) {
        const int step = ustep(unit0 + l);
        v_fs = (l < NU && unit0 + l < 40) ? fbase + (step & 7) * 256 + (step >> 3) * 16 + wn * 8 : OOB;
        a_st = L_ST + (grp * 4 + q) * 64;
        PIN(v_fs); PIN(a_st);
      } else if constexpr (k == 2) {
        const unsigned tr = (wn ? LWB(2) : LWB(1)) + 8192 + wt * 2048;
        const int pr = 4 * q + ((l >> 2) & 3);
        a_tw = tr + i * 64 + ((q ^ (i >> 1)) & 3) * 16;
        a_tr = tr + pr * 64 + ((l ^ (pr >> 1)) & 3) * 16;
        PIN(a_tw); PIN(a_tr);
      } else if constexpr (k == 3) {
        c.vh2 = WS_H + (c.rb * 4 + c.rt) * H_RT + c.wc * 4096 + l * 16;
        c.vfl = WS_FLAGH + (c.rb * 4 + c.rt) * 2048 + c.wc * 256 + (l & 31) * 8;
        PIN(c.vh2); PIN(c.vfl);
      } else {
        c.vw2 = WS_W2T + c.ct * W2T_CT + c.wc * 5120 + l * 16;
        const unsigned c0 = w * 64 + l, r0 = c0 / 10, c1 = c0 + 512, r1 = c1 / 10, xb = (c.rb * 256 + c.rt * 64) * 1280 + c.ct * 160;
        v_x0 = xb + r0 * 1280 + (c0 - r0 * 10) * 16;
        v_x1 = w < 2 ? xb + r1 * 1280 + (c1 - r1 * 10) * 16 : ((w == 2 && l < 10) ? c.ct * 160 + l * 16 : OOB);
        PIN(c.vw2); PIN(v_x0); PIN(v_x1);
      }
    }
  }
};

template <int TYPE> DI void phase1(Ctx& c, const u16* lre, const u16* lim, const u16* ldt) {
  const int l = c.lane, i = l & 15, q = l >> 4, w = c.w, wt = w & 3, wn = w >> 2;
  const int rb = c.rb, cg = c.cg;
  constexpr int NU = G1<TYPE>::NU, NP = G1<TYPE>::NP;
  G1<TYPE> g;
  Side<NU> sd;
  const int srow = rb * 256 + cg * 8 + w;
  const int si = rb * 5 + (cg - 27), sj = si, mi = rb * 27 + cg;
  const int unit0 = TYPE == 0 ? cg : 27 + 3 * (cg - 27);
  g.init_stage(c, unit0);
  unsigned ep;
  {
    const u16* xrow = c.x + (size_t)srow * D_;
    asm volatile(
        "global_load_dword %5, %8, %9 sc1\n\t"
        "global_load_dword %0, %6, %7\n\t"
        "global_load_dword %1, %6, %7 offset:256\n\t"
        "global_load_dword %2, %6, %7 offset:512\n\t"
        "global_load_dword %3, %6, %7 offset:768\n\t"
        "global_load_dword %4, %6, %7 offset:1024"
        : "=&v"(sd.xr[0]), "=&v"(sd.xr[1]), "=&v"(sd.xr[2]), "=&v"(sd.xr[3]), "=&v"(sd.xr[4]), "=&v"(ep)
        : "v"(l * 4), "s"(xrow), "v"(0), "s"(c.ws)
        : "memory");
    const int pidx = TYPE == 0 ? unit0 * 16 + i : (w == 0 ? (sj >> 2) * 64 + l : min(unit0 * 16 + l, 639));
    asm volatile("global_load_ushort %0, %3, %4\n\tglobal_load_ushort %1, %3, %5\n\tglobal_load_ushort %2, %3, %6"
                 : "=&v"(sd.pa), "=&v"(sd.pb), "=&v"(sd.pd) : "v"(pidx * 2), "s"(lre), "s"(lim), "s"(ldt) : "memory");
  }
  if (TYPE == 0) {
    g.template half0<0>(); g.template half1<0>();
    const v4u rj = mkrsrc(c.wd, F_ * D_ * 2);
    const unsigned c0 = w * 64 + l, r0 = c0 / 20, c1 = c0 + 512, r1 = c1 / 20;
    glds16<0, LWB(2) + 8192>(rj, r0 * 1280 + (c0 - r0 * 20) * 16, (mi >> 2) * 40960 + (mi & 3) * 320, g.w1k);
    glds16<0, LWS(2)>(rj, w < 2 ? r1 * 1280 + (c1 - r1 * 20) * 16 : OOB, (mi >> 2) * 40960 + (mi & 3) * 320, w < 2 ? g.w1k : 65536);
  } else {
    const v4u rj = mkrsrc(wn ? c.cim : c.cre, 640 * 640 * 2);
#pragma unroll
    for (int j = 0; j < 5; ++j) {
      const unsigned row = (wt * 5 + j) * 8 + (l >> 3);
      const unsigned vo = row * 1280 + (((l & 7) ^ ((row >> 1) & 7)) * 16);
      glds16<0, LX(2)>(rj, vo, (sj & 3) * 160 * 1280 + (sj >> 2) * 128, (w * 5 + j) * 1024);
    }
    g.template half0<0>(); g.template half1<0>();
  }
  auto ticks = [&](auto self, auto N, auto E) { if constexpr (N.v < E.v) { sd.template tick<N.v>(c, srow, unit0); self(self, IC<N.v + 1>{}, E); } };
  g.init_read(c);
  unsigned wflag = OOB, ja = 0, jb = 0, jc = 0, jo0 = OOB, jo1 = OOB;
  if (TYPE == 0) {
    ticks(ticks, IC<8 + 14>{}, IC<Side<NU>::NCH>{});
    const int step = mi >> 2, f = 4 * q + ((l >> 2) & 3);
    ja = f * 320 + w * 32 + (l & 3) * 8;
    jb = ja + 5120; jc = ja + 5376;
    ja += LWB(2) + 8192; jb += jb < 8192 ? LWB(2) + 8192 : LWS(2) - 8192; jc += jc < 8192 ? LWB(2) + 8192 : LWS(2) - 8192;
    PIN(jb); PIN(jc);
    const int dt = (mi & 3) * 10 + w, ct = dt / 5;
    jo0 = WS_W2T + ct * W2T_CT + step * 5120 + (dt - ct * 5) * 1024 + l * 16;
    jo1 = w < 2 ? WS_W2T + ((mi & 3) * 2 + 1) * W2T_CT + step * 5120 + (3 + w) * 1024 + l * 16 : OOB;
    wflag = (w == 0 && l < 2) ? WS_FLAGW + (((mi & 3) * 2 + l) * 8 + (step & 7)) * 128 + (step >> 3) * 8 : OOB;
    PIN(ja); PIN(jo0); PIN(jo1); PIN(wflag);
  }
  g.template half0<1>();
  asm volatile("s_waitcnt vmcnt(%6)" : "+v"(sd.xr[0]), "+v"(sd.xr[1]), "+v"(sd.xr[2]), "+v"(sd.xr[3]), "+v"(sd.xr[4]), "+v"(ep) : "n"((TYPE == 0 ? 13 : 15) - 2 * P6D) : "memory");
  asm volatile("" : "+v"(sd.pa), "+v"(sd.pb), "+v"(sd.pd));
  STAMP(1);
  c.T = __builtin_amdgcn_readfirstlane(ep) + 1u;
  c.T2 = (c.T * 0x9E3779B1u) ^ 0x5bd1e995u;
  c.tag = v2u{c.T, c.T2};
  PIN(c.tag);
  typename G1<TYPE>::Frags fa, fb;
  if (TYPE == 0) {
    ticks(ticks, IC<0>{}, IC<7>{});
    g.template half1<1>();
    ticks(ticks, IC<7>{}, IC<PRE>{});
    waitv<8 - P6D>();
    BAR();
    g.template rdall<0>(fa);
  } else {
    if (w < 2) {
      const float ar = bf2f(sd.pa), ai = bf2f(sd.pb), dt = rbf(fexp(bf2f(sd.pd))), e = fexp(dt * ar);
      float sn, cs;
      fsincos(dt * ai, sn, cs);
      cpx p = {e * cs, e * sn};
      if (w == 0) {
        const float inv = 2.f / (ar * ar + ai * ai);
        ldsw<f32x2>(L_COEF + l * 8, cmul(cpx{p.x - 1.f, p.y}, cpx{ar * inv, -ai * inv}));
      } else {
        ldsw<f32x2>(L_POW + l * 8, p);
        p = cmul(p, p); p = cmul(p, p);
#pragma unroll
        for (int k = 1; k < 6; ++k) { p = cmul(p, p); ldsw<f32x2>(L_POW + k * 512 + l * 8, p); }
      }
    }
    g.template half1<1>();
    waitv<12 - 2 * P6D>();
    BAR();
    const f32x4 k0 = ldsr<f32x4>(L_COEF + (16 * wt + 4 * q) * 8), k1 = ldsr<f32x4>(L_COEF + (16 * wt + 4 * q) * 8 + 16);
    const cpx kc[4] = {{k0.x, k0.y}, {k0.z, k0.w}, {k1.x, k1.y}, {k1.z, k1.w}};
    const int step = ustep(4 * (sj >> 2) + wt), ct = 2 * (sj & 3) + wn;
#pragma unroll
    for (int j = 0; j < 5; ++j) {
      const unsigned row = (wn * 5 + j) * 16 + i;
      const unsigned a = LX(2) + row * 128 + (((2 * wt + (q >> 1)) ^ ((row >> 1) & 7)) * 16) + (q & 1) * 8;
      const v2u cr = ldsr<v2u>(a), ci = ldsr<v2u>(a + 20480);
      const unsigned cr0 = cr.x, cr1 = cr.y, ci0 = ci.x, ci1 = ci.y;
      const cpx p0 = cmul(kc[0], cpx{bf2f(cr0), bf2f(ci0)}), p1 = cmul(kc[1], cpx{bfhi(cr0), bfhi(ci0)});
      const cpx p2 = cmul(kc[2], cpx{bf2f(cr1), bf2f(ci1)}), p3 = cmul(kc[3], cpx{bfhi(cr1), bfhi(ci1)});
      const v4u o = {pk2(p0.x, p1.x), pk2(p2.x, p3.x), pk2(-p0.y, -p1.y), pk2(-p2.y, -p3.y)};
      st128(c, o, l * 16 + j * 1024 + (WS_W2T + ct * W2T_CT + step * 5120));
    }
    wflag = l == 0 ? WS_FLAGW + (ct * 8 + (step & 7)) * 128 + (step >> 3) * 8 : OOB;
    SB0();
    waitv<11 - P6D>();
    BAR();
    g.template rdall<0>(fa);
  }
  STAMP(2); CSTAMP(0);
  SB0();
  v4u sg; v2u fw; unsigned pfd = 0;
  bf16x8 jv0, jv1;
  const v4u rwsv = mkrsrc(c.ws, WS_END);
  auto w2l = [&](auto CH, auto A) { glds16sc1<CH.v * 40960 + A.v * 1024, tile_base(CH.v, A.v)>(rwsv, c.vw2, A.v < 4 ? g.w4k : g.w1k); };
  auto side = [&](auto SUB, auto G) {
    constexpr int k = TYPE == 0 ? (G.v == 8 ? 0 : -1) : G.v - 1;
    constexpr int n = TYPE == 0 ? PRE + SUB.v : SUB.v * 5 + k;
    if constexpr (k >= 0 && n < (TYPE == 0 ? 22 : Side<NU>::NCH)) sd.template tick<n>(c, srow, unit0);
  };
  static_assert(TYPE == 0 ? (22 - PRE <= 19 && PRE + 10 >= 8) : Side<NU>::NCH <= 70, "side work schedule");
  constexpr int GA = TYPE == 0 ? 4 : 2, GB = TYPE == 0 ? 6 : 4;
  auto step = [&](auto SS) {
    constexpr int S = SS.v;
    LGKM0();
    g.template part<2 * S>(fa, fb, [&](auto G) {
      if constexpr (S == 0) {
        if constexpr (TYPE == 0) {
          if (G.v == 7) { jv0 = cat(trread(ja), trread(jb)); jv1 = cat(trread(ja + 256), trread(jc)); }
        }
        if (G.v == 0) g.template piece<0, 2>();
        if (G.v == 1) g.template piece<4, 2>();
        if (G.v == 2) g.template piece<1, 2>();
        if (G.v == 3) g.template piece<2, 2>();
        if (G.v == 4) g.template piece<3, 2>();
      } else if constexpr (S + 2 < 10) {
        if (G.v == 1) g.template piece<2, S + 2>();
        if (G.v == GA) g.template piece<5, S + 2>();
        if (G.v == GB) g.template piece<3, S + 2>();
      } else {
        if (G.v == 1) w2l(IC<S - 8>{}, IC<3>{});
        if (G.v == GB) w2l(IC<S - 8>{}, IC<4>{});
      }
      side(IC<2 * S>{}, G);
    });
    if constexpr (S < 9) {
      if (S == SCHK) asm volatile("s_waitcnt vmcnt(%2)" : "+v"(sg), "+v"(fw) : "n"(NP) : "memory");
      else if (S == 8) waitv<5>();
      else if (S == 0) waitv<TYPE == 0 ? 5 : 10>();
      else if (S == JS + 1 && TYPE == 0) waitv<NP + 1>();
      else waitv<NP>();
      LGKM0();
      asm volatile("s_barrier" ::: "memory");
    } else LGKM0();
    if (S == 0) { g.template piece<6, 2>(); g.template piece<5, 2>(); }
    if (S == JS && TYPE == 0) { st128(c, __builtin_bit_cast(v4u, jv0), jo0); if (w < 2) st128(c, __builtin_bit_cast(v4u, jv1), jo1); }
    if (S == (TYPE == 0 ? JS + 2 : 1)) st_flag(c, wflag);
    if (S == SCHK - 1) {
      const GAS char* sp = (const GAS char*)c.ws + WS_STAT + rb * 4096;
      const GAS char* fp = (const GAS char*)c.ws + WS_FLAGW + (c.ct * 8 + c.wc) * 128;
      asm volatile("global_load_dwordx4 %0, %2, %3 sc1\n\tglobal_load_dwordx2 %1, %4, %5 sc1"
                   : "=&v"(sg), "=&v"(fw) : "v"(wt * 1024 + l * 16), "s"(sp), "v"(i * 8), "s"(fp) : "memory");
    }
    if (S == SCHK) {
      const bool need = i < 12 && c.wc + 8 * i < NS2;
      SPIN_LOOP {
        if (__all((!need || (fw.x == c.T && fw.y == c.T2)) && sg.y == c.T && sg.w == c.T2)) break;
        __builtin_amdgcn_s_sleep(1);
        asm volatile("" ::: "memory");
        sg = ld128(c, WS_STAT + rb * 4096 + wt * 1024 + l * 16);
        fw = ld64(c, WS_FLAGW + (c.ct * 8 + c.wc) * 128 + i * 8);
      }
      ldsw<v2u>(L_ST + (wt * 64 + l) * 8, v2u{sg.x, sg.z});
    }
    SB0();
    g.template part<2 * S + 1>(fb, fa, [&](auto G) {
      if constexpr (false) {
      } else if constexpr (S + 3 < 10) {
        if (G.v == 0) g.template piece<6, S + 3>();
        if (G.v == (TYPE == 0 ? 2 : 1)) g.template piece<0, S + 3>();
        if (G.v == GA) g.template piece<4, S + 3>();
        if (G.v == GB) g.template piece<1, S + 3>();
      } else if constexpr (S < 9) {
        if (G.v == 1) w2l(IC<S - 7>{}, IC<0>{});
        if (G.v == GA) w2l(IC<S - 7>{}, IC<1>{});
        if (G.v == GB) w2l(IC<S - 7>{}, IC<2>{});
      } else {
        if (G.v == G1<TYPE>::NG - 1) {
          if (TYPE == 0) ldsw<float>(L_CS + ((wn * 4 + wt) * 16 + i) * 4, g.cs0.x);
          const unsigned ca = L_CSS + (TYPE == 0 ? w : (w >> 1) % 3) * 128 + i * 4;
          ldsw<float>(ca, g.cs1.x); ldsw<float>(ca + 64, g.cs2.x);
        }
      }
      side(IC<2 * S + 1>{}, G);
    });
  };
  step(IC<0>{}); step(IC<1>{}); step(IC<2>{}); step(IC<3>{}); step(IC<4>{}); step(IC<5>{}); step(IC<6>{});
  CSTAMP(1); STAMP(9);
  step(IC<7>{}); step(IC<8>{}); step(IC<9>{});
  asm volatile("" ::"v"(pfd));
  MFMA_DRAIN();
  BAR();
  SB0();
  STAMP(3);
  auto w2c2 = [&]() { w2l(IC<2>{}, IC<0>{}); w2l(IC<2>{}, IC<1>{}); w2l(IC<2>{}, IC<2>{}); w2l(IC<2>{}, IC<3>{}); w2l(IC<2>{}, IC<4>{});
    glds16<0, L_XT0>(g.rx, sd.v_x0, 0, g.w1k);
    glds16<0, L_XT1>(w == 2 ? mkrsrc(c.dsk, 1280) : g.rx, sd.v_x1, 0, w < 3 ? g.w1k : 65536);
  };
  if (ORD & 1) w2c2();
  __builtin_amdgcn_s_setprio(3);
  if (TYPE == 0) {
    f32x4 cs[4];
    f32x2 mu[4];
#pragma unroll
    for (int a = 0; a < 4; ++a) cs[a] = ldsr<f32x4>(sd.a_cs ^ (a * 64));
#pragma unroll
    for (int m = 0; m < 4; ++m) mu[m] = ldsr<f32x2>((m < 2 ? sd.a_mu : (sd.a_mu ^ 256)) + (m & 1) * 32);
    auto glu = [&](auto ODD) {
#pragma unroll
      for (int m = 0; m < 4; ++m) {
        const f32x4 mv = {-mu[m].x, -mu[m].x, -mu[m].x, -mu[m].x};
        const float r2 = mu[m].y * mu[m].y;
        const f32x4 h0 = __builtin_elementwise_fma(mv, cs[ODD.v], g.acc[ODD.v][m]) * __builtin_elementwise_fma(mv, cs[2 + ODD.v], g.acc[2 + ODD.v][m]) * r2;
        const f32x4 h1 = __builtin_elementwise_fma(mv, cs[1 - ODD.v], g.acc[1 - ODD.v][m]) * __builtin_elementwise_fma(mv, cs[3 - ODD.v], g.acc[3 - ODD.v][m]) * r2;
        st128(c, v4u{pk2(h0[0], h0[1]), pk2(h0[2], h0[3]), pk2(h1[0], h1[1]), pk2(h1[2], h1[3])}, (m < 2 ? sd.v_hg : (sd.v_hg ^ 2048)) + (m & 1) * 1024);
        asm volatile("" ::: "memory");
      }
    };
    if (w & 1) glu(IC<1>{}); else glu(IC<0>{});
    SB0();
  }
  cpx v[NU][8], eq[NU], tot[NU];
  {
    f32x4 st[4];
#pragma unroll
    for (int e = 0; e < 4; ++e) st[e] = ldsr<f32x4>(sd.a_st + e * 16);
#pragma unroll
    for (int u = 0; u < NU; ++u) {
      float csx = ldsr<float>(sd.a_css[u]), csy = ldsr<float>(sd.a_css[u] + 64);
      if (TYPE == 0) { csx = sumq(csx + ldsr<float>(sd.a_css[u] + 512)); csy = sumq(csy + ldsr<float>(sd.a_css[u] + 576)); }
#pragma unroll
      for (int j = 0; j < 8; ++j) {
        const float mu = (j & 1) ? st[j >> 1].z : st[j >> 1].x, rs = (j & 1) ? st[j >> 1].w : st[j >> 1].y;
        float a = fmaf(-mu, csx, g.accs[2 * u][j >> 2][j & 3]), b = fmaf(-mu, csy, g.accs[2 * u + 1][j >> 2][j & 3]);
        asm("" : "+v"(a), "+v"(b));
        v[u][j] = cpx{a * rs, b * rs};
        asm("" : "+v"(v[u][j]));
      }
      cpx h = v[u][0];
#pragma unroll
      for (int j = 1; j < 8; ++j) h = cfma(sd.lam[u], h, v[u][j]);
      float ax[4], ay[4];
      gatherq(h.x, ax); gatherq(h.y, ay);
      eq[u] = cfma(sd.u0[u], cpx{ax[0], ay[0]}, cfma(sd.u1[u], cpx{ax[1], ay[1]}, cpx{sd.u2 * ax[2], sd.u2 * ay[2]}));
      tot[u] = cfma(sd.L8[u], eq[u], h);
      ldsw<f32x2>(sd.a_aw[u], tot[u]);
    }
  }
  SB0();
  BAR();
  cpx pm[NU];
#pragma unroll
  for (int u = 0; u < NU; ++u) {
    const f32x4 t = ldsr<f32x4>(sd.a_ar[u]);
    pm[u] = cfma(sd.W0[u], cpx{t.x, t.y}, cmul(sd.W1[u], cpx{t.z, t.w}));
  }
  if (w == 7) {
#pragma unroll
    for (int u = 0; u < NU; ++u) {
      const cpx P = cfma(sd.L32[u], cpx{sumq(pm[u].x), sumq(pm[u].y)}, tot[u]);
      st128(c, v4u{__float_as_uint(P.x), c.T, __float_as_uint(P.y), c.T2}, sd.v_pub[u]);
    }
  }
  SB0();
  if (TYPE == 0) {
    if (w == 7) waitv<1>(); else waitv<0>();
    st_flag(c, sd.v_fg);
    STAMP(4);
  }
  asm volatile("s_barrier" ::: "memory");
  auto ldwr = [&](auto A, auto B) {
    if constexpr (A.v <= 0 && 0 < B.v) ldw<3>(c, c.wf[0]);
    if constexpr (A.v <= 1 && 1 < B.v) ldw<4>(c, c.wf[1]);
    if constexpr (A.v <= 2 && 2 < B.v) ldw<5>(c, c.wf[2]);
    if constexpr (A.v <= 3 && 3 < B.v) ldw<6>(c, c.wf[3]);
    SB0();
  };
  PRI(2);
  if (!(ORD & 1)) w2c2();
  ldwr(IC<0>{}, IC<NA>{});
  PRI(3);
  v4u lk0[NU], lk1[NU];
#pragma unroll
  for (int u = 0; u < NU; ++u) { lk0[u] = ld128(c, sd.v_lk0[u]); lk1[u] = ld128(c, sd.v_lk1[u]); }
  if (TYPE == 0) c.fl = ld64(c, c.vfl);
  PRI(1);
  ldwr(IC<NA>{}, IC<NA + NB>{});
  PRI(3);
  auto hwait = [&]() {
    if ((__ballot(c.fl.x == c.T && c.fl.y == c.T2) & HM) != HM) {
      SPIN_LOOP {
        asm volatile("" ::: "memory");
        c.fl = ld64(c, c.vfl);
        if ((__ballot(c.fl.x == c.T && c.fl.y == c.T2) & HM) == HM) break;
      }
    }
    __builtin_amdgcn_fence(__ATOMIC_ACQUIRE, "wavefront");
    PRI(1);
    ldh<0>(c, c.hf[0]); ldh<1>(c, c.hf[1]);
    PRI(3);
    SB0();
  };
  if (TYPE == 0) hwait();
  auto lkok = [&]() {
    bool ok = true;
#pragma unroll
    for (int u = 0; u < NU; ++u)
      ok = ok && (sd.v_lk0[u] == OOB || (lk0[u].y == c.T && lk0[u].w == c.T2)) && (sd.v_lk1[u] == OOB || (lk1[u].y == c.T && lk1[u].w == c.T2));
    return __all(ok);
  };
  if (!lkok()) {
    SPIN_LOOP {
      asm volatile("" ::: "memory");
#pragma unroll
      for (int u = 0; u < NU; ++u) { lk0[u] = ld128(c, sd.v_lk0[u]); lk1[u] = ld128(c, sd.v_lk1[u]); }
      if (lkok()) break;
    }
  }
  SB0();
  STAMP(5);
#pragma unroll
  for (int u = 0; u < NU; ++u) {
    const unsigned x0 = lk0[u].x, y0 = lk0[u].z, x1 = lk1[u].x, y1 = lk1[u].z;
    const cpx cy = cfma(sd.wk1[u], cpx{__uint_as_float(x1), __uint_as_float(y1)}, cfma(sd.wk0[u], cpx{__uint_as_float(x0), __uint_as_float(y0)}, pm[u]));
    cpx h = cfma(sd.f1[u], cpx{sumq(cy.x), sumq(cy.y)}, eq[u]);
#pragma unroll
    for (int j = 0; j < 8; ++j) { h = cfma(sd.lam[u], h, v[u][j]); v[u][j] = h; }
    ldsw<v4u>(sd.a_tw, v4u{pk2(v[u][0].x, v[u][1].x), pk2(v[u][2].x, v[u][3].x), pk2(v[u][4].x, v[u][5].x), pk2(v[u][6].x, v[u][7].x)});
    ldsw<v4u>(sd.a_tw + 1024, v4u{pk2(v[u][0].y, v[u][1].y), pk2(v[u][2].y, v[u][3].y), pk2(v[u][4].y, v[u][5].y), pk2(v[u][6].y, v[u][7].y)});
    asm volatile("s_waitcnt lgkmcnt(0)" ::: "memory");
#pragma unroll
    for (int mm = 0; mm < 2; ++mm) st128(c, __builtin_bit_cast(v4u, cat(trread(sd.a_tr + mm * 8), trread(sd.a_tr + mm * 8 + 1024))), sd.v_hs[u] + mm * 1024);
    if (u + 1 < NU) asm volatile("s_waitcnt lgkmcnt(0)" ::: "memory");
  }
  SB0();
  c.vfs = sd.v_fs;
  if (TYPE == 1) c.fl = ld64(c, c.vfl);
  PRI(0);
  ldwr(IC<NA + NB>{}, IC<4>{});
  if (TYPE == 1) {
    waitv<(4 - NA - NB) * 5 + 1>();
    PRI(3);
    st_flag(c, sd.v_fs);
    c.vfs = OOB;
    STAMP(4);
    hwait();
    PRI(0);
  }
  if (TYPE == 1) {
    if (si == 39) {
      SPIN_LOOP {
        bool ok = true;
        asm volatile("" ::: "memory");
#pragma unroll
        for (int k = 0; k < 4; ++k) {
          const v4u t = ld128(c, WS_STAT + (k * 512 + w * 64 + l) * 16);
          ok = ok && t.y == c.T && t.w == c.T2;
        }
        if (__all(ok)) break;
        __builtin_amdgcn_s_sleep(1);
      }
    }
  }
  c.bump = TYPE == 1 && si == 39;
  SB0();
}

DI void phase2(Ctx& c, const u16* dsk, float* out) {
  const int w = c.w, rt = c.rt, ct = c.ct;
  v4u wf[12][5], hf[12][4];
  f32x4 acc[4][5];
  v2u fl = c.fl;
  STAMP(6); CSTAMP(2);
  f32x4 base[3];
  unsigned oo;
  auto body = [&](auto JJ) {
    constexpr int j = JJ.v;
    if constexpr (j < 3) {
      int l = threadIdx.x & 63; PIN(l);
      const unsigned a4 = tile_base(j, 0) + w * 4096 + l * 16;
#pragma unroll
      for (int a = 0; a < 4; ++a) wf[j][a] = ldsr<v4u>(a4 + a * 1024);
      wf[j][4] = ldsr<v4u>(tile_base(j, 4) + w * 1024 + l * 16);
    } else if constexpr (j < 3 + NWR) {
#pragma unroll
      for (int a = 0; a < 5; ++a) wf[j][a] = c.wf[j - 3][a];
    }
    if constexpr (j < 2) {
#pragma unroll
      for (int m = 0; m < 4; ++m) hf[j][m] = c.hf[j][m];
    }
    if constexpr (j == 4 || j == JC) {
      const unsigned long long msk = j == 4 ? ((1ull << (2 * JC + 4)) - (1ull << 12)) : ((c.wc >= 6 ? (1ull << 22) : (1ull << 24)) - (1ull << (2 * JC + 4)));
      if (__builtin_expect((__ballot(fl.x == c.T && fl.y == c.T2) & msk) != msk, 0)) {
        SPIN_LOOP {
          asm volatile("" ::: "memory");
          const v2u f = ld64(c, c.vfl);
          if ((__ballot(f.x == c.T && f.y == c.T2) & msk) == msk) break;
        }
      }
      __builtin_amdgcn_fence(__ATOMIC_ACQUIRE, "wavefront");
    }
    if constexpr (j == 2 || j == JC - 2 + (JC == 6)) fl = ld64(c, c.vfl);
    if constexpr (j + 2 < 12) ldh<j + 2>(c, hf[j + 2]);
    if constexpr (j + 3 < 12 && j + 3 >= 3 + NWR) ldw<j + 3>(c, wf[j + 3]);
    if constexpr (j == JE) {
      int l = threadIdx.x & 63; PIN(l);
      const int i = l & 15, q = l >> 4;
      const int tl = ((w >> 1) & 1) * 32 + 8 * (i >> 2) + 4 * (w & 1) + (i & 3);
      const f32x2 st = ldsr<f32x2>(L_ST + (rt * 64 + tl) * 8);
      v2u xv[3], dv[3];
#pragma unroll
      for (int k = 0; k < 3; ++k) {
        const int dl = min((w >> 2) + 2 * k, 4) * 16 + 4 * q;
        const unsigned xo = tl * 160 + dl * 2;
        xv[k] = ldsr<v2u>(xo < 8192 ? L_XT0 + xo : L_XT1 - 8192 + xo);
        dv[k] = ldsr<v2u>(L_XT1 + 2048 + dl * 2);
      }
      oo = (c.rb * 256 + rt * 64 + tl) * D_ + ct * 80 + (w >> 2) * 16 + 4 * q;
      PIN(oo);
#pragma unroll
      for (int k = 0; k < 3; ++k) {
        const unsigned x0 = xv[k].x, x1 = xv[k].y, d0 = dv[k].x, d1 = dv[k].y;
        const float xf[4] = {bf2f(x0), bfhi(x0), bf2f(x1), bfhi(x1)}, df[4] = {bf2f(d0), bfhi(d0), bf2f(d1), bfhi(d1)};
#pragma unroll
        for (int e = 0; e < 4; ++e) {
          float xn = rbf(rbf(xf[e] - st.x) * st.y);
          asm("" : "+v"(xn));
          base[k][e] = xf[e] + rbf(xn * df[e]);
        }
        PIN(base[k]);
      }
    }
    SB0();
#pragma unroll
    for (int a = 0; a < 5; ++a)
#pragma unroll
      for (int m = 0; m < 4; ++m) mf<j == 0>(acc[m][a], __builtin_bit_cast(bf16x8, wf[j][a]), __builtin_bit_cast(bf16x8, hf[j][m]));
    if constexpr (j == JF) {
      waitv<(4 - NA - NB) * 5 + 4 * (JF + 1) + (JF >= 2)>();
      PRI(3);
      st_flag(c, c.vfs);
      SB0();
      __builtin_amdgcn_s_setprio(0);
    }
    if constexpr (j == JB) asm volatile("s_barrier" ::: "memory");
    if constexpr (j == JE) {
      BAR();
      __builtin_amdgcn_raw_buffer_store_b32(c.T, c.rws, (c.bump && threadIdx.x == 0) ? WS_EPOCH : OOB, 0, 16);
    }
    if (j == 1) STAMP(10);
    if (j == 3) STAMP(11);
    if (j == 4) STAMP(12);
    if (j == 7) STAMP(13);
    SB0();
  };
  body(IC<0>{}); body(IC<1>{}); body(IC<2>{}); body(IC<3>{}); body(IC<4>{}); body(IC<5>{});
  body(IC<6>{}); body(IC<7>{}); body(IC<8>{}); body(IC<9>{}); body(IC<10>{});
  int l = threadIdx.x & 63; PIN(l);
  unsigned bw[3], br[3];
#pragma unroll
  for (int k = 0; k < 3; ++k) { bw[k] = w * 1024 + l * 16 + k * 65536; br[k] = w * 8192 + l * 16 + k * 65536; PIN(bw[k]); PIN(br[k]); }
  body(IC<11>{});
  MFMA_DRAIN();
  SB0();
  STAMP(7);
#pragma unroll
  for (int a = 0; a < 5; ++a)
#pragma unroll
    for (int m = 0; m < 4; ++m) ldsw<f32x4>(bw[(a * 4 + m) >> 3] + ((a * 4 + m) & 7) * 8192, acc[m][a]);
  BAR();
  f32x4 p[2][8];
#pragma unroll
  for (int k = 0; k < 2; ++k)
#pragma unroll
    for (int v = 0; v < 8; ++v) p[k][v] = ldsr<f32x4>(br[k] + v * 1024);
  SB0();
  if (w < 4) {
    f32x4 p2[8];
#pragma unroll
    for (int v = 0; v < 8; ++v) p2[v] = ldsr<f32x4>(br[2] + v * 1024);
    SB0();
#pragma unroll
    for (int k = 0; k < 2; ++k) *(f32x4*)(out + oo + 32 * k) = ((p[k][0] + p[k][1]) + (p[k][2] + p[k][3])) + ((p[k][4] + p[k][5]) + (p[k][6] + p[k][7])) + base[k];
    *(f32x4*)(out + oo + 64) = ((p2[0] + p2[1]) + (p2[2] + p2[3])) + ((p2[4] + p2[5]) + (p2[6] + p2[7])) + base[2];
  } else {
#pragma unroll
    for (int k = 0; k < 2; ++k) *(f32x4*)(out + oo + 32 * k) = ((p[k][0] + p[k][1]) + (p[k][2] + p[k][3])) + ((p[k][4] + p[k][5]) + (p[k][6] + p[k][7])) + base[k];
  }
}

__global__ void __launch_bounds__(512, 2)
fused_kernel(const u16* x, const u16* lre, const u16* lim, const u16* bre, const u16* bim, const u16* cre, const u16* cim, const u16* ldt,
             const u16* dsk, const u16* wg, const u16* wu, const u16* wd, float* out, char* ws) {
  Ctx c;
  c.dsk = dsk; c.x = x; c.bre = bre; c.bim = bim; c.cre = cre; c.cim = cim; c.wg = wg; c.wu = wu; c.wd = wd; c.ws = ws;
  c.lane = threadIdx.x & 63;
  c.w = __builtin_amdgcn_readfirstlane(threadIdx.x >> 6);
  const int xc = blockIdx.x & 7, ix = blockIdx.x >> 3;
  c.rb = (xc >> 1) * 2 + (ix >> 4); c.cg = (xc & 1) * 16 + (ix & 15);
  c.rt = c.cg >> 3; c.ct = c.cg & 7;
  c.shr = ix >> 3;
  c.wc = (c.w + (ROTC ? c.ct + 2 * c.shr : 0)) & 7;
  c.rws = __builtin_amdgcn_make_buffer_rsrc(ws, 0, WS_END, 0x00020000);
  STAMP(0);
  if (c.cg < 27) phase1<0>(c, lre, lim, ldt);
  else phase1<1>(c, lre, lim, ldt);
  phase2(c, dsk, out);
}

extern "C" void kernel_launch(void* const* d_in, const int* in_sizes, int n_in, void* d_out, int out_size, void* d_ws, size_t ws_size,
                              hipStream_t stream) {
  (void)in_sizes; (void)n_in; (void)out_size; (void)ws_size;
  (void)hipFuncSetAttribute((const void*)fused_kernel, hipFuncAttributeMaxDynamicSharedMemorySize, LDS_BYTES);
  fused_kernel<<<256, 512, LDS_BYTES, stream>>>((const u16*)d_in[0], (const u16*)d_in[1], (const u16*)d_in[2], (const u16*)d_in[3], (const u16*)d_in[4],
                                                (const u16*)d_in[5], (const u16*)d_in[6], (const u16*)d_in[7], (const u16*)d_in[8], (const u16*)d_in[10],
                                                (const u16*)d_in[11], (const u16*)d_in[12], (float*)d_out, (char*)d_ws);
}
```

```cpp
#include <hip/hip_runtime.h>
#include <stdint.h>

#define GAS __attribute__((address_space(1)))
#define LAS __attribute__((address_space(3)))
#define DI __device__ __forceinline__
typedef unsigned short u16;
typedef short bf16x8 __attribute__((ext_vector_type(8)));
typedef short s16x4 __attribute__((ext_vector_type(4)));
typedef __bf16 bf2 __attribute__((ext_vector_type(2)));
typedef float f32x4 __attribute__((ext_vector_type(4)));
typedef float f32x2 __attribute__((ext_vector_type(2)));
typedef unsigned v4u __attribute__((ext_vector_type(4)));
typedef unsigned v2u __attribute__((ext_vector_type(2)));

#define XMAP 2
#define SCHK 7
#define L2PF 0
#define PRE 12
#define ROTC 0
#define ORD 0
#define UO 1
#define JC 6
#define JE 8
#define L2P 0
#define NSHR (XMAP == 1 ? 8 : 4)
#define JF 0
#define P6BR
#define P6D 1
#define JS 2
#define IL 2
#define SA 2
#define CPF 50000
#define CPS 8
#define LP0 7
#define LP1 9
#define LP2 10
#define WA 3
#define FP 1
#define HF 0
#define HM (HF ? 0xfull : 0xfffull)
#define NA 2
#define NB 2
#define PRIO 2
#define JB 1
#define NWR 4
#define PH 2
#define NWP 7
#define PWD 3

constexpr int L_ = 2048, D_ = 640, F_ = 1728;
constexpr int ustep(int u) { return UO ? (u >= 27 ? 27 + u : 67 + u) : 54 + u; }
constexpr int NS2 = 94;
constexpr unsigned WS_EPOCH = 0;
constexpr unsigned WS_STAT = 4096;
constexpr unsigned WS_AGG = WS_STAT + 2048 * 16;
constexpr unsigned WS_FLAGH = WS_AGG + 8 * 640 * 16;
constexpr unsigned WS_FLAGW = WS_FLAGH + 32 * 8 * 256;
constexpr unsigned WS_CTL_END = WS_FLAGW + 8 * 8 * 128;
constexpr unsigned WS_W2T = 262144;
constexpr unsigned W2T_CT = NS2 * 5 * 1024;
constexpr unsigned WS_H = WS_W2T + 8 * W2T_CT;
constexpr unsigned H_RT = NS2 * 4 * 1024;
constexpr unsigned WS_END = WS_H + 32 * H_RT;
constexpr unsigned OOB = 0x70000000u;
static_assert(WS_CTL_END <= WS_W2T, "ws map");
constexpr unsigned LX(int k) { return k * 32768; }
constexpr unsigned LWB(int k) { return 98304 + ((k + 1) % 3) * 16384; }
constexpr unsigned LWS(int k) { return 147456 + k * 4096; }
constexpr unsigned L_ST = 159744;
constexpr unsigned L_CS = L_ST + 2048;
constexpr unsigned L_CSS = L_CS + 512;
constexpr unsigned L_COEF = L_CSS + 1024;
constexpr unsigned L_POW = LWS(0);
constexpr unsigned L_AGG = LWS(2);
constexpr unsigned L_DUMMY = LWS(2) + 3072;
constexpr unsigned L_XT0 = LWB(1) + 8192;
constexpr unsigned L_XT1 = LWS(1);
constexpr unsigned LDS_BYTES = 163840;
static_assert(L_COEF + 512 <= LDS_BYTES && LWB(2) == LX(2) + 32768, "lds map");
constexpr unsigned tile_base(int ch, int j) {
  return ch < 3 ? (j < 4 ? LX((ch + 1) % 3) + j * 1024 : LWB((ch + 1) % 3)) : (j < 3 ? LWB(j) + 8192 : LWS(0) + (j - 3) * 1024);
}
constexpr unsigned tile_wstride(int ch, int j) { return ch < 3 ? (j < 4 ? 4096 : 1024) : (j < 3 ? 1024 : 2048); }
constexpr int SPIN_CAP = 1 << 15;

DI float bf2f(unsigned v) { return __uint_as_float(v << 16); }
DI float bfhi(unsigned v) { return __uint_as_float(v & 0xffff0000u); }
DI unsigned pk2(float a, float b) { bf2 v = {(__bf16)a, (__bf16)b}; return __builtin_bit_cast(unsigned, v); }
DI float rbf(float a) { return bf2f(pk2(a, 0.f) & 0xffffu); }
DI void mfa(f32x4& c, bf16x8 a, bf16x8 b) { asm volatile("v_mfma_f32_16x16x32_bf16 %0, %1, %2, %0" : "+v"(c) : "v"(a), "v"(b)); }
DI void mfz(f32x4& c, bf16x8 a, bf16x8 b) { asm volatile("v_mfma_f32_16x16x32_bf16 %0, %1, %2, 0" : "=&v"(c) : "v"(a), "v"(b)); }
template <bool Z> DI void mf(f32x4& c, bf16x8 a, bf16x8 b) { if (Z) mfz(c, a, b); else mfa(c, a, b); }
#define MFMA_DRAIN() asm volatile("s_nop 7\n\ts_nop 1" ::: "memory")
DI v4u mkrsrc(const void* p, unsigned bytes) {
  uint64_t a = (uint64_t)p;
  v4u r; r.x = (unsigned)a; r.y = (unsigned)(a >> 32) & 0xffffu; r.z = bytes; r.w = 0x00020000u;
  return r;
}
template <unsigned SI, unsigned LI> DI void glds16(v4u rs, unsigned voff, unsigned sb, unsigned lb) {
  unsigned t;
  asm volatile("s_add_u32 m0, %4, %6\n\ts_add_u32 %0, %3, %5\n\tbuffer_load_dwordx4 %1, %2, %0 offen lds"
               : "=&s"(t) : "v"(voff), "s"(rs), "s"(sb), "s"(lb), "i"(SI), "i"(LI) : "memory", "scc");
}
template <unsigned SI, unsigned LI> DI void glds16sc1(v4u rs, unsigned voff, unsigned lb) {
  unsigned t;
  asm volatile("s_add_u32 m0, %3, %5\n\ts_mov_b32 %0, %4\n\tbuffer_load_dwordx4 %1, %2, %0 offen sc1 lds"
               : "=&s"(t) : "v"(voff), "s"(rs), "s"(lb), "i"(SI), "i"(LI) : "memory", "scc");
}
template <class T> DI T ldsr(unsigned a) { return *(LAS T*)(uintptr_t)a; }
template <class T> DI void ldsw(unsigned a, T v) { *(LAS T*)(uintptr_t)a = v; }
DI s16x4 trread(unsigned a) { return __builtin_amdgcn_ds_read_tr16_b64_v4i16((LAS s16x4*)(uintptr_t)a); }
DI bf16x8 cat(s16x4 lo, s16x4 hi) { return __builtin_shufflevector(lo, hi, 0, 1, 2, 3, 4, 5, 6, 7); }
template <int N> DI void waitv() { asm volatile("s_waitcnt vmcnt(%0)" ::"n"(N) : "memory"); }
#define BAR() asm volatile("s_waitcnt lgkmcnt(0)\n\ts_barrier" ::: "memory")
#define LGKM0() __builtin_amdgcn_s_waitcnt(0xc07f)
#define PRI(n) do { if (PRIO == 2) { SB0(); __builtin_amdgcn_s_setprio(n); SB0(); } } while (0)
#define SB0() __builtin_amdgcn_sched_barrier(0)
#define PIN(x) asm volatile("" : "+v"(x))
#define SLPN 0
#define SLP() do { if (SLPN) __builtin_amdgcn_s_sleep(SLPN); } while (0)
#define SPIN_LOOP _Pragma("nounroll") for (int sp = 0; sp < SPIN_CAP; ++sp)
template <int N> DI float ror(float v) { return __builtin_bit_cast(float, __builtin_amdgcn_update_dpp(0, __builtin_bit_cast(int, v), 0x120 + N, 0xf, 0xf, false)); }
DI void sw16(float v, float& e, float& o) { unsigned u = __float_as_uint(v); auto r = __builtin_amdgcn_permlane16_swap(u, u, false, false); e = __uint_as_float(r[0]); o = __uint_as_float(r[1]); }
DI void sw32(float v, float& lo, float& hi) { unsigned u = __float_as_uint(v); auto r = __builtin_amdgcn_permlane32_swap(u, u, false, false); lo = __uint_as_float(r[0]); hi = __uint_as_float(r[1]); }
DI float sumq(float v) { float a, b; sw16(v, a, b); v = a + b; sw32(v, a, b); return a + b; }
DI float sumr(float v) { v += ror<8>(v); v += ror<4>(v); v += ror<2>(v); v += ror<1>(v); return v; }
DI void gatherq(float v, float (&a)[4]) { float e, o; sw16(v, e, o); sw32(e, a[0], a[2]); sw32(o, a[1], a[3]); }
typedef f32x2 cpx;
DI cpx cfma(cpx a, cpx b, cpx c) {
  cpx r;
  asm("v_pk_fma_f32 %0, %1, %2, %3 op_sel:[1,1,0] op_sel_hi:[1,0,1] neg_lo:[1,0,0]\n\tv_pk_fma_f32 %0, %1, %2, %0 op_sel_hi:[0,1,1]" : "=&v"(r) : "v"(a), "v"(b), "v"(c));
  return r;
}
DI cpx cmul(cpx a, cpx b) {
  cpx r;
  asm("v_pk_mul_f32 %0, %1, %2 op_sel:[1,1] op_sel_hi:[1,0] neg_lo:[1,0]\n\tv_pk_fma_f32 %0, %1, %2, %0 op_sel_hi:[0,1,1]" : "=&v"(r) : "v"(a), "v"(b));
  return r;
}
DI cpx csel(bool p, cpx a) { return cpx{p ? a.x : 1.f, p ? a.y : 0.f}; }
DI cpx czero(bool p, cpx a) { return cpx{p ? 0.f : a.x, p ? 0.f : a.y}; }
template <int V> struct IC { static constexpr int v = V; };

#define STAMP(k) do {} while (0)
#define CSTAMP(k) do {} while (0)
struct Ctx {
  const u16 *dsk, *x, *bre, *bim, *cre, *cim, *wg, *wu, *wd;
  char* ws;
  __amdgpu_buffer_rsrc_t rws;
  v2u tag;
  unsigned T, T2;
  int lane, w, wc, rb, cg, rt, ct, shr;
  f32x4 acc0[5];
  unsigned vw2, vh2, vfl;
  v4u wf[NWR][5], hf[2][4];
  v2u fl;
  unsigned vfs;
  bool bump;
  unsigned long long pc;
};
DI v4u ld128(const Ctx& c, unsigned vo, unsigned so = 0) { return __builtin_amdgcn_raw_buffer_load_b128(c.rws, vo, so, 16); }
DI v2u ld64(const Ctx& c, unsigned vo) { return __builtin_amdgcn_raw_buffer_load_b64(c.rws, vo, 0, 16); }
DI void st128(const Ctx& c, v4u v, unsigned vo) { __builtin_amdgcn_raw_buffer_store_b128(v, c.rws, vo, 0, 16); }
DI void st_flag(const Ctx& c, unsigned vo) { __builtin_amdgcn_raw_buffer_store_b64(c.tag, c.rws, vo, 0, 16); }

DI float fexp(float x) {
  const float t = x * 1.44269502162933349609375f;
  const float r = fmaf(x, 1.925963033500011e-8f, fmaf(x, 1.44269502162933349609375f, -t));
  const float e = __builtin_amdgcn_exp2f(t);
  return fmaf(e, r * 0.693147182464599609375f, e);
}
DI void fsincos(float z, float& sn, float& cs) {
  const float k = rintf(z * 0.63661977236758134f);
  float r = fmaf(k, -1.5707963705062866f, z);
  r = fmaf(k, 4.3711388286737929e-08f, r);
  r = fmaf(k, 1.7151245100058819e-15f, r);
  const float r2 = r * r;
  const float sp = fmaf(r * r2, fmaf(r2, fmaf(r2, -1.9515295891e-4f, 8.3321608736e-3f), -1.6666654611e-1f), r);
  const float cp = fmaf(r2 * r2, fmaf(r2, fmaf(r2, 2.443315711809948e-5f, -1.388731625493765e-3f), 4.166664568298827e-2f), fmaf(r2, -0.5f, 1.f));
  const int n = (int)k;
  sn = (n & 1) ? cp : sp; cs = (n & 1) ? sp : cp;
  if (n & 2) sn = -sn;
  if ((n + 1) & 2) cs = -cs;
}
constexpr int LP(int ch) { return ch == 0 ? LP0 : (ch == 1 ? LP1 : LP2); }
constexpr int lds_chunk(int j) { return j == LP0 ? 0 : (j == LP1 ? 1 : (j == LP2 ? 2 : -1)); }
template <int J> DI void ldw(const Ctx& c, v4u (&d)[5]) {
  const unsigned vo = J == 11 ? (c.wc < 6 ? c.vw2 : OOB) : c.vw2;
#pragma unroll
  for (int a = 0; a < 4; ++a) d[a] = ld128(c, vo + a * 1024, J * 40960);
  d[4] = ld128(c, vo, J * 40960 + 4096);
}
template <int J> DI void ldh(const Ctx& c, v4u (&d)[4]) {
  const unsigned vo = J == 11 ? (c.wc < 6 ? c.vh2 : OOB) : c.vh2;
#pragma unroll
  for (int m = 0; m < 4; ++m) d[m] = ld128(c, vo ^ (m * 1024), J * 32768);
}

template <int TYPE> struct G1 {
  static constexpr int NU = TYPE == 0 ? 1 : 3;
  static constexpr int NG = TYPE == 0 ? 10 : 6;
  static constexpr int NR = TYPE == 0 ? 14 : 8;
  static constexpr int NP = (TYPE == 0 ? 7 : 6) - P6D;
  struct Frags { bf16x8 x[TYPE == 0 ? 4 : 2]; s16x4 wl[4], wh[4]; bf16x8 s[2 * NU]; };
  v4u rx, rw0, rw1, rs;
  unsigned vx0, vx1, vw, vs;
  unsigned sx, sw, ss, ss2;
  unsigned ax[2][2][2], aw[4], as[NU][2];
  f32x4 acc[4][4];
  f32x4 accs[2 * NU][2];
  f32x4 cs0, cs1, cs2;
  bf16x8 ones;
  int w;
  unsigned w1k, w4k, w1k6, vs6;

  DI void init_stage(const Ctx& c, int unit0) {
    const int l = c.lane;
    w = c.w; w1k = w * 1024; w4k = w * 4096;
    rx = mkrsrc(c.x, L_ * D_ * 2);
    const unsigned ch = (l & 7) ^ ((l >> 3) & 3);
    vx0 = (l >> 3) * 1280 + ch * 16;
    vx1 = (l >> 3) * 1280 + (ch ^ 4) * 16;
    sx = (c.rb * 256 + w * 32) * 1280;
    rs = mkrsrc((w & 2) ? c.bim : c.bre, 640 * 640 * 2);
    vs = (l >> 3) * 1280 + (((l & 7) ^ (l >> 3)) * 16);
    vs6 = w < 4 ? vs : OOB; w1k6 = w < 4 ? w1k : 65536;
    if (TYPE == 0) {
      rw0 = mkrsrc(c.wg, D_ * F_ * 2); rw1 = mkrsrc(c.wu, D_ * F_ * 2);
      const unsigned key2 = ((l >> 4) & 1) | ((w & 1) << 1);
      vw = (l >> 3) * 3456 + (((l & 7) ^ (key2 << 1)) * 16);
      sw = w * 8 * 3456 + c.cg * 128;
      ss2 = (unit0 * 16 + (w & 1) * 8) * 1280;
    } else {
      ss = (min(unit0 + (w >> 2), 39) * 16 + (w & 1) * 8) * 1280;
      ss2 = (min(unit0 + 2, 39) * 16 + (w & 1) * 8) * 1280;
    }
  }
  DI void init_read(const Ctx& c) {
    const int l = c.lane, i = l & 15, q = l >> 4;
    const int wt = w & 3, wn = w >> 2;
#pragma unroll
    for (int kk = 0; kk < 2; ++kk) {
      const unsigned ch = (((kk * 4 + q) ^ (i & 7)) * 16);
#pragma unroll
      for (int pr = 0; pr < (TYPE == 0 ? 2 : 1); ++pr) {
        ax[0][pr][kk] = (wt * 64 + (pr ^ wn) * 32 + 8 * (i >> 2) + (i & 3)) * 128 + ch;
        ax[1][pr][kk] = ax[0][pr][kk] + 65536;
        PIN(ax[0][pr][kk]); PIN(ax[1][pr][kk]);
      }
#pragma unroll
      for (int u = 0; u < NU; ++u) {
        as[u][kk] = (TYPE == 0 ? LWS(0) : LWB(2) + ((u + (w >> 1)) % 3) * 4096) + i * 128 + ch;
        PIN(as[u][kk]);
      }
    }
    if (TYPE == 0) {
      const int r = (l >> 2) & 3, cc = l & 3;
      const unsigned key2 = ((r >> 1) & 1) | ((q & 1) << 1);
#pragma unroll
      for (int a = 0; a < 4; ++a) {
        const int act = a ^ wt;
        aw[a] = LWB(2) + (act >> 1) * 8192 + (8 * q + r) * 128 + (((2 * wn + (act & 1)) ^ key2) * 32) + cc * 8;
        PIN(aw[a]);
      }
      cs1 = f32x4{0.f, 0.f, 0.f, 0.f}; cs2 = f32x4{0.f, 0.f, 0.f, 0.f};
      PIN(cs1); PIN(cs2);
    }
    ones = bf16x8{0x3f80, 0x3f80, 0x3f80, 0x3f80, 0x3f80, 0x3f80, 0x3f80, 0x3f80};
    PIN(ones);
  }
  template <int P, int S> DI void piece() {
    constexpr int K = S % 3;
    if constexpr (P < 4) glds16<S * 128 + P * 10240, LX(K) + P * 1024>(rx, (P & 1) ? vx1 : vx0, sx, w4k);
    else if constexpr (P < 6) {
      if constexpr (TYPE == 0) glds16<S * (64 * 3456), LWB(K) + (P - 4) * 8192>(P == 4 ? rw0 : rw1, vw, sw, w1k);
      else if constexpr (P == 4) glds16<S * 128, LWB(K)>(rs, vs, ss, w1k);
    } else {
      if (w < 4)
      glds16<S * 128, (TYPE == 0 ? LWS(K) : LWB(K) + 8192)>(rs, vs6, ss2, w1k6);
    }
  }
  template <int S> DI void half0() { piece<6, S>(); piece<0, S>(); piece<4, S>(); piece<1, S>(); }
  template <int S> DI void half1() { piece<2, S>(); piece<5, S>(); piece<3, S>(); }

  template <int K, int KK, int R> DI void rd(Frags& f) {
    constexpr unsigned ow = LWB(K) - LWB(2), ox = K == 1 ? 32768 : 0;
    if constexpr (TYPE == 0) {
      if (R == 0) f.wl[0] = trread(aw[0] + (ow + KK * 4096));
      else if (R == 1) f.wh[0] = trread(aw[0] + (ow + KK * 4096 + 512));
      else if (R < 6) f.x[(R - 2) & 3] = ldsr<bf16x8>(ax[K == 2][((R - 2) >> 1) & 1][KK] + (ox + ((R - 2) & 1) * 512));
      else if (R < 12) {
        constexpr int a = ((R - 4) >> 1) & 3;
        if (R & 1) f.wh[a] = trread(aw[a] + (ow + KK * 4096 + 512)); else f.wl[a] = trread(aw[a] + (ow + KK * 4096));
      } else f.s[R & 1] = ldsr<bf16x8>(as[0][KK] + (K * 4096 + (R & 1) * 2048));
    } else {
      if (R == 1 || R == 2) f.x[(R - 1) & 1] = ldsr<bf16x8>(ax[K == 2][0][KK] + (ox + ((R - 1) & 1) * 512));
      else { constexpr int a = R == 0 ? 0 : R - 2; f.s[a] = ldsr<bf16x8>(as[a >> 1][KK] + (ow + (a & 1) * 2048)); }
    }
  }
  template <bool Z, int M> DI void mm(const Frags& f) {
    if constexpr (TYPE == 0 && M < 16) mf<Z>(acc[M >> 2][M & 3], cat(f.wl[M >> 2], f.wh[M >> 2]), f.x[M & 3]);
    else { constexpr int a = (M - (TYPE == 0 ? 16 : 0)) >> 1; mf<Z>(accs[a][M & 1], f.x[M & 1], f.s[a]); }
  }
  template <int SUB, int G, class H> DI void group(const Frags& cur, Frags& nxt, H& hook) {
    constexpr int N = SUB + 1, K = (N >> 1) % 3, KK = N & 1;
    constexpr bool RD = N < 20;
    mm<SUB == 0, 2 * G>(cur);
    if constexpr (RD && 2 * G < NR) rd<K, KK, 2 * G>(nxt);
    mm<SUB == 0, 2 * G + 1>(cur);
    if constexpr (RD && 2 * G + 1 < NR) rd<K, KK, 2 * G + 1>(nxt);
    if constexpr (TYPE == 0) {
      if (G == 0) mf<SUB == 0>(cs0, ones, cat(cur.wl[0], cur.wh[0]));
      if (G == 2) { if (__builtin_expect(w == (SUB & 7), 0)) { mfa(cs1, ones, cur.s[0]); mfa(cs2, ones, cur.s[1]); } }
    } else {
      if (G == 0) { mf<SUB == 0>(cs1, ones, cur.s[0]); mf<SUB == 0>(cs2, ones, cur.s[1]); }
    }
    hook(IC<G>{});
    SB0();
    if constexpr (G + 1 < NG) group<SUB, G + 1>(cur, nxt, hook);
  }
  template <int SUB, class H> DI void part(const Frags& cur, Frags& nxt, H hook) { group<SUB, 0>(cur, nxt, hook); }
  template <int R> DI void rdall(Frags& f) { rd<0, 0, R>(f); if constexpr (R + 1 < NR) rdall<R + 1>(f); }
};

template <int NU> struct Side {
  unsigned xr[5], pa, pb, pd;
  float v[10], s, dt, e, sn, cs;
  cpx lam[NU], L8[NU], L32[NU], u0[NU], u1[NU], f1[NU], W0[NU], W1[NU], wk0[NU], wk1[NU], t0, t1, t2;
  float u2;
  unsigned a_mu, a_cs, v_hg, v_fg, a_st, a_css[NU], a_aw[NU], a_ar[NU], v_pub[NU], v_lk0[NU], v_lk1[NU], v_hs[NU], v_fs, a_tw, a_tr, v_x0, v_x1, a_bs, a_bx;
  static constexpr int PERU = 16;
  static constexpr int NCH = 8 + PERU * NU + 5;
  template <int N> DI void tick(Ctx& c, int srow, int unit0) {
    const int l = c.lane, i = l & 15, q = l >> 4, w = c.w, wt = w & 3, wn = w >> 2, grp = wt * 2 + wn;
    if constexpr (N == 0) {
#pragma unroll
      for (int j = 0; j < 5; ++j) { v[2 * j] = bf2f(xr[j]); v[2 * j + 1] = bfhi(xr[j]); PIN(v[2 * j]); PIN(v[2 * j + 1]); }
    } else if constexpr (N == 1) {
      s = ((v[0] + v[1]) + (v[2] + v[3])) + ((v[4] + v[5]) + (v[6] + v[7])) + (v[8] + v[9]); PIN(s);
    } else if constexpr (N == 2) { s = sumr(s); PIN(s); }
    else if constexpr (N == 3) { s = sumq(s) * (1.f / 640.f); PIN(s); }
    else if constexpr (N == 4) {
#pragma unroll
      for (int j = 0; j < 10; ++j) { v[j] -= s; PIN(v[j]); }
    } else if constexpr (N == 5) {
      e = v[0] * v[0];
#pragma unroll
      for (int j = 1; j < 10; ++j) e = fmaf(v[j], v[j], e);
      PIN(e);
    } else if constexpr (N == 6) { e = sumr(e); PIN(e); }
    else if constexpr (N == 7) {
      const float var = rbf(sumq(e) * (1.f / 640.f));
      const float sd = rbf(__builtin_amdgcn_sqrtf(rbf(var + 1e-5f)));
      float rc = __builtin_amdgcn_rcpf(sd);
      rc = fmaf(fmaf(-sd, rc, 1.f), rc, rc);
      st128(c, v4u{__float_as_uint(rbf(s)), c.T, __float_as_uint(rc), c.T2}, l == 0 ? WS_STAT + srow * 16 : OOB);
    } else if constexpr (N < 8 + PERU * NU) {
      constexpr int u = (N - 8) / PERU, k = (N - 8) % PERU;
      const int ul = NU == 1 ? 0 : (u + (w >> 1)) % 3, ua = unit0 + ul;
      const int e1 = grp - 2 - 2 * q, k0 = c.rb - 1 - q;
      const unsigned pw = L_POW + (ul * 16 + i) * 8;
      if constexpr (k == 0) { if (NU == 1) { dt = rbf(fexp(bf2f(pd))); PIN(dt); } }
      else if constexpr (k == 1) { if (NU == 1) { e = fexp(dt * bf2f(pa)); PIN(e); } }
      else if constexpr (k == 2) { if (NU == 1) { fsincos(dt * bf2f(pb), sn, cs); PIN(sn); PIN(cs); } }
      else if constexpr (k == 3) {
        if (NU == 1) { lam[u] = cpx{e * cs, e * sn}; t0 = cmul(lam[u], lam[u]); t0 = cmul(t0, t0); L8[u] = cmul(t0, t0); t0 = cmul(L8[u], L8[u]); L32[u] = cmul(t0, t0); }
        else { lam[u] = ldsr<cpx>(pw); L8[u] = ldsr<cpx>(pw + 512); t0 = ldsr<cpx>(pw + 1024); L32[u] = ldsr<cpx>(pw + 1536); }
      }
      else if constexpr (k == 4) { f1[u] = cmul(csel(q & 1, L8[u]), csel(q & 2, t0)); PIN(lam[u]); PIN(L8[u]); PIN(t0); PIN(L32[u]); PIN(f1[u]); }
      else if constexpr (k == 5) {
        u1[u] = czero(q < 2, csel(q == 3, L8[u])); PIN(u1[u]);
        u2 = q == 3 ? 1.f : 0.f; PIN(u2);
      } else if constexpr (k == 6) { u0[u] = czero(q == 0, q == 3 ? t0 : csel(q == 2, L8[u])); PIN(u0[u]); }
      else if constexpr (k == 7) {
        if (NU == 1) { t0 = cmul(L32[u], L32[u]); t1 = cmul(t0, t0); PIN(t0); PIN(t1); } else { t0 = ldsr<cpx>(pw + 2048); t1 = ldsr<cpx>(pw + 2560); }
      }
      else if constexpr (k == 8) { W1[u] = cmul(csel(e1 & 1, L32[u]), csel(e1 & 2, t0)); t2 = cmul(csel(grp & 1, L32[u]), csel(grp & 2, t0)); PIN(W1[u]); PIN(t2); }
      else if constexpr (k == 9) { W1[u] = cmul(W1[u], csel(e1 & 4, t1)); t2 = cmul(t2, csel(grp & 4, t1)); PIN(W1[u]); PIN(t2); }
      else if constexpr (k == 10) { W0[u] = czero(e1 < -1, csel(e1 >= 0, cmul(W1[u], L32[u]))); W1[u] = czero(e1 < 0, W1[u]); PIN(W0[u]); PIN(W1[u]); }
      else if constexpr (k == 11) { t0 = cmul(t1, t1); t1 = cmul(t0, t0); wk1[u] = cmul(t2, csel(k0 & 1, t0)); PIN(t1); PIN(wk1[u]); }
      else if constexpr (k == 12) { wk1[u] = cmul(wk1[u], csel(k0 & 2, t1)); t1 = cmul(t1, t1); PIN(t1); PIN(wk1[u]); }
      else if constexpr (k == 13) { wk0[u] = czero(k0 < 0, cmul(wk1[u], csel(k0 & 4, t1))); wk1[u] = czero(k0 < 4, wk1[u]); PIN(wk0[u]); PIN(wk1[u]); }
      else if constexpr (k == 14) {
        a_css[u] = L_CSS + (NU == 1 ? q * 128 : ul * 128) + i * 4;
        a_ar[u] = L_AGG + (ul * 16 + i) * 64 + q * 16;
        a_aw[u] = q == 3 ? L_AGG + (ul * 16 + i) * 64 + grp * 8 : L_DUMMY + l * 8;
        v_hs[u] = ua < 40 ? WS_H + (c.rb * 4 + wt) * H_RT + ustep(ua) * 4096 + wn * 2048 + l * 16 : OOB;
        PIN(a_css[u]); PIN(a_ar[u]); PIN(a_aw[u]); PIN(v_hs[u]);
      } else {
        const unsigned po = WS_AGG + (ua * 16 + i) * 16;
        v_pub[u] = (q == 3 && ua < 40) ? po + c.rb * 10240 : OOB;
        v_lk0[u] = (k0 >= 0 && ua < 40) ? po + q * 10240 : OOB;
        v_lk1[u] = (k0 >= 4 && ua < 40) ? po + (q + 4) * 10240 : OOB;
        PIN(v_pub[u]); PIN(v_lk0[u]); PIN(v_lk1[u]);
      }
    } else {
      constexpr int k = N - 8 - PERU * NU;
      const unsigned fbase = WS_FLAGH + (c.rb * 4 + wt) * 2048;
      if constexpr (k == 0) {
        if (NU == 1) {
          const int step = 2 * c.cg + wn;
          a_mu = L_ST + (wt * 64 + wn * 32 + 8 * (i >> 2) + (i & 3)) * 8;
          a_cs = L_CS + wn * 256 + wt * 64 + q * 16;
          v_hg = WS_H + (c.rb * 4 + wt) * H_RT + step * 4096 + wn * 2048 + l * 16;
          v_fg = l < 2 ? fbase + (step & 7) * 256 + (step >> 3) * 16 + l * 8 : OOB;
          PIN(a_mu); PIN(a_cs); PIN(v_hg); PIN(v_fg);
        }
      } else if constexpr (k == 1) {
        const int step = ustep(unit0 + l);
        v_fs = (l < NU && unit0 + l < 40) ? fbase + (step & 7) * 256 + (step >> 3) * 16 + wn * 8 : OOB;
        a_st = L_ST + (grp * 4 + q) * 64;
        PIN(v_fs); PIN(a_st);
      } else if constexpr (k == 2) {
        const unsigned tr = (wn ? LWB(2) : LWB(0)) + 8192 + wt * 2048;
        const int pr = 4 * q + ((l >> 2) & 3);
        a_tw = tr + i * 64 + ((q ^ (i >> 1)) & 3) * 16;
        a_tr = tr + pr * 64 + ((l ^ (pr >> 1)) & 3) * 16;
        const int tl = (wt >> 1) * 32 + 8 * (i >> 2) + 4 * (wt & 1) + (i & 3);
        a_bs = L_ST + (c.rt * 64 + tl) * 8;
        a_bx = tl * 160 + q * 8;
        PIN(a_tw); PIN(a_tr); PIN(a_bs); PIN(a_bx);
      } else if constexpr (k == 3) {
        c.vh2 = WS_H + (c.rb * 4 + c.rt) * H_RT + c.wc * 4096 + (w & 3) * 1024 + l * 16;
        c.vfl = WS_FLAGH + (c.rb * 4 + c.rt) * 2048 + c.wc * 256 + (l & 31) * 8;
        PIN(c.vh2); PIN(c.vfl);
      } else {
        c.vw2 = WS_W2T + c.ct * W2T_CT + c.wc * 5120 + l * 16;
        const unsigned c0 = w * 64 + l, r0 = c0 / 10, c1 = c0 + 512, r1 = c1 / 10, xb = (c.rb * 256 + c.rt * 64) * 1280 + c.ct * 160;
        v_x0 = xb + r0 * 1280 + (c0 - r0 * 10) * 16;
        v_x1 = w < 2 ? xb + r1 * 1280 + (c1 - r1 * 10) * 16 : ((w == 2 && l < 10) ? c.ct * 160 + l * 16 : OOB);
        PIN(c.vw2); PIN(v_x0); PIN(v_x1);
      }
    }
  }
};

template <int TYPE> DI void phase1(Ctx& c, const u16* lre, const u16* lim, const u16* ldt) {
  const int l = c.lane, i = l & 15, q = l >> 4, w = c.w, wt = w & 3, wn = w >> 2;
  const int rb = c.rb, cg = c.cg;
  constexpr int NU = G1<TYPE>::NU, NP = G1<TYPE>::NP;
  G1<TYPE> g;
  Side<NU> sd;
  const int srow = rb * 256 + cg * 8 + w;
  const int si = rb * 5 + (cg - 27), sj = si, mi = rb * 27 + cg;
  const int unit0 = TYPE == 0 ? cg : 27 + 3 * (cg - 27);
  __builtin_amdgcn_s_waitcnt(0x0070);
  unsigned cpf;
  {
    const unsigned n = ((blockIdx.x >> 3) & (CPS - 1)) * (512 / CPS) + (threadIdx.x & (512 / CPS - 1));
    if (w < 8 / CPS) asm volatile("global_load_dword %0, %1, %2" : "=v"(cpf) : "v"(min(n * 128u, (unsigned)CPF)), "s"(c.pc) : "memory");
  }
  g.init_stage(c, unit0);
  unsigned ep;
  {
    const u16* xrow = c.x + (size_t)srow * D_;
    asm volatile(
        "s_nop 4\n\tglobal_load_dword %5, %8, %9 sc1\n\t"
        "global_load_dword %0, %6, %7\n\t"
        "global_load_dword %1, %6, %7 offset:256\n\t"
        "global_load_dword %2, %6, %7 offset:512\n\t"
        "global_load_dword %3, %6, %7 offset:768\n\t"
        "global_load_dword %4, %6, %7 offset:1024"
        : "=&v"(sd.xr[0]), "=&v"(sd.xr[1]), "=&v"(sd.xr[2]), "=&v"(sd.xr[3]), "=&v"(sd.xr[4]), "=&v"(ep)
        : "v"(l * 4), "s"(xrow), "v"(0), "s"(c.ws)
        : "memory");
    const int pidx = TYPE == 0 ? unit0 * 16 + i : (w == 0 ? (sj >> 2) * 64 + l : min(unit0 * 16 + l, 639));
    asm volatile("s_nop 4\n\tglobal_load_ushort %0, %3, %4\n\tglobal_load_ushort %1, %3, %5\n\tglobal_load_ushort %2, %3, %6"
                 : "=&v"(sd.pa), "=&v"(sd.pb), "=&v"(sd.pd) : "v"(pidx * 2), "s"(lre), "s"(lim), "s"(ldt) : "memory");
  }
  if (TYPE == 0) {
    g.template half0<0>(); g.template half1<0>();
    const v4u rj = mkrsrc(c.wd, F_ * D_ * 2);
    const unsigned c0 = w * 64 + l, r0 = c0 / 20, c1 = c0 + 512, r1 = c1 / 20;
    glds16<0, LWB(2) + 8192>(rj, r0 * 1280 + (c0 - r0 * 20) * 16, (mi >> 2) * 40960 + (mi & 3) * 320, g.w1k);
    glds16<0, LWS(2)>(rj, w < 2 ? r1 * 1280 + (c1 - r1 * 20) * 16 : OOB, (mi >> 2) * 40960 + (mi & 3) * 320, w < 2 ? g.w1k : 65536);
  } else {
    const v4u rj = mkrsrc(wn ? c.cim : c.cre, 640 * 640 * 2);
#pragma unroll
    for (int j = 0; j < 5; ++j) {
      const unsigned row = (wt * 5 + j) * 8 + (l >> 3);
      const unsigned vo = row * 1280 + (((l & 7) ^ ((row >> 1) & 7)) * 16);
      glds16<0, LX(2)>(rj, vo, (sj & 3) * 160 * 1280 + (sj >> 2) * 128, (w * 5 + j) * 1024);
    }
    g.template half0<0>(); g.template half1<0>();
  }
  auto ticks = [&](auto self, auto N, auto E) { if constexpr (N.v < E.v) { sd.template tick<N.v>(c, srow, unit0); self(self, IC<N.v + 1>{}, E); } };
  g.init_read(c);
  unsigned wflag = OOB, ja = 0, jb = 0, jc = 0, jo0 = OOB, jo1 = OOB;
  if (TYPE == 0) {
    ticks(ticks, IC<8 + 14>{}, IC<Side<NU>::NCH>{});
    const int step = mi >> 2, f = 4 * q + ((l >> 2) & 3);
    ja = f * 320 + w * 32 + (l & 3) * 8;
    jb = ja + 5120; jc = ja + 5376;
    ja += LWB(2) + 8192; jb += jb < 8192 ? LWB(2) + 8192 : LWS(2) - 8192; jc += jc < 8192 ? LWB(2) + 8192 : LWS(2) - 8192;
    PIN(jb); PIN(jc);
    const int dt = (mi & 3) * 10 + w, ct = dt / 5;
    jo0 = WS_W2T + ct * W2T_CT + step * 5120 + (dt - ct * 5) * 1024 + l * 16;
    jo1 = w < 2 ? WS_W2T + ((mi & 3) * 2 + 1) * W2T_CT + step * 5120 + (3 + w) * 1024 + l * 16 : OOB;
    wflag = (w == 0 && l < 2) ? WS_FLAGW + (((mi & 3) * 2 + l) * 8 + (step & 7)) * 128 + (step >> 3) * 8 : OOB;
    PIN(ja); PIN(jo0); PIN(jo1); PIN(wflag);
  }
  g.template half0<1>();
  asm volatile("s_waitcnt vmcnt(%6)" : "+v"(sd.xr[0]), "+v"(sd.xr[1]), "+v"(sd.xr[2]), "+v"(sd.xr[3]), "+v"(sd.xr[4]), "+v"(ep) : "n"((TYPE == 0 ? 13 : 15) - 2 * P6D) : "memory");
  asm volatile("" : "+v"(sd.pa), "+v"(sd.pb), "+v"(sd.pd));
  STAMP(1);
  asm volatile("" ::"v"(cpf));
  c.T = __builtin_amdgcn_readfirstlane(ep) + 1u;
  c.T2 = (c.T * 0x9E3779B1u) ^ 0x5bd1e995u;
  c.tag = v2u{c.T, c.T2};
  PIN(c.tag);
  typename G1<TYPE>::Frags fa, fb;
  if (TYPE == 0) {
    ticks(ticks, IC<0>{}, IC<7>{});
    g.template half1<1>();
    ticks(ticks, IC<7>{}, IC<PRE>{});
    waitv<8 - P6D>();
    BAR();
    g.template rdall<0>(fa);
  } else {
    if (w < 2) {
      const float ar = bf2f(sd.pa), ai = bf2f(sd.pb), dt = rbf(fexp(bf2f(sd.pd))), e = fexp(dt * ar);
      float sn, cs;
      fsincos(dt * ai, sn, cs);
      cpx p = {e * cs, e * sn};
      if (w == 0) {
        const float inv = 2.f / (ar * ar + ai * ai);
        ldsw<f32x2>(L_COEF + l * 8, cmul(cpx{p.x - 1.f, p.y}, cpx{ar * inv, -ai * inv}));
      } else {
        ldsw<f32x2>(L_POW + l * 8, p);
        p = cmul(p, p); p = cmul(p, p);
#pragma unroll
        for (int k = 1; k < 6; ++k) { p = cmul(p, p); ldsw<f32x2>(L_POW + k * 512 + l * 8, p); }
      }
    }
    g.template half1<1>();
    waitv<12 - 2 * P6D>();
    BAR();
    const f32x4 k0 = ldsr<f32x4>(L_COEF + (16 * wt + 4 * q) * 8), k1 = ldsr<f32x4>(L_COEF + (16 * wt + 4 * q) * 8 + 16);
    const cpx kc[4] = {{k0.x, k0.y}, {k0.z, k0.w}, {k1.x, k1.y}, {k1.z, k1.w}};
    const int step = ustep(4 * (sj >> 2) + wt), ct = 2 * (sj & 3) + wn;
#pragma unroll
    for (int j = 0; j < 5; ++j) {
      const unsigned row = (wn * 5 + j) * 16 + i;
      const unsigned a = LX(2) + row * 128 + (((2 * wt + (q >> 1)) ^ ((row >> 1) & 7)) * 16) + (q & 1) * 8;
      const v2u cr = ldsr<v2u>(a), ci = ldsr<v2u>(a + 20480);
      const unsigned cr0 = cr.x, cr1 = cr.y, ci0 = ci.x, ci1 = ci.y;
      const cpx p0 = cmul(kc[0], cpx{bf2f(cr0), bf2f(ci0)}), p1 = cmul(kc[1], cpx{bfhi(cr0), bfhi(ci0)});
      const cpx p2 = cmul(kc[2], cpx{bf2f(cr1), bf2f(ci1)}), p3 = cmul(kc[3], cpx{bfhi(cr1), bfhi(ci1)});
      const v4u o = {pk2(p0.x, p1.x), pk2(p2.x, p3.x), pk2(-p0.y, -p1.y), pk2(-p2.y, -p3.y)};
      st128(c, o, l * 16 + j * 1024 + (WS_W2T + ct * W2T_CT + step * 5120));
    }
    wflag = l == 0 ? WS_FLAGW + (ct * 8 + (step & 7)) * 128 + (step >> 3) * 8 : OOB;
    SB0();
    waitv<11 - P6D>();
    BAR();
    g.template rdall<0>(fa);
  }
  STAMP(2); CSTAMP(0);
  SB0();
  v4u sg; v2u fw; unsigned pfd = 0;
  bf16x8 jv0, jv1;
  const v4u rwsv = mkrsrc(c.ws, WS_END);
  auto w2l = [&](auto CH, auto A) { glds16sc1<LP(CH.v) * 40960 + A.v * 1024, tile_base(CH.v, A.v)>(rwsv, c.vw2, A.v < 4 ? g.w4k : g.w1k); };
  auto side = [&](auto SUB, auto G) {
    constexpr int k = TYPE == 0 ? (G.v == 8 ? 0 : -1) : G.v - 1;
    constexpr int n = TYPE == 0 ? PRE + SUB.v : SUB.v * 5 + k;
    if constexpr (k >= 0 && n < (TYPE == 0 ? 22 : Side<NU>::NCH)) sd.template tick<n>(c, srow, unit0);
  };
  static_assert(TYPE == 0 ? (22 - PRE <= 19 && PRE + 10 >= 8) : Side<NU>::NCH <= 70, "side work schedule");
  constexpr int GA = TYPE == 0 ? 4 : 2, GB = TYPE == 0 ? 6 : 4;
  auto step = [&](auto SS) {
    constexpr int S = SS.v;
    LGKM0();
    g.template part<2 * S>(fa, fb, [&](auto G) {
      if constexpr (S == 0) {
        if constexpr (TYPE == 0) {
          if (G.v == 7) { jv0 = cat(trread(ja), trread(jb)); jv1 = cat(trread(ja + 256), trread(jc)); }
        }
        if (G.v == 0) g.template piece<0, 2>();
        if (G.v == 1) g.template piece<4, 2>();
        if (G.v == 2) g.template piece<1, 2>();
        if (G.v == 3) g.template piece<2, 2>();
        if (G.v == 4) g.template piece<3, 2>();
      } else if constexpr (S + 2 < 10) {
        if (G.v == 1) g.template piece<2, S + 2>();
        if (G.v == GA) g.template piece<5, S + 2>();
        if (G.v == GB) g.template piece<3, S + 2>();
      } else {
        if (G.v == 1) w2l(IC<S - 8>{}, IC<3>{});
        if (G.v == GB) w2l(IC<S - 8>{}, IC<4>{});
      }
      side(IC<2 * S>{}, G);
    });
    if constexpr (S < 9) {
      if (S == SCHK) asm volatile("s_waitcnt vmcnt(%2)" : "+v"(sg), "+v"(fw) : "n"(NP) : "memory");
      else if (S == 8) waitv<5>();
      else if (S == 0) waitv<TYPE == 0 ? 5 : 10>();
      else if (S == JS + 1 && TYPE == 0) waitv<NP + 1>();
      else waitv<NP>();
      LGKM0();
      asm volatile("s_barrier" ::: "memory");
    } else LGKM0();
    if (S == 0) { g.template piece<6, 2>(); g.template piece<5, 2>(); }
    if (S == JS && TYPE == 0) { st128(c, __builtin_bit_cast(v4u, jv0), jo0); if (w < 2) st128(c, __builtin_bit_cast(v4u, jv1), jo1); }
    if (S == (TYPE == 0 ? JS + 2 : 1)) st_flag(c, wflag);
    if (S == SCHK - 1) {
      const GAS char* sp = (const GAS char*)c.ws + WS_STAT + rb * 4096;
      const GAS char* fp = (const GAS char*)c.ws + WS_FLAGW + (c.ct * 8 + c.wc) * 128;
      asm volatile("s_nop 4\n\tglobal_load_dwordx4 %0, %2, %3 sc1\n\tglobal_load_dwordx2 %1, %4, %5 sc1"
                   : "=&v"(sg), "=&v"(fw) : "v"(wt * 1024 + l * 16), "s"(sp), "v"(i * 8), "s"(fp) : "memory");
    }
    if (S == SCHK) {
      const bool need = i < 12 && c.wc + 8 * i < NS2;
      SPIN_LOOP {
        if (__all((!need || (fw.x == c.T && fw.y == c.T2)) && sg.y == c.T && sg.w == c.T2)) break;
        __builtin_amdgcn_s_sleep(1);
        asm volatile("" ::: "memory");
        sg = ld128(c, WS_STAT + rb * 4096 + wt * 1024 + l * 16);
        fw = ld64(c, WS_FLAGW + (c.ct * 8 + c.wc) * 128 + i * 8);
      }
      ldsw<v2u>(L_ST + (wt * 64 + l) * 8, v2u{sg.x, sg.z});
    }
    if (S == 7) {
      glds16<0, L_XT0>(g.rx, sd.v_x0, 0, g.w1k);
      if (w < 3) glds16<0, L_XT1>(w == 2 ? mkrsrc(c.dsk, 1280) : g.rx, sd.v_x1, 0, g.w1k);
    }
    SB0();
    g.template part<2 * S + 1>(fb, fa, [&](auto G) {
      if constexpr (false) {
      } else if constexpr (S + 3 < 10) {
        if (G.v == 0) g.template piece<6, S + 3>();
        if (G.v == (TYPE == 0 ? 2 : 1)) g.template piece<0, S + 3>();
        if (G.v == GA) g.template piece<4, S + 3>();
        if (G.v == GB) g.template piece<1, S + 3>();
      } else if constexpr (S < 9) {
        if (G.v == 1) w2l(IC<S - 7>{}, IC<0>{});
        if (G.v == GA) w2l(IC<S - 7>{}, IC<1>{});
        if (G.v == GB) w2l(IC<S - 7>{}, IC<2>{});
      } else {
        if (G.v == G1<TYPE>::NG - 1) {
          if (TYPE == 0) ldsw<float>(L_CS + ((wn * 4 + wt) * 16 + i) * 4, g.cs0.x);
          const unsigned ca = L_CSS + (TYPE == 0 ? w : (w >> 1) % 3) * 128 + i * 4;
          ldsw<float>(ca, g.cs1.x); ldsw<float>(ca + 64, g.cs2.x);
        }
      }
      side(IC<2 * S + 1>{}, G);
    });
  };
  step(IC<0>{}); step(IC<1>{}); step(IC<2>{}); step(IC<3>{}); step(IC<4>{}); step(IC<5>{}); step(IC<6>{});
  CSTAMP(1); STAMP(9);
  step(IC<7>{}); step(IC<8>{}); step(IC<9>{});
  asm volatile("" ::"v"(pfd));
  MFMA_DRAIN();
  BAR();
  SB0();
  STAMP(3);
  auto w2c2 = [&]() { w2l(IC<2>{}, IC<0>{}); w2l(IC<2>{}, IC<1>{}); w2l(IC<2>{}, IC<2>{}); w2l(IC<2>{}, IC<3>{}); w2l(IC<2>{}, IC<4>{}); };
  __builtin_amdgcn_s_setprio(3);
  if (TYPE == 0) {
    f32x4 cs[4];
    f32x2 mu[4];
#pragma unroll
    for (int a = 0; a < 4; ++a) cs[a] = ldsr<f32x4>(sd.a_cs ^ (a * 64));
#pragma unroll
    for (int m = 0; m < 4; ++m) mu[m] = ldsr<f32x2>((m < 2 ? sd.a_mu : (sd.a_mu ^ 256)) + (m & 1) * 32);
    auto glu = [&](auto ODD) {
#pragma unroll
      for (int m = 0; m < 4; ++m) {
        const f32x4 mv = {-mu[m].x, -mu[m].x, -mu[m].x, -mu[m].x};
        const float r2 = mu[m].y * mu[m].y;
        const f32x4 h0 = __builtin_elementwise_fma(mv, cs[ODD.v], g.acc[ODD.v][m]) * __builtin_elementwise_fma(mv, cs[2 + ODD.v], g.acc[2 + ODD.v][m]) * r2;
        const f32x4 h1 = __builtin_elementwise_fma(mv, cs[1 - ODD.v], g.acc[1 - ODD.v][m]) * __builtin_elementwise_fma(mv, cs[3 - ODD.v], g.acc[3 - ODD.v][m]) * r2;
        st128(c, v4u{pk2(h0[0], h0[1]), pk2(h0[2], h0[3]), pk2(h1[0], h1[1]), pk2(h1[2], h1[3])}, (m < 2 ? sd.v_hg : (sd.v_hg ^ 2048)) + (m & 1) * 1024);
        asm volatile("" ::: "memory");
        if (IL >= 1) {
          if (m == 0) { w2l(IC<2>{}, IC<0>{}); w2l(IC<2>{}, IC<1>{}); }
          if (m == 1) { w2l(IC<2>{}, IC<2>{}); w2l(IC<2>{}, IC<3>{}); }
          if (m == 2) { w2l(IC<2>{}, IC<4>{}); }
        }
      }
    };
    if (IL >= 2) { ldw<0>(c, c.wf[0]); SB0(); }
    if (w & 1) glu(IC<1>{}); else glu(IC<0>{});
    if (IL >= 3) { ldw<1>(c, c.wf[1]); SB0(); }
    SB0();
  }
  if (TYPE == 1) { w2c2(); if (SA > 0) ldw<0>(c, c.wf[0]); if (SA > 1) ldw<1>(c, c.wf[1]); SB0(); }
  cpx v[NU][8], eq[NU], tot[NU];
  {
    f32x4 st[4];
#pragma unroll
    for (int e = 0; e < 4; ++e) st[e] = ldsr<f32x4>(sd.a_st + e * 16);
#pragma unroll
    for (int u = 0; u < NU; ++u) {
      float csx = ldsr<float>(sd.a_css[u]), csy = ldsr<float>(sd.a_css[u] + 64);
      if (TYPE == 0) { csx = sumq(csx + ldsr<float>(sd.a_css[u] + 512)); csy = sumq(csy + ldsr<float>(sd.a_css[u] + 576)); }
#pragma unroll
      for (int j = 0; j < 8; ++j) {
        const float mu = (j & 1) ? st[j >> 1].z : st[j >> 1].x, rs = (j & 1) ? st[j >> 1].w : st[j >> 1].y;
        float a = fmaf(-mu, csx, g.accs[2 * u][j >> 2][j & 3]), b = fmaf(-mu, csy, g.accs[2 * u + 1][j >> 2][j & 3]);
        asm("" : "+v"(a), "+v"(b));
        v[u][j] = cpx{a * rs, b * rs};
        asm("" : "+v"(v[u][j]));
      }
      cpx h = v[u][0];
#pragma unroll
      for (int j = 1; j < 8; ++j) h = cfma(sd.lam[u], h, v[u][j]);
      float ax[4], ay[4];
      gatherq(h.x, ax); gatherq(h.y, ay);
      eq[u] = cfma(sd.u0[u], cpx{ax[0], ay[0]}, cfma(sd.u1[u], cpx{ax[1], ay[1]}, cpx{sd.u2 * ax[2], sd.u2 * ay[2]}));
      tot[u] = cfma(sd.L8[u], eq[u], h);
      ldsw<f32x2>(sd.a_aw[u], tot[u]);
    }
  }
  SB0();
  BAR();
  cpx pm[NU];
#pragma unroll
  for (int u = 0; u < NU; ++u) {
    const f32x4 t = ldsr<f32x4>(sd.a_ar[u]);
    pm[u] = cfma(sd.W0[u], cpx{t.x, t.y}, cmul(sd.W1[u], cpx{t.z, t.w}));
  }
  if (w == 7) {
#pragma unroll
    for (int u = 0; u < NU; ++u) {
      const cpx P = cfma(sd.L32[u], cpx{sumq(pm[u].x), sumq(pm[u].y)}, tot[u]);
      st128(c, v4u{__float_as_uint(P.x), c.T, __float_as_uint(P.y), c.T2}, sd.v_pub[u]);
    }
  }
  SB0();
  if (TYPE == 0) {
    if (w == 7) waitv<1 + (IL >= 3 ? 5 : 0)>(); else waitv<(IL >= 3 ? 5 : 0)>();
    st_flag(c, sd.v_fg);
    STAMP(4);
  }
  asm volatile("s_barrier" ::: "memory");
  auto ldwr = [&](auto A, auto B) {
    if constexpr (A.v <= 0 && 0 < B.v) ldw<0>(c, c.wf[0]);
    if constexpr (A.v <= 1 && 1 < B.v) ldw<1>(c, c.wf[1]);
    if constexpr (A.v <= 2 && 2 < B.v) ldw<2>(c, c.wf[2]);
    if constexpr (A.v <= 3 && 3 < B.v) ldw<3>(c, c.wf[3]);
    SB0();
  };
  PRI(2);
  if (TYPE == 0) { if (IL == 0) w2c2(); ldwr(IC<(IL >= 2 ? IL - 1 : 0)>{}, IC<NA>{}); }
  PRI(3);
  v4u lk0[NU], lk1[NU];
#pragma unroll
  for (int u = 0; u < NU; ++u) { lk0[u] = ld128(c, sd.v_lk0[u]); lk1[u] = ld128(c, sd.v_lk1[u]); }
  if (TYPE == 0) c.fl = ld64(c, c.vfl);
  PRI(1);
  if (TYPE == 0) ldwr(IC<NA>{}, IC<NA + NB>{}); else ldwr(IC<SA>{}, IC<SA + 1>{});
  PRI(3);
  auto hwait = [&]() {
    if ((__ballot(c.fl.x == c.T && c.fl.y == c.T2) & HM) != HM) {
      SPIN_LOOP {
        asm volatile("" ::: "memory");
        c.fl = ld64(c, c.vfl);
        if ((__ballot(c.fl.x == c.T && c.fl.y == c.T2) & HM) == HM) break;
      }
    }
    __builtin_amdgcn_fence(__ATOMIC_ACQUIRE, "wavefront");
    PRI(1);
    if (HF) c.fl = ld64(c, c.vfl);
    ldh<0>(c, c.hf[0]); ldh<1>(c, c.hf[1]);
    PRI(3);
    SB0();
  };
  if (TYPE == 0) hwait();
  {
    const f32x2 st = ldsr<f32x2>(sd.a_bs);
    auto item = [&](int a) {
      const unsigned xo = sd.a_bx + a * 32;
      const v2u xv = ldsr<v2u>(xo < 8192 ? L_XT0 + xo : L_XT1 - 8192 + xo), dv = ldsr<v2u>(L_XT1 + 2048 + q * 8 + a * 32);
      const unsigned x0 = xv.x, x1 = xv.y, d0 = dv.x, d1 = dv.y;
      const float xf[4] = {bf2f(x0), bfhi(x0), bf2f(x1), bfhi(x1)}, df[4] = {bf2f(d0), bfhi(d0), bf2f(d1), bfhi(d1)};
      f32x4 r;
#pragma unroll
      for (int e = 0; e < 4; ++e) {
        float xn = rbf(rbf(xf[e] - st.x) * st.y);
        asm("" : "+v"(xn));
        r[e] = xf[e] + rbf(xn * df[e]);
      }
      return r;
    };
    const f32x4 z = {0.f, 0.f, 0.f, 0.f};
    if (wn == 0) { c.acc0[0] = item(0); c.acc0[1] = item(1); c.acc0[2] = item(2); c.acc0[3] = z; c.acc0[4] = z; }
    else { c.acc0[0] = z; c.acc0[1] = z; c.acc0[2] = z; c.acc0[3] = item(3); c.acc0[4] = item(4); }
#pragma unroll
    for (int a = 0; a < 5; ++a) PIN(c.acc0[a]);
    SB0();
  }
  auto lkok = [&]() {
    bool ok = true;
#pragma unroll
    for (int u = 0; u < NU; ++u)
      ok = ok && (sd.v_lk0[u] == OOB || (lk0[u].y == c.T && lk0[u].w == c.T2)) && (sd.v_lk1[u] == OOB || (lk1[u].y == c.T && lk1[u].w == c.T2));
    return __all(ok);
  };
  if (!lkok()) {
    SPIN_LOOP {
      asm volatile("" ::: "memory");
#pragma unroll
      for (int u = 0; u < NU; ++u) { lk0[u] = ld128(c, sd.v_lk0[u]); lk1[u] = ld128(c, sd.v_lk1[u]); }
      if (lkok()) break;
    }
  }
  SB0();
  STAMP(5);
#pragma unroll
  for (int u = 0; u < NU; ++u) {
    const unsigned x0 = lk0[u].x, y0 = lk0[u].z, x1 = lk1[u].x, y1 = lk1[u].z;
    const cpx cy = cfma(sd.wk1[u], cpx{__uint_as_float(x1), __uint_as_float(y1)}, cfma(sd.wk0[u], cpx{__uint_as_float(x0), __uint_as_float(y0)}, pm[u]));
    cpx h = cfma(sd.f1[u], cpx{sumq(cy.x), sumq(cy.y)}, eq[u]);
#pragma unroll
    for (int j = 0; j < 8; ++j) { h = cfma(sd.lam[u], h, v[u][j]); v[u][j] = h; }
    ldsw<v4u>(sd.a_tw, v4u{pk2(v[u][0].x, v[u][1].x), pk2(v[u][2].x, v[u][3].x), pk2(v[u][4].x, v[u][5].x), pk2(v[u][6].x, v[u][7].x)});
    ldsw<v4u>(sd.a_tw + 1024, v4u{pk2(v[u][0].y, v[u][1].y), pk2(v[u][2].y, v[u][3].y), pk2(v[u][4].y, v[u][5].y), pk2(v[u][6].y, v[u][7].y)});
    asm volatile("s_waitcnt lgkmcnt(0)" ::: "memory");
#pragma unroll
    for (int mm = 0; mm < 2; ++mm) st128(c, __builtin_bit_cast(v4u, cat(trread(sd.a_tr + mm * 8), trread(sd.a_tr + mm * 8 + 1024))), sd.v_hs[u] + mm * 1024);
    if (u + 1 < NU) asm volatile("s_waitcnt lgkmcnt(0)" ::: "memory");
  }
  SB0();
  c.vfs = sd.v_fs;
  if (TYPE == 1) c.fl = ld64(c, c.vfl);
  PRI(0);
  if (TYPE == 0) ldwr(IC<NA + NB>{}, IC<4>{}); else ldwr(IC<SA + 1>{}, IC<4>{});
  if (TYPE == 1) {
    waitv<(3 - SA) * 5 + 1>();
    PRI(3);
    st_flag(c, sd.v_fs);
    c.vfs = OOB;
    STAMP(4);
    hwait();
    PRI(0);
  }
  if (TYPE == 1) {
    if (si == 39) {
      SPIN_LOOP {
        bool ok = true;
        asm volatile("" ::: "memory");
#pragma unroll
        for (int k = 0; k < 4; ++k) {
          const v4u t = ld128(c, WS_STAT + (k * 512 + w * 64 + l) * 16);
          ok = ok && t.y == c.T && t.w == c.T2;
        }
        if (__all(ok)) break;
        __builtin_amdgcn_s_sleep(1);
      }
    }
  }
  c.bump = TYPE == 1 && si == 39;
  SB0();
}

DI void phase2(Ctx& c, const u16* dsk, float* out) {
  const int w = c.w, rt = c.rt, ct = c.ct;
  v4u wf[12][5], hf[12][4];
  f32x4 acc[4][5];
  v2u fl = c.fl;
  STAMP(6); CSTAMP(2);
#pragma unroll
  for (int a = 0; a < 5; ++a) acc[0][a] = c.acc0[a];
  auto body = [&](auto JJ) {
    constexpr int j = JJ.v;
    if constexpr (lds_chunk(j + 1) >= 0) {
      constexpr int ch = lds_chunk(j + 1);
      int l = threadIdx.x & 63; PIN(l);
      const unsigned a4 = tile_base(ch, 0) + w * 4096 + l * 16;
#pragma unroll
      for (int a = 0; a < 4; ++a) wf[j + 1][a] = ldsr<v4u>(a4 + a * 1024);
      wf[j + 1][4] = ldsr<v4u>(tile_base(ch, 4) + w * 1024 + l * 16);
    }
    if constexpr (j < 4) {
#pragma unroll
      for (int a = 0; a < 5; ++a) wf[j][a] = c.wf[j][a];
    }
    if constexpr (j < 2) {
#pragma unroll
      for (int m = 0; m < 4; ++m) hf[j][m] = c.hf[j][m];
    }
    if constexpr (j + WA < 12 && j + WA >= 4 && lds_chunk(j + WA) < 0) { ldw<j + WA>(c, wf[j + WA]); SB0(); }
    if constexpr (j == 4 || j == 6) {
      const unsigned long long msk = j == 4 ? (15ull << 12) : ((c.wc >= 6 ? (1ull << 22) : (1ull << 24)) - (1ull << 16));
      if (__builtin_expect((__ballot(fl.x == c.T && fl.y == c.T2) & msk) != msk, 0)) {
        SPIN_LOOP {
          asm volatile("" ::: "memory");
          const v2u f = ld64(c, c.vfl);
          if ((__ballot(f.x == c.T && f.y == c.T2) & msk) == msk) break;
        }
      }
      __builtin_amdgcn_fence(__ATOMIC_ACQUIRE, "wavefront");
    }
    if constexpr (j == 3 || j == 5) fl = ld64(c, c.vfl);
    if constexpr (j + 2 < 12) ldh<j + 2>(c, hf[j + 2]);
    SB0();
#pragma unroll
    for (int a = 0; a < 5; ++a)
#pragma unroll
      for (int m = 0; m < 4; ++m) {
        if (j == 0 && m > 0) mfz(acc[m][a], __builtin_bit_cast(bf16x8, wf[j][a]), __builtin_bit_cast(bf16x8, hf[j][m]));
        else mfa(acc[m][a], __builtin_bit_cast(bf16x8, wf[j][a]), __builtin_bit_cast(bf16x8, hf[j][m]));
      }
    if constexpr (j == JF) {
      waitv<(4 - NA - NB) * 5 + 4 * (JF + 1)>();
      PRI(3);
      st_flag(c, c.vfs);
      SB0();
      __builtin_amdgcn_s_setprio(FP ? 3 : 0);
    }
    if constexpr (FP && (j == 2 || j == 5 || j == 8)) __builtin_amdgcn_s_setprio(j == 2 ? 2 : (j == 5 ? 1 : 0));
    if constexpr (j == JB) {
      BAR();
      __builtin_amdgcn_raw_buffer_store_b32(c.T, c.rws, (c.bump && threadIdx.x == 0) ? WS_EPOCH : OOB, 0, 16);
    }
    if (j == 1) STAMP(10);
    if (j == 3) STAMP(11);
    if (j == 4) STAMP(12);
    if (j == 7) STAMP(13);
    SB0();
  };
  body(IC<0>{}); body(IC<1>{}); body(IC<2>{}); body(IC<3>{}); body(IC<4>{}); body(IC<5>{});
  body(IC<6>{}); body(IC<7>{}); body(IC<8>{}); body(IC<9>{}); body(IC<10>{});
  int l = threadIdx.x & 63; PIN(l);
  unsigned b4 = w * 4096 + l * 16, b4h = b4 + 65536, b1 = 98304 + w * 1024 + l * 16, b2 = 147456 + w * 2048 + l * 16;
  PIN(b4); PIN(b4h); PIN(b1); PIN(b2);
  const int tl = ((w >> 1) & 1) * 32 + 8 * ((l & 15) >> 2) + 4 * (w & 1) + (l & 3);
  GAS float* op = (GAS float*)out + (size_t)(c.rb * 256 + rt * 64 + tl) * D_ + ct * 80 + (w >> 2) * 16 + 4 * (l >> 4);
  PIN(op);
  unsigned r4[4], r1[4], r2;
#pragma unroll
  for (int x = 0; x < 4; ++x) { r4[x] = ((w & 3) ^ x) * 4096 + l * 16 + (x == 1 ? 65536 : 0); r1[x] = 98304 + ((w & 3) ^ x) * 1024 + l * 16; PIN(r4[x]); PIN(r1[x]); }
  r2 = 147456 + ((w & 3) ^ 3) * 2048 + l * 16; PIN(r2);
  body(IC<11>{});
  MFMA_DRAIN();
  SB0();
  STAMP(7);
#pragma unroll
  for (int m = 0; m < 4; ++m)
#pragma unroll
    for (int a = 0; a < 5; ++a) {
      const unsigned tb = tile_base(m, a), ts = tile_wstride(m, a);
      ldsw<f32x4>(ts == 4096 ? (tb >= 65536 ? b4h + (tb - 65536) : b4 + tb) : (ts == 1024 ? b1 + (tb - 98304) : b2 + (tb - 147456)), acc[m][a]);
    }
  BAR();
  auto sum = [&](auto AA) {
    constexpr int a = AA.v;
    f32x4 p[8];
#pragma unroll
    for (int x = 0; x < 4; ++x)
#pragma unroll
      for (int hh = 0; hh < 2; ++hh) {
        const unsigned tb = tile_base(x, a), ts = tile_wstride(x, a);
        p[x * 2 + hh] = ldsr<f32x4>(ts == 4096 ? r4[x] + (tb - (x == 1 ? 65536 : 0)) + hh * 16384 : (ts == 1024 ? r1[x] + (tb - 98304) + hh * 4096 : r2 + (tb - 147456) + hh * 8192));
      }
    return ((p[0] + p[1]) + (p[2] + p[3])) + ((p[4] + p[5]) + (p[6] + p[7]));
  };
  if (w < 4) {
    const f32x4 s0 = sum(IC<0>{}), s1 = sum(IC<2>{}), s2 = sum(IC<4>{});
    *(GAS f32x4*)op = s0; *(GAS f32x4*)(op + 32) = s1; *(GAS f32x4*)(op + 64) = s2;
  } else {
    const f32x4 s0 = sum(IC<1>{}), s1 = sum(IC<3>{});
    *(GAS f32x4*)op = s0; *(GAS f32x4*)(op + 32) = s1;
  }
}

__global__ void __launch_bounds__(512, 2)
fused_kernel(const u16* x, const u16* lre, const u16* lim, const u16* bre, const u16* bim, const u16* cre, const u16* cim, const u16* ldt,
             const u16* dsk, const u16* wg, const u16* wu, const u16* wd, float* out, char* ws) {
  Ctx c;
  asm volatile("s_getpc_b64 %0" : "=s"(c.pc));
  c.dsk = dsk; c.x = x; c.bre = bre; c.bim = bim; c.cre = cre; c.cim = cim; c.wg = wg; c.wu = wu; c.wd = wd; c.ws = ws;
  c.lane = threadIdx.x & 63;
  c.w = __builtin_amdgcn_readfirstlane(threadIdx.x >> 6);
  const int xc = blockIdx.x & 7, ix = blockIdx.x >> 3;
  c.rb = (xc >> 1) * 2 + (ix >> 4); c.cg = (xc & 1) * 16 + (ix & 15);
  c.rt = c.cg >> 3; c.ct = c.cg & 7;
  c.shr = ix >> 3;
  c.wc = (c.w + (ROTC ? c.ct + 2 * c.shr : 0)) & 7;
  c.rws = __builtin_amdgcn_make_buffer_rsrc(ws, 0, WS_END, 0x00020000);
  STAMP(0);
  if (__builtin_expect(c.cg < 27, 1)) phase1<0>(c, lre, lim, ldt);
  else phase1<1>(c, lre, lim, ldt);
  phase2(c, dsk, out);
}

extern "C" void kernel_launch(void* const* d_in, const int* in_sizes, int n_in, void* d_out, int out_size, void* d_ws, size_t ws_size,
                              hipStream_t stream) {
  (void)in_sizes; (void)n_in; (void)out_size; (void)ws_size;
  (void)hipFuncSetAttribute((const void*)fused_kernel, hipFuncAttributeMaxDynamicSharedMemorySize, LDS_BYTES);
  fused_kernel<<<256, 512, LDS_BYTES, stream>>>((const u16*)d_in[0], (const u16*)d_in[1], (const u16*)d_in[2], (const u16*)d_in[3], (const u16*)d_in[4],
                                                (const u16*)d_in[5], (const u16*)d_in[6], (const u16*)d_in[7], (const u16*)d_in[8], (const u16*)d_in[10],
                                                (const u16*)d_in[11], (const u16*)d_in[12], (float*)d_out, (char*)d_ws);
}
```

```cpp
#include <hip/hip_runtime.h>
#include <stdint.h>

#define GAS __attribute__((address_space(1)))
#define LAS __attribute__((address_space(3)))
#define DI __device__ __forceinline__
typedef unsigned short u16;
typedef short bf16x8 __attribute__((ext_vector_type(8)));
typedef short s16x4 __attribute__((ext_vector_type(4)));
typedef __bf16 bf2 __attribute__((ext_vector_type(2)));
typedef float f32x4 __attribute__((ext_vector_type(4)));
typedef float f32x2 __attribute__((ext_vector_type(2)));
typedef unsigned v4u __attribute__((ext_vector_type(4)));
typedef unsigned v2u __attribute__((ext_vector_type(2)));

#define XMAP 2
#define SCHK 7
#define L2PF 0
#define PRE 8
#define ROTC 0
#define ORD 0
#define UO 1
#define JC 6
#define JE 8
#define L2P 0
#define NSHR (XMAP == 1 ? 8 : 4)
#define JF 0
#define P6BR
#define P6D 1
#define JS 2
#define IL 0
#define SA 2
#define CPF 50000
#define CPS 8
#define LP0 7
#define LP1 9
#define LP2 10
#define WA 3
#define FP 1
#define TPP 1
#define LW 1
#define HP 3
#define HA 2
#define NOJB 0
#define HF 0
#define HM (HF ? 0xfull : 0xfffull)
#define NA 4
#define NB 0
#define PRIO 2
#define JB 1
#define NWR 4
#define PH 2
#define NWP 7
#define PWD 3

constexpr int L_ = 2048, D_ = 640, F_ = 1728;
constexpr int ustep(int u) { return UO ? (u >= 27 ? 27 + u : 67 + u) : 54 + u; }
constexpr int NS2 = 94;
constexpr unsigned WS_EPOCH = 0;
constexpr unsigned WS_STAT = 4096;
constexpr unsigned WS_AGG = WS_STAT + 2048 * 16;
constexpr unsigned AGG_RB = 42 * 256;
constexpr unsigned WS_FLAGH = WS_AGG + 8 * AGG_RB;
constexpr unsigned WS_FLAGW = WS_FLAGH + 32 * 8 * 256;
constexpr unsigned WS_CTL_END = WS_FLAGW + 8 * 8 * 128;
constexpr unsigned WS_W2T = 262144;
constexpr unsigned W2T_CT = NS2 * 5 * 1024;
constexpr unsigned WS_H = WS_W2T + 8 * W2T_CT;
constexpr unsigned H_RT = NS2 * 4 * 1024;
constexpr unsigned WS_END = WS_H + 32 * H_RT;
constexpr unsigned OOB = 0x70000000u;
static_assert(WS_CTL_END <= WS_W2T, "ws map");
constexpr unsigned LX(int k) { return k * 32768; }
constexpr unsigned LWB(int k) { return 98304 + ((k + 1) % 3) * 16384; }
constexpr unsigned LWS(int k) { return 147456 + k * 4096; }
constexpr unsigned L_ST = 159744;
constexpr unsigned L_CS = L_ST + 2048;
constexpr unsigned L_CSS = L_CS + 512;
constexpr unsigned L_CS3 = LWB(2) + 8192;
constexpr unsigned L_COEF = L_CSS + 1024;
constexpr unsigned L_POW(int type, int u) { return type == 0 ? LWB(2) + 8192 : (u == 1 ? LWS(0) : LWB(u) + 12288); }
constexpr unsigned L_AGG = LWS(2);
constexpr unsigned L_DUMMY = LWS(0);
constexpr unsigned L_XT0 = LWB(1) + 8192;
constexpr unsigned L_XT1 = LWS(1);
constexpr unsigned LDS_BYTES = 163840;
static_assert(L_COEF + 512 <= LDS_BYTES && LWB(2) == LX(2) + 32768, "lds map");
constexpr unsigned tile_base(int ch, int j) {
  return ch < 3 ? (j < 4 ? LX((ch + 1) % 3) + j * 1024 : LWB((ch + 1) % 3)) : (j < 3 ? LWB(j) + 8192 : LWS(0) + (j - 3) * 1024);
}
constexpr unsigned tile_wstride(int ch, int j) { return ch < 3 ? (j < 4 ? 4096 : 1024) : (j < 3 ? 1024 : 2048); }
constexpr int SPIN_CAP = 1 << 15;

DI float bf2f(unsigned v) { return __uint_as_float(v << 16); }
DI float bfhi(unsigned v) { return __uint_as_float(v & 0xffff0000u); }
DI unsigned pk2(float a, float b) { bf2 v = {(__bf16)a, (__bf16)b}; return __builtin_bit_cast(unsigned, v); }
DI float rbf(float a) { return bf2f(pk2(a, 0.f) & 0xffffu); }
DI void mfa(f32x4& c, bf16x8 a, bf16x8 b) { asm volatile("v_mfma_f32_16x16x32_bf16 %0, %1, %2, %0" : "+v"(c) : "v"(a), "v"(b)); }
DI void mfz(f32x4& c, bf16x8 a, bf16x8 b) { asm volatile("v_mfma_f32_16x16x32_bf16 %0, %1, %2, 0" : "=&v"(c) : "v"(a), "v"(b)); }
template <bool Z> DI void mf(f32x4& c, bf16x8 a, bf16x8 b) { if (Z) mfz(c, a, b); else mfa(c, a, b); }
#define MFMA_DRAIN() asm volatile("s_nop 7\n\ts_nop 1" ::: "memory")
DI v4u mkrsrc(const void* p, unsigned bytes) {
  uint64_t a = (uint64_t)p;
  v4u r; r.x = (unsigned)a; r.y = (unsigned)(a >> 32) & 0xffffu; r.z = bytes; r.w = 0x00020000u;
  return r;
}
template <unsigned SI, unsigned LI> DI void glds16(v4u rs, unsigned voff, unsigned sb, unsigned lb) {
  unsigned t;
  asm volatile("s_add_u32 m0, %4, %6\n\ts_add_u32 %0, %3, %5\n\tbuffer_load_dwordx4 %1, %2, %0 offen lds"
               : "=&s"(t) : "v"(voff), "s"(rs), "s"(sb), "s"(lb), "i"(SI), "i"(LI) : "memory", "scc");
}
template <unsigned SI, unsigned LI> DI void glds16sc1(v4u rs, unsigned voff, unsigned lb) {
  unsigned t;
  asm volatile("s_add_u32 m0, %3, %5\n\ts_mov_b32 %0, %4\n\tbuffer_load_dwordx4 %1, %2, %0 offen sc1 lds"
               : "=&s"(t) : "v"(voff), "s"(rs), "s"(lb), "i"(SI), "i"(LI) : "memory", "scc");
}
template <class T> DI T ldsr(unsigned a) { return *(LAS T*)(uintptr_t)a; }
template <class T> DI void ldsw(unsigned a, T v) { *(LAS T*)(uintptr_t)a = v; }
DI s16x4 trread(unsigned a) { return __builtin_amdgcn_ds_read_tr16_b64_v4i16((LAS s16x4*)(uintptr_t)a); }
DI bf16x8 cat(s16x4 lo, s16x4 hi) { return __builtin_shufflevector(lo, hi, 0, 1, 2, 3, 4, 5, 6, 7); }
template <int N> DI void waitv() { asm volatile("s_waitcnt vmcnt(%0)" ::"n"(N) : "memory"); }
#define BAR() asm volatile("s_waitcnt lgkmcnt(0)\n\ts_barrier" ::: "memory")
#define LGKM0() __builtin_amdgcn_s_waitcnt(0xc07f)
#define PRI(n) do { if (PRIO == 2) { SB0(); __builtin_amdgcn_s_setprio(n); SB0(); } } while (0)
#define SB0() __builtin_amdgcn_sched_barrier(0)
#define PIN(x) asm volatile("" : "+v"(x))
#define SLPN 0
#define SLP() do { if (SLPN) __builtin_amdgcn_s_sleep(SLPN); } while (0)
#define SPIN_LOOP _Pragma("nounroll") for (int sp = 0; sp < SPIN_CAP; ++sp)
template <int N> DI float ror(float v) { return __builtin_bit_cast(float, __builtin_amdgcn_update_dpp(0, __builtin_bit_cast(int, v), 0x120 + N, 0xf, 0xf, false)); }
DI void sw16(float v, float& e, float& o) { unsigned u = __float_as_uint(v); auto r = __builtin_amdgcn_permlane16_swap(u, u, false, false); e = __uint_as_float(r[0]); o = __uint_as_float(r[1]); }
DI void sw32(float v, float& lo, float& hi) { unsigned u = __float_as_uint(v); auto r = __builtin_amdgcn_permlane32_swap(u, u, false, false); lo = __uint_as_float(r[0]); hi = __uint_as_float(r[1]); }
DI float sumq(float v) { float a, b; sw16(v, a, b); v = a + b; sw32(v, a, b); return a + b; }
DI float sumr(float v) { v += ror<8>(v); v += ror<4>(v); v += ror<2>(v); v += ror<1>(v); return v; }
DI void gatherq(float v, float (&a)[4]) { float e, o; sw16(v, e, o); sw32(e, a[0], a[2]); sw32(o, a[1], a[3]); }
typedef f32x2 cpx;
DI cpx cfma(cpx a, cpx b, cpx c) {
  cpx r;
  asm("v_pk_fma_f32 %0, %1, %2, %3 op_sel:[1,1,0] op_sel_hi:[1,0,1] neg_lo:[1,0,0]\n\tv_pk_fma_f32 %0, %1, %2, %0 op_sel_hi:[0,1,1]" : "=&v"(r) : "v"(a), "v"(b), "v"(c));
  return r;
}
DI cpx cmul(cpx a, cpx b) {
  cpx r;
  asm("v_pk_mul_f32 %0, %1, %2 op_sel:[1,1] op_sel_hi:[1,0] neg_lo:[1,0]\n\tv_pk_fma_f32 %0, %1, %2, %0 op_sel_hi:[0,1,1]" : "=&v"(r) : "v"(a), "v"(b));
  return r;
}
DI cpx csel(bool p, cpx a) { return cpx{p ? a.x : 1.f, p ? a.y : 0.f}; }
DI cpx czero(bool p, cpx a) { return cpx{p ? 0.f : a.x, p ? 0.f : a.y}; }
template <int V> struct IC { static constexpr int v = V; };

#define STAMP(k) do {} while (0)
#define CSTAMP(k) do {} while (0)
struct Ctx {
  const u16 *dsk, *x, *bre, *bim, *cre, *cim, *wg, *wu, *wd;
  char* ws;
  __amdgpu_buffer_rsrc_t rws;
  v2u tag;
  unsigned T, T2;
  int lane, w, wc, rb, cg, rt, ct, shr;
  f32x4 acc0[5];
  unsigned vw2, vh2, vfl;
  v4u wf[NWR][5], hf[HP][4];
  v2u fl;
  unsigned vfs;
  bool bump;
  unsigned long long pc;
};
DI v4u ld128(const Ctx& c, unsigned vo, unsigned so = 0) { return __builtin_amdgcn_raw_buffer_load_b128(c.rws, vo, so, 16); }
DI v2u ld64(const Ctx& c, unsigned vo) { return __builtin_amdgcn_raw_buffer_load_b64(c.rws, vo, 0, 16); }
DI void st128(const Ctx& c, v4u v, unsigned vo) { __builtin_amdgcn_raw_buffer_store_b128(v, c.rws, vo, 0, 16); }
DI void st_flag(const Ctx& c, unsigned vo) { __builtin_amdgcn_raw_buffer_store_b64(c.tag, c.rws, vo, 0, 16); }

DI float fexp(float x) {
  const float t = x * 1.44269502162933349609375f;
  const float r = fmaf(x, 1.925963033500011e-8f, fmaf(x, 1.44269502162933349609375f, -t));
  const float e = __builtin_amdgcn_exp2f(t);
  return fmaf(e, r * 0.693147182464599609375f, e);
}
DI void fsincos(float z, float& sn, float& cs) {
  const float k = rintf(z * 0.63661977236758134f);
  float r = fmaf(k, -1.5707963705062866f, z);
  r = fmaf(k, 4.3711388286737929e-08f, r);
  r = fmaf(k, 1.7151245100058819e-15f, r);
  const float r2 = r * r;
  const float sp = fmaf(r * r2, fmaf(r2, fmaf(r2, -1.9515295891e-4f, 8.3321608736e-3f), -1.6666654611e-1f), r);
  const float cp = fmaf(r2 * r2, fmaf(r2, fmaf(r2, 2.443315711809948e-5f, -1.388731625493765e-3f), 4.166664568298827e-2f), fmaf(r2, -0.5f, 1.f));
  const int n = (int)k;
  sn = (n & 1) ? cp : sp; cs = (n & 1) ? sp : cp;
  if (n & 2) sn = -sn;
  if ((n + 1) & 2) cs = -cs;
}
constexpr int LP(int ch) { return ch == 0 ? LP0 : (ch == 1 ? LP1 : LP2); }
constexpr int lds_chunk(int j) { return j == LP0 ? 0 : (j == LP1 ? 1 : (j == LP2 ? 2 : -1)); }
template <int J> DI void ldw(const Ctx& c, v4u (&d)[5]) {
  const unsigned vo = J == 11 ? (c.wc < 6 ? c.vw2 : OOB) : c.vw2;
#pragma unroll
  for (int a = 0; a < 4; ++a) d[a] = ld128(c, vo + a * 1024, J * 40960);
  d[4] = ld128(c, vo, J * 40960 + 4096);
}
template <int J> DI void ldh(const Ctx& c, v4u (&d)[4]) {
  const unsigned vo = J == 11 ? (c.wc < 6 ? c.vh2 : OOB) : c.vh2;
#pragma unroll
  for (int m = 0; m < 4; ++m) d[m] = ld128(c, vo ^ (m * 1024), J * 32768);
}

template <int TYPE> struct G1 {
  static constexpr int NU = TYPE == 0 ? 1 : 3;
  static constexpr int NG = TYPE == 0 ? 10 : 6;
  static constexpr int NR = TYPE == 0 ? 14 : 8;
  static constexpr int NP = (TYPE == 0 ? 7 : 6) - P6D;
  struct Frags { bf16x8 x[TYPE == 0 ? 4 : 2]; s16x4 wl[4], wh[4]; bf16x8 s[2 * NU]; };
  v4u rx, rw0, rw1, rs;
  unsigned vx0, vx1, vw, vs;
  unsigned sx, sw, ss, ss2;
  unsigned ax[2][2][2], aw[4], as[NU][2];
  f32x4 acc[4][4];
  f32x4 accs[2 * NU][2];
  f32x4 cs0, cs1, cs2, css[6];
  bf16x8 ones;
  int w;
  unsigned w1k, w4k, w1k6, vs6;

  DI void init_stage(const Ctx& c, int unit0) {
    const int l = c.lane;
    w = c.w; w1k = w * 1024; w4k = w * 4096;
    rx = mkrsrc(c.x, L_ * D_ * 2);
    const unsigned ch = (l & 7) ^ ((l >> 3) & 3);
    vx0 = (l >> 3) * 1280 + ch * 16;
    vx1 = (l >> 3) * 1280 + (ch ^ 4) * 16;
    sx = (c.rb * 256 + w * 32) * 1280;
    rs = mkrsrc((w & 2) ? c.bim : c.bre, 640 * 640 * 2);
    vs = (l >> 3) * 1280 + (((l & 7) ^ (l >> 3)) * 16);
    vs6 = w < 4 ? vs : OOB; w1k6 = w < 4 ? w1k : 65536;
    if (TYPE == 0) {
      rw0 = mkrsrc(c.wg, D_ * F_ * 2); rw1 = mkrsrc(c.wu, D_ * F_ * 2);
      const unsigned key2 = ((l >> 4) & 1) | ((w & 1) << 1);
      vw = (l >> 3) * 3456 + (((l & 7) ^ (key2 << 1)) * 16);
      sw = w * 8 * 3456 + c.cg * 128;
      ss2 = (unit0 * 16 + (w & 1) * 8) * 1280;
    } else {
      ss = (min(unit0 + (w >> 2), 39) * 16 + (w & 1) * 8) * 1280;
      ss2 = (min(unit0 + 2, 39) * 16 + (w & 1) * 8) * 1280;
    }
  }
  DI void init_read(const Ctx& c) {
    const int l = c.lane, i = l & 15, q = l >> 4;
    const int wt = w & 3, wn = w >> 2;
#pragma unroll
    for (int kk = 0; kk < 2; ++kk) {
      const unsigned ch = (((kk * 4 + q) ^ (i & 7)) * 16);
#pragma unroll
      for (int pr = 0; pr < (TYPE == 0 ? 2 : 1); ++pr) {
        ax[0][pr][kk] = (wt * 64 + (pr ^ wn) * 32 + 8 * (i >> 2) + (i & 3)) * 128 + ch;
        ax[1][pr][kk] = ax[0][pr][kk] + 65536;
        PIN(ax[0][pr][kk]); PIN(ax[1][pr][kk]);
      }
#pragma unroll
      for (int u = 0; u < 1; ++u) {
        as[u][kk] = (TYPE == 0 ? LWS(0) : LWB(2)) + i * 128 + ch;
        PIN(as[u][kk]);
      }
    }
    if (TYPE == 0) {
      const int r = (l >> 2) & 3, cc = l & 3;
      const unsigned key2 = ((r >> 1) & 1) | ((q & 1) << 1);
#pragma unroll
      for (int a = 0; a < 4; ++a) {
        const int act = a ^ wt;
        aw[a] = LWB(2) + (act >> 1) * 8192 + (8 * q + r) * 128 + (((2 * wn + (act & 1)) ^ key2) * 32) + cc * 8;
        PIN(aw[a]);
      }
      cs1 = f32x4{0.f, 0.f, 0.f, 0.f}; cs2 = f32x4{0.f, 0.f, 0.f, 0.f};
      PIN(cs1); PIN(cs2);
    }
    if (TYPE == 1) {
#pragma unroll
      for (int a = 0; a < 6; ++a) { css[a] = f32x4{0.f, 0.f, 0.f, 0.f}; PIN(css[a]); }
    }
    ones = bf16x8{0x3f80, 0x3f80, 0x3f80, 0x3f80, 0x3f80, 0x3f80, 0x3f80, 0x3f80};
    PIN(ones);
  }
  template <int P, int S> DI void piece() {
    constexpr int K = S % 3;
    if constexpr (P < 4) glds16<S * 128 + P * 10240, LX(K) + P * 1024>(rx, (P & 1) ? vx1 : vx0, sx, w4k);
    else if constexpr (P < 6) {
      if constexpr (TYPE == 0) glds16<S * (64 * 3456), LWB(K) + (P - 4) * 8192>(P == 4 ? rw0 : rw1, vw, sw, w1k);
      else if constexpr (P == 4) glds16<S * 128, LWB(K)>(rs, vs, ss, w1k);
    } else {
      if (w < 4)
      glds16<S * 128, (TYPE == 0 ? LWS(K) : LWB(K) + 8192)>(rs, vs6, ss2, w1k6);
    }
  }
  template <int S> DI void half0() { piece<6, S>(); piece<0, S>(); piece<4, S>(); piece<1, S>(); }
  template <int S> DI void half1() { piece<2, S>(); piece<5, S>(); piece<3, S>(); }

  template <int K, int KK, int R> DI void rd(Frags& f) {
    constexpr unsigned ow = LWB(K) - LWB(2), ox = K == 1 ? 32768 : 0;
    if constexpr (TYPE == 0) {
      if (R == 0) f.wl[0] = trread(aw[0] + (ow + KK * 4096));
      else if (R == 1) f.wh[0] = trread(aw[0] + (ow + KK * 4096 + 512));
      else if (R < 6) f.x[(R - 2) & 3] = ldsr<bf16x8>(ax[K == 2][((R - 2) >> 1) & 1][KK] + (ox + ((R - 2) & 1) * 512));
      else if (R < 12) {
        constexpr int a = ((R - 4) >> 1) & 3;
        if (R & 1) f.wh[a] = trread(aw[a] + (ow + KK * 4096 + 512)); else f.wl[a] = trread(aw[a] + (ow + KK * 4096));
      } else f.s[R & 1] = ldsr<bf16x8>(as[0][KK] + (K * 4096 + (R & 1) * 2048));
    } else {
      if (R == 1 || R == 2) f.x[(R - 1) & 1] = ldsr<bf16x8>(ax[K == 2][0][KK] + (ox + ((R - 1) & 1) * 512));
      else { constexpr int a = R == 0 ? 0 : R - 2; f.s[a] = ldsr<bf16x8>(as[0][KK] + (ow + a * 2048)); }
    }
  }
  template <bool Z, int M> DI void mm(const Frags& f) {
    if constexpr (TYPE == 0 && M < 16) mf<Z>(acc[M >> 2][M & 3], cat(f.wl[M >> 2], f.wh[M >> 2]), f.x[M & 3]);
    else { constexpr int a = (M - (TYPE == 0 ? 16 : 0)) >> 1; mf<Z>(accs[a][M & 1], f.x[M & 1], f.s[a]); }
  }
  template <int SUB, int G, class H> DI void group(const Frags& cur, Frags& nxt, H& hook) {
    constexpr int N = SUB + 1, K = (N >> 1) % 3, KK = N & 1;
    constexpr bool RD = N < 20;
    mm<SUB == 0, 2 * G>(cur);
    if constexpr (RD && 2 * G < NR) rd<K, KK, 2 * G>(nxt);
    mm<SUB == 0, 2 * G + 1>(cur);
    if constexpr (RD && 2 * G + 1 < NR) rd<K, KK, 2 * G + 1>(nxt);
    if constexpr (TYPE == 0) {
      if (G == 0) mf<SUB == 0>(cs0, ones, cat(cur.wl[0], cur.wh[0]));
      if (G == 2) { if (__builtin_expect(w == (SUB & 7), 0)) { mfa(cs1, ones, cur.s[0]); mfa(cs2, ones, cur.s[1]); } }
    } else {
      if (G == 0) {
        if (__builtin_expect(w == (SUB & 7), 0)) {
#pragma unroll
          for (int a = 0; a < 6; ++a) mfa(css[a], ones, cur.s[a]);
        }
      }
    }
    hook(IC<G>{});
    SB0();
    if constexpr (G + 1 < NG) group<SUB, G + 1>(cur, nxt, hook);
  }
  template <int SUB, class H> DI void part(const Frags& cur, Frags& nxt, H hook) { group<SUB, 0>(cur, nxt, hook); }
  template <int R> DI void rdall(Frags& f) { rd<0, 0, R>(f); if constexpr (R + 1 < NR) rdall<R + 1>(f); }
};

template <int NU> struct Side {
  unsigned xr[5], pa[NU], pb[NU], pd[NU];
  float v[10], s, e;
  unsigned a_mu, a_cs, v_hg, v_fg, a_st, a_css, a_aw, a_ar, v_pub, v_lk0, v_lk1, v_hs[NU], v_fs, a_tw, a_tr, v_x0, v_x1, a_bs, a_bx, a_p8, a_w0, a_w1, a_k0, a_k1;
  static constexpr int NCH = 8 + 8;
  DI void table(const Ctx& c, int u) const {
    if (c.w >= 6) return;
    const int k = threadIdx.x >> 4;
    const float fn = (float)(k < 7 ? 8 * k - 24 : (k < 16 ? 32 * k - 256 : (k < 23 ? 256 * k - 4096 : 1)));
    const float dt = rbf(fexp(bf2f(pd[u])));
    const float ea = dt * bf2f(pa[u]) * 1.44269504088896341f, eb = dt * bf2f(pb[u]) * 0.15915494309189535f;
    const float m = fn < 0.f ? 0.f : __builtin_amdgcn_exp2f(fn * ea);
    const float r = __builtin_amdgcn_fractf(fn * eb);
    ldsw<f32x2>(L_POW(NU == 1 ? 0 : 1, u) + threadIdx.x * 8, f32x2{m * __builtin_amdgcn_cosf(r), m * __builtin_amdgcn_sinf(r)});
  }
  template <int N> DI void tick(Ctx& c, int srow, int unit0) {
    const int l = c.lane, i = l & 15, q = l >> 4, w = c.w, wt = w & 3, wn = w >> 2, grp = wt * 2 + wn;
    if constexpr (N == 0) {
#pragma unroll
      for (int j = 0; j < 5; ++j) { v[2 * j] = bf2f(xr[j]); v[2 * j + 1] = bfhi(xr[j]); PIN(v[2 * j]); PIN(v[2 * j + 1]); }
    } else if constexpr (N == 1) {
      s = ((v[0] + v[1]) + (v[2] + v[3])) + ((v[4] + v[5]) + (v[6] + v[7])) + (v[8] + v[9]); PIN(s);
    } else if constexpr (N == 2) { s = sumr(s); PIN(s); }
    else if constexpr (N == 3) { s = sumq(s) * (1.f / 640.f); PIN(s); }
    else if constexpr (N == 4) {
#pragma unroll
      for (int j = 0; j < 10; ++j) { v[j] -= s; PIN(v[j]); }
    } else if constexpr (N == 5) {
      e = v[0] * v[0];
#pragma unroll
      for (int j = 1; j < 10; ++j) e = fmaf(v[j], v[j], e);
      PIN(e);
    } else if constexpr (N == 6) { e = sumr(e); PIN(e); }
    else if constexpr (N == 7) {
      const float var = rbf(sumq(e) * (1.f / 640.f));
      const float sd = rbf(__builtin_amdgcn_sqrtf(rbf(var + 1e-5f)));
      float rc = __builtin_amdgcn_rcpf(sd);
      rc = fmaf(fmaf(-sd, rc, 1.f), rc, rc);
      st128(c, v4u{__float_as_uint(rbf(s)), c.T, __float_as_uint(rc), c.T2}, WS_STAT + srow * 16);
    } else if constexpr (N < 11) {
      const int k0 = c.rb - 1 - q;
      if constexpr (N == 8) {
        a_css = (NU == 1 ? L_CSS : L_CS3) + q * 128 + i * 4;
        a_ar = L_AGG + i * 64 + q * 16;
        a_aw = a_ar + grp * 8 - 48;
#pragma unroll
        for (int u = 0; u < NU; ++u) { v_hs[u] = (unit0 + u < 40 ? WS_H + (c.rb * 4 + wt) * H_RT + ustep(unit0 + u) * 4096 + wn * 2048 : OOB) + l * 16; PIN(v_hs[u]); }
        PIN(a_css);
        PIN(a_ar); PIN(a_aw);
      } else if constexpr (N == 9) {
        const unsigned po = (WS_AGG + unit0 * 256) + i * 16;
        v_pub = q == 3 ? po + c.rb * AGG_RB : OOB;
        v_lk0 = k0 >= 0 ? po + q * AGG_RB : OOB;
        v_lk1 = k0 >= 4 ? po + (q + 4) * AGG_RB : OOB;
        PIN(v_pub); PIN(v_lk0); PIN(v_lk1);
      } else {
        const unsigned tb = L_POW(NU == 1 ? 0 : 1, 2) + i * 8;
        a_p8 = tb + q * 128;
        a_w0 = tb + (8 + max(grp - 1 - 2 * q, -1)) * 128;
        a_w1 = tb + (8 + max(grp - 2 - 2 * q, -1)) * 128;
        a_k0 = tb + (16 + max(k0, 0)) * 128;
        a_k1 = tb + (16 + max(k0 - 4, 0)) * 128;
        PIN(a_p8); PIN(a_w0); PIN(a_w1); PIN(a_k0); PIN(a_k1);
      }
    } else {
      constexpr int k = N - 11;
      const unsigned fbase = WS_FLAGH + (c.rb * 4 + wt) * 2048;
      const unsigned r8 = ((l & 12) << 4) | ((l & 3) << 3);
      if constexpr (k == 0) {
        if (NU == 1) {
          const int step = 2 * c.cg + wn;
          a_mu = (L_ST + wt * 512 + wn * 256) + r8;
          a_cs = (L_CS + wn * 256 + wt * 64) + q * 16;
          v_hg = (WS_H + (c.rb * 4 + wt) * H_RT + step * 4096 + wn * 2048) + l * 16;
          v_fg = (fbase + (step & 7) * 256 + (step >> 3) * 16) + (l & 1) * 8;
          PIN(a_mu); PIN(a_cs); PIN(v_hg); PIN(v_fg);
        }
      } else if constexpr (k == 1) {
        const int step = ustep(NU == 1 ? unit0 : min(unit0 + min(l, 2), 39));
        v_fs = fbase + (step & 7) * 256 + (step >> 3) * 16 + wn * 8;
        a_st = (L_ST + grp * 256) + q * 64;
        a_bs = (L_ST + c.rt * 512 + (wt >> 1) * 256 + (wt & 1) * 32) + r8;
        a_bx = ((wt >> 1) * 5120 + (wt & 1) * 640) + r8 * 20 + q * 8;
        PIN(v_fs); PIN(a_st); PIN(a_bs); PIN(a_bx);
      } else if constexpr (k == 2) {
        const unsigned tr = LWB(2) + 8192 + w * 1024;
        a_tw = tr + i * 64 + ((q ^ (i >> 1)) & 3) * 16;
        a_tr = tr + (l >> 2) * 64 + ((l ^ (l >> 3)) & 3) * 16;
        PIN(a_tw); PIN(a_tr);
      } else if constexpr (k == 3) {
        c.vh2 = (WS_H + (c.rb * 4 + c.rt) * H_RT + c.wc * 4096 + wt * 1024) + l * 16;
        c.vfl = (WS_FLAGH + (c.rb * 4 + c.rt) * 2048 + c.wc * 256) + (l & 31) * 8;
        c.vw2 = (WS_W2T + c.ct * W2T_CT + c.wc * 5120) + l * 16;
        PIN(c.vh2); PIN(c.vfl); PIN(c.vw2);
      } else {
        const unsigned c0 = w * 64 + l, r0 = (c0 * 6554) >> 16, c1 = c0 + 512, r1 = (c1 * 6554) >> 16, xb = (c.rb * 256 + c.rt * 64) * 1280 + c.ct * 160;
        v_x0 = xb + r0 * 1120 + c0 * 16;
        v_x1 = w < 2 ? xb + r1 * 1120 + c1 * 16 : c.ct * 160 + l * 16;
        PIN(v_x0); PIN(v_x1);
      }
    }
  }
};

template <int TYPE> DI void phase1(Ctx& c, const u16* lre, const u16* lim, const u16* ldt) {
  const int l = c.lane, i = l & 15, q = l >> 4, w = c.w, wt = w & 3, wn = w >> 2;
  const int rb = c.rb, cg = c.cg;
  constexpr int NU = G1<TYPE>::NU, NP = G1<TYPE>::NP;
  G1<TYPE> g;
  Side<NU> sd;
  const int srow = rb * 256 + cg * 8 + w;
  const int si = rb * 5 + (cg - 27), sj = si, mi = rb * 27 + cg;
  const int unit0 = TYPE == 0 ? cg : 27 + 3 * (cg - 27);
  __builtin_amdgcn_s_waitcnt(0x0070);
  unsigned cpf;
  {
    const unsigned n = ((blockIdx.x >> 3) & (CPS - 1)) * (512 / CPS) + (threadIdx.x & (512 / CPS - 1));
    if (w < 8 / CPS) asm volatile("global_load_dword %0, %1, %2" : "=v"(cpf) : "v"(min(n * 128u, (unsigned)CPF)), "s"(c.pc) : "memory");
  }
  STAMP(56);
  unsigned ep, qa, qb, qd;
  {
    const u16* xrow = c.x + (size_t)srow * D_;
    asm volatile(
        "s_nop 4\n\tglobal_load_dword %5, %8, %9 sc1\n\t"
        "global_load_dword %0, %6, %7\n\t"
        "global_load_dword %1, %6, %7 offset:256\n\t"
        "global_load_dword %2, %6, %7 offset:512\n\t"
        "global_load_dword %3, %6, %7 offset:768\n\t"
        "global_load_dword %4, %6, %7 offset:1024"
        : "=&v"(sd.xr[0]), "=&v"(sd.xr[1]), "=&v"(sd.xr[2]), "=&v"(sd.xr[3]), "=&v"(sd.xr[4]), "=&v"(ep)
        : "v"(l * 4), "s"(xrow), "v"(0), "s"(c.ws)
        : "memory");
    if (TYPE == 1)
      asm volatile("s_nop 4\n\tglobal_load_ushort %0, %3, %4\n\tglobal_load_ushort %1, %3, %5\n\tglobal_load_ushort %2, %3, %6"
                   : "=&v"(qa), "=&v"(qb), "=&v"(qd) : "v"(((sj >> 2) * 64 + l) * 2), "s"(lre), "s"(lim), "s"(ldt) : "memory");
  }
  auto params = [&]() {
#pragma unroll
    for (int u = 0; u < NU; ++u) {
      const int ua = min(unit0 + u, 39);
      asm volatile("s_nop 4\n\tglobal_load_ushort %0, %3, %4\n\tglobal_load_ushort %1, %3, %5\n\tglobal_load_ushort %2, %3, %6"
                   : "=&v"(sd.pa[u]), "=&v"(sd.pb[u]), "=&v"(sd.pd[u]) : "v"((ua * 16 + i) * 2), "s"(lre), "s"(lim), "s"(ldt) : "memory");
    }
  };
  auto ticks = [&](auto self, auto N, auto E) { if constexpr (N.v < E.v) { sd.template tick<N.v>(c, srow, unit0); self(self, IC<N.v + 1>{}, E); } };
  g.init_stage(c, unit0);
  STAMP(57);
  unsigned wflag = OOB, ja = 0, jb = 0, jc = 0;
  if (TYPE == 0) {
    params();
    if (wn == 0) { g.template half0<0>(); g.template half1<0>(); }
    STAMP(58);
    g.init_read(c);
    ja = (l >> 2) * 320 + w * 32 + (l & 3) * 8;
    jb = ja + 5120; jc = ja + 5376;
    ja += LWB(2) + 8192; jb += jb < 8192 ? LWB(2) + 8192 : LWS(2) - 8192; jc += jc < 8192 ? LWB(2) + 8192 : LWS(2) - 8192;
    PIN(ja); PIN(jb); PIN(jc);
    ticks(ticks, IC<8>{}, IC<Side<NU>::NCH>{});
    STAMP(59);
    if (wn != 0) { g.template half0<0>(); g.template half1<0>(); }
    const v4u rj = mkrsrc(c.wd, F_ * D_ * 2);
    const unsigned c0 = w * 64 + l, r0 = (c0 * 3277) >> 16, c1 = c0 + 512, r1 = (c1 * 3277) >> 16;
    glds16<0, LWB(2) + 8192>(rj, r0 * 960 + c0 * 16, (mi >> 2) * 40960 + (mi & 3) * 320, g.w1k);
    if (w < 2) glds16<0, LWS(2)>(rj, r1 * 960 + c1 * 16, (mi >> 2) * 40960 + (mi & 3) * 320, g.w1k);
    STAMP(60);
  } else {
    params();
    auto req = [&]() {
      const v4u rj = mkrsrc(wn ? c.cim : c.cre, 640 * 640 * 2);
      const unsigned r0 = wt * 40 + (l >> 3);
#pragma unroll
      for (int j = 0; j < 5; ++j) {
        const unsigned row = r0 + j * 8;
        glds16<0, LX(2)>(rj, row * 1280 + (((l & 7) ^ ((row >> 1) & 7)) * 16), (sj & 3) * 160 * 1280 + (sj >> 2) * 128, (w * 5 + j) * 1024);
      }
    };
    if (wn == 0) req();
    g.init_read(c);
    ticks(ticks, IC<8>{}, IC<Side<NU>::NCH>{});
    if (wn != 0) req();
    g.template half0<0>(); g.template half1<0>();
  }
  asm volatile("s_waitcnt vmcnt(%6)" : "+v"(sd.xr[0]), "+v"(sd.xr[1]), "+v"(sd.xr[2]), "+v"(sd.xr[3]), "+v"(sd.xr[4]), "+v"(ep) : "n"(TYPE == 0 ? 7 : 10) : "memory");
  STAMP(1);
  asm volatile("" ::"v"(cpf));
  c.T = __builtin_amdgcn_readfirstlane(ep) + 1u;
  c.T2 = (c.T * 0x9E3779B1u) ^ 0x5bd1e995u;
  c.tag = v2u{c.T, c.T2};
  PIN(c.tag);
  typename G1<TYPE>::Frags fa, fb;
  if (TYPE == 0) {
    asm volatile("" : "+v"(sd.pa[0]), "+v"(sd.pb[0]), "+v"(sd.pd[0]));
    ticks(ticks, IC<0>{}, IC<(PRE < 4 ? PRE : 4)>{});
    g.template half0<1>();
    STAMP(61);
    ticks(ticks, IC<4>{}, IC<(PRE < 7 ? PRE : 7)>{});
    g.template half1<1>();
    ticks(ticks, IC<7>{}, IC<PRE>{});
    STAMP(62);
    waitv<6 + (PRE >= 8)>();
    STAMP(63);
    BAR();
    g.template rdall<0>(fa);
  } else {
    asm volatile("" : "+v"(qa), "+v"(qb), "+v"(qd));
    if (w == 7) {
      const float ar = bf2f(qa), ai = bf2f(qb), dt = rbf(fexp(bf2f(qd))), e = fexp(dt * ar);
      float sn, cs;
      fsincos(dt * ai, sn, cs);
      const float inv = 2.f / (ar * ar + ai * ai);
      ldsw<f32x2>(L_COEF + l * 8, cmul(cpx{e * cs - 1.f, e * sn}, cpx{ar * inv, -ai * inv}));
    }
#pragma unroll
    for (int u = 0; u < NU; ++u) { asm volatile("" : "+v"(sd.pa[u]), "+v"(sd.pb[u]), "+v"(sd.pd[u])); sd.table(c, u); }
    g.template half0<1>(); g.template half1<1>();
    waitv<10>();
    BAR();
    const f32x4 k0 = ldsr<f32x4>(L_COEF + (16 * wt + 4 * q) * 8), k1 = ldsr<f32x4>(L_COEF + (16 * wt + 4 * q) * 8 + 16);
    const cpx kc[4] = {{k0.x, k0.y}, {k0.z, k0.w}, {k1.x, k1.y}, {k1.z, k1.w}};
    const int step = ustep(4 * (sj >> 2) + wt), ct = 2 * (sj & 3) + wn;
#pragma unroll
    for (int j = 0; j < 5; ++j) {
      const unsigned row = (wn * 5 + j) * 16 + i;
      const unsigned a = LX(2) + row * 128 + (((2 * wt + (q >> 1)) ^ ((row >> 1) & 7)) * 16) + (q & 1) * 8;
      const v2u cr = ldsr<v2u>(a), ci = ldsr<v2u>(a + 20480);
      const unsigned cr0 = cr.x, cr1 = cr.y, ci0 = ci.x, ci1 = ci.y;
      const cpx p0 = cmul(kc[0], cpx{bf2f(cr0), bf2f(ci0)}), p1 = cmul(kc[1], cpx{bfhi(cr0), bfhi(ci0)});
      const cpx p2 = cmul(kc[2], cpx{bf2f(cr1), bf2f(ci1)}), p3 = cmul(kc[3], cpx{bfhi(cr1), bfhi(ci1)});
      const v4u o = {pk2(p0.x, p1.x), pk2(p2.x, p3.x), pk2(-p0.y, -p1.y), pk2(-p2.y, -p3.y)};
      st128(c, o, l * 16 + j * 1024 + (WS_W2T + ct * W2T_CT + step * 5120));
    }
    wflag = WS_FLAGW + (ct * 8 + (step & 7)) * 128 + (step >> 3) * 8;
    SB0();
    waitv<10>();
    BAR();
    g.template rdall<0>(fa);
  }
  STAMP(2); CSTAMP(0);
  SB0();
  v4u sg; v2u fw; unsigned pfd = 0;
  bf16x8 jv0, jv1;
  const v4u rwsv = mkrsrc(c.ws, WS_END);
  auto w2l = [&](auto CH, auto A) { glds16sc1<LP(CH.v) * 40960 + A.v * 1024, tile_base(CH.v, A.v)>(rwsv, c.vw2, A.v < 4 ? g.w4k : g.w1k); };
  auto side = [&](auto SUB, auto G) {
    constexpr int k = TYPE == 0 ? (G.v >= 10 - TPP ? G.v - (10 - TPP) : -1) : G.v - 4;
    constexpr int n = TYPE == 0 ? PRE + SUB.v * TPP + k : SUB.v * 2 + k;
    if constexpr (k >= 0 && n < 8) sd.template tick<n>(c, srow, unit0);
    if constexpr (TYPE == 0 && SUB.v == 17 && G.v == 8) sd.table(c, 0);
    if constexpr (SUB.v >= 20 - LW) {
      constexpr int J = SUB.v - (20 - LW);
      constexpr int a = TYPE == 0 ? (G.v == 0 ? 0 : (G.v == 2 ? 1 : (G.v == 3 ? 2 : (G.v == 5 ? 3 : (G.v == 7 ? 4 : -1))))) : G.v - 1;
      if constexpr (a >= 0) c.wf[J][a] = a < 4 ? ld128(c, c.vw2 + a * 1024, J * 40960) : ld128(c, c.vw2, J * 40960 + 4096);
    }
  };
  constexpr int GA = TYPE == 0 ? 4 : 2, GB = TYPE == 0 ? 6 : 4;
  auto step = [&](auto SS) {
    constexpr int S = SS.v;
    LGKM0();
    g.template part<2 * S>(fa, fb, [&](auto G) {
      if constexpr (S == 0) {
        if constexpr (TYPE == 0) {
          if (G.v == 7) { jv0 = cat(trread(ja), trread(jb)); jv1 = cat(trread(ja + 256), trread(jc)); }
        }
        if (G.v == 0) g.template piece<0, 2>();
        if (G.v == 1) g.template piece<4, 2>();
        if (G.v == 2) g.template piece<1, 2>();
        if (G.v == 3) g.template piece<2, 2>();
        if (G.v == 4) g.template piece<3, 2>();
      } else if constexpr (S + 2 < 10) {
        if (G.v == 1) g.template piece<2, S + 2>();
        if (G.v == GA) g.template piece<5, S + 2>();
        if (G.v == GB) g.template piece<3, S + 2>();
      } else {
        if (G.v == 1) w2l(IC<S - 8>{}, IC<3>{});
        if (G.v == GB) w2l(IC<S - 8>{}, IC<4>{});
      }
      side(IC<2 * S>{}, G);
    });
    if constexpr (S < 9) {
      if (S == SCHK) asm volatile("s_waitcnt vmcnt(%2)" : "+v"(sg), "+v"(fw) : "n"(NP) : "memory");
      else if (S == 8) waitv<5>();
      else if (S == 0) waitv<TYPE == 0 ? 5 : 10>();
      else if (S == JS + 1 && TYPE == 0) waitv<NP + 1>();
      else waitv<NP>();
      LGKM0();
      asm volatile("s_barrier" ::: "memory");
    } else LGKM0();
    if (S == 0) { g.template piece<6, 2>(); g.template piece<5, 2>(); }
    if (S == JS && TYPE == 0) {
      const unsigned jo = WS_W2T + (mi & 3) * 2 * W2T_CT + (mi >> 2) * 5120;
      st128(c, __builtin_bit_cast(v4u, jv0), (jo + (w < 5 ? w * 1024 : W2T_CT + (w - 5) * 1024)) + l * 16);
      if (w < 2) st128(c, __builtin_bit_cast(v4u, jv1), (jo + W2T_CT + (3 + w) * 1024) + l * 16);
    }
    if (S == JS + 2 && TYPE == 0) { if (w == 0) st_flag(c, (WS_FLAGW + ((mi & 3) * 16 + ((mi >> 2) & 7)) * 128 + (mi >> 5) * 8) + (l & 1) * 1024); }
    if (S == 1 && TYPE == 1) st_flag(c, wflag);
    if (S == SCHK - 1) {
      const GAS char* sp = (const GAS char*)c.ws + WS_STAT + rb * 4096;
      const GAS char* fp = (const GAS char*)c.ws + WS_FLAGW + (c.ct * 8 + c.wc) * 128;
      asm volatile("s_nop 4\n\tglobal_load_dwordx4 %0, %2, %3 sc1\n\tglobal_load_dwordx2 %1, %4, %5 sc1"
                   : "=&v"(sg), "=&v"(fw) : "v"(wt * 1024 + l * 16), "s"(sp), "v"(i * 8), "s"(fp) : "memory");
    }
    if (S == SCHK) {
      const bool need = i < 12 && c.wc + 8 * i < NS2;
      SPIN_LOOP {
        if (__all((!need || (fw.x == c.T && fw.y == c.T2)) && sg.y == c.T && sg.w == c.T2)) break;
        __builtin_amdgcn_s_sleep(1);
        asm volatile("" ::: "memory");
        sg = ld128(c, WS_STAT + rb * 4096 + wt * 1024 + l * 16);
        fw = ld64(c, WS_FLAGW + (c.ct * 8 + c.wc) * 128 + i * 8);
      }
      ldsw<v2u>(L_ST + (wt * 64 + l) * 8, v2u{sg.x, sg.z});
    }
    if (S == 7) {
      glds16<0, L_XT0>(g.rx, sd.v_x0, 0, g.w1k);
      if (w < 3) glds16<0, L_XT1>(w == 2 ? mkrsrc(c.dsk, 1280) : g.rx, sd.v_x1, 0, g.w1k);
    }
    SB0();
    g.template part<2 * S + 1>(fb, fa, [&](auto G) {
      if constexpr (false) {
      } else if constexpr (S + 3 < 10) {
        if (G.v == 0) g.template piece<6, S + 3>();
        if (G.v == (TYPE == 0 ? 2 : 1)) g.template piece<0, S + 3>();
        if (G.v == GA) g.template piece<4, S + 3>();
        if (G.v == GB) g.template piece<1, S + 3>();
      } else if constexpr (S < 9) {
        if (G.v == 1) w2l(IC<S - 7>{}, IC<0>{});
        if (G.v == GA) w2l(IC<S - 7>{}, IC<1>{});
        if (G.v == GB) w2l(IC<S - 7>{}, IC<2>{});
      } else {
        if (G.v == G1<TYPE>::NG - 1) {
          if (TYPE == 0) ldsw<float>(L_CS + ((wn * 4 + wt) * 16 + i) * 4, g.cs0.x);
          if (TYPE == 0) { const unsigned ca = L_CSS + w * 128 + i * 4; ldsw<float>(ca, g.cs1.x); ldsw<float>(ca + 64, g.cs2.x); }
          else {
            const unsigned ca = L_CS3 + w * 128 + i * 4;
#pragma unroll
            for (int a = 0; a < 6; ++a) ldsw<float>(ca + (a >> 1) * 1024 + (a & 1) * 64, g.css[a].x);
          }
        }
      }
      side(IC<2 * S + 1>{}, G);
    });
  };
  step(IC<0>{}); step(IC<1>{}); step(IC<2>{}); step(IC<3>{}); step(IC<4>{}); step(IC<5>{}); step(IC<6>{});
  CSTAMP(1); STAMP(9);
  step(IC<7>{}); step(IC<8>{}); step(IC<9>{});
  asm volatile("" ::"v"(pfd));
  MFMA_DRAIN();
  BAR();
  SB0();
  STAMP(3);
  auto w2c2 = [&]() { w2l(IC<2>{}, IC<0>{}); w2l(IC<2>{}, IC<1>{}); w2l(IC<2>{}, IC<2>{}); w2l(IC<2>{}, IC<3>{}); w2l(IC<2>{}, IC<4>{}); };
  __builtin_amdgcn_s_setprio(3);
  if (TYPE == 0) {
    f32x4 cs[4];
    f32x2 mu[4];
#pragma unroll
    for (int a = 0; a < 4; ++a) cs[a] = ldsr<f32x4>(sd.a_cs ^ (a * 64));
#pragma unroll
    for (int m = 0; m < 4; ++m) mu[m] = ldsr<f32x2>((m < 2 ? sd.a_mu : (sd.a_mu ^ 256)) + (m & 1) * 32);
    auto glu = [&](auto ODD) {
#pragma unroll
      for (int m = 0; m < 4; ++m) {
        const f32x4 mv = {-mu[m].x, -mu[m].x, -mu[m].x, -mu[m].x};
        const float r2 = mu[m].y * mu[m].y;
        const f32x4 h0 = __builtin_elementwise_fma(mv, cs[ODD.v], g.acc[ODD.v][m]) * __builtin_elementwise_fma(mv, cs[2 + ODD.v], g.acc[2 + ODD.v][m]) * r2;
        const f32x4 h1 = __builtin_elementwise_fma(mv, cs[1 - ODD.v], g.acc[1 - ODD.v][m]) * __builtin_elementwise_fma(mv, cs[3 - ODD.v], g.acc[3 - ODD.v][m]) * r2;
        st128(c, v4u{pk2(h0[0], h0[1]), pk2(h0[2], h0[3]), pk2(h1[0], h1[1]), pk2(h1[2], h1[3])}, (m < 2 ? sd.v_hg : (sd.v_hg ^ 2048)) + (m & 1) * 1024);
        asm volatile("" ::: "memory");
        if (IL >= 1) {
          if (m == 0) { w2l(IC<2>{}, IC<0>{}); w2l(IC<2>{}, IC<1>{}); }
          if (m == 1) { w2l(IC<2>{}, IC<2>{}); w2l(IC<2>{}, IC<3>{}); }
          if (m == 2) { w2l(IC<2>{}, IC<4>{}); }
        }
      }
    };
    if (w & 1) glu(IC<1>{}); else glu(IC<0>{});

    SB0();
  }
  if (TYPE == 1) { w2c2(); if (SA > 0 && LW < 1) ldw<0>(c, c.wf[0]); if (SA > 1 && LW < 2) ldw<1>(c, c.wf[1]); SB0(); }
  cpx v[NU][8], eq[NU], tot[NU], lam[NU], L32[NU], f1[NU], W0[NU], W1[NU], wk0[NU], wk1[NU];
  {
    f32x4 st[4];
#pragma unroll
    for (int e = 0; e < 4; ++e) st[e] = ldsr<f32x4>(sd.a_st + e * 16);
    const unsigned a_pi = sd.a_p8 & ~384u;
#pragma unroll
    for (int u = 0; u < NU; ++u) {
      const unsigned to = L_POW(TYPE, u) - L_POW(TYPE, 2);
      lam[u] = ldsr<cpx>(a_pi + to + 23 * 128);
      const cpx L8 = ldsr<cpx>(a_pi + to + 4 * 128), u2 = ldsr<cpx>(sd.a_p8 + to), u1 = ldsr<cpx>(sd.a_p8 + to + 128), u0 = ldsr<cpx>(sd.a_p8 + to + 256);
      f1[u] = ldsr<cpx>(sd.a_p8 + to + 384); L32[u] = ldsr<cpx>(a_pi + to + 9 * 128);
      W0[u] = ldsr<cpx>(sd.a_w0 + to); W1[u] = ldsr<cpx>(sd.a_w1 + to);
      const cpx lg = ldsr<cpx>(a_pi + (to + 8 * 128) + (wt * 2 + wn) * 128);
      wk0[u] = ldsr<cpx>(sd.a_k0 + to); wk1[u] = ldsr<cpx>(sd.a_k1 + to);
      const unsigned ac = sd.a_css + u * 1024;
      const float csx = sumq(ldsr<float>(ac) + ldsr<float>(ac + 512)), csy = sumq(ldsr<float>(ac + 64) + ldsr<float>(ac + 576));
#pragma unroll
      for (int j = 0; j < 8; ++j) {
        const float mu = (j & 1) ? st[j >> 1].z : st[j >> 1].x, rs = (j & 1) ? st[j >> 1].w : st[j >> 1].y;
        float a = fmaf(-mu, csx, g.accs[2 * u][j >> 2][j & 3]), b = fmaf(-mu, csy, g.accs[2 * u + 1][j >> 2][j & 3]);
        asm("" : "+v"(a), "+v"(b));
        v[u][j] = cpx{a * rs, b * rs};
        asm("" : "+v"(v[u][j]));
      }
      cpx h = v[u][0];
#pragma unroll
      for (int j = 1; j < 8; ++j) h = cfma(lam[u], h, v[u][j]);
      float ax[4], ay[4];
      gatherq(h.x, ax); gatherq(h.y, ay);
      eq[u] = cfma(u0, cpx{ax[0], ay[0]}, cfma(u1, cpx{ax[1], ay[1]}, cpx{u2.x * ax[2], u2.x * ay[2]}));
      tot[u] = cfma(L8, eq[u], h);
      if (l >= 48) ldsw<f32x2>(sd.a_aw + u * 1024, tot[u]);
      wk0[u] = cmul(wk0[u], lg); wk1[u] = cmul(wk1[u], lg);
    }
  }
  SB0();
  BAR();
  cpx pm[NU];
#pragma unroll
  for (int u = 0; u < NU; ++u) {
    const f32x4 t = ldsr<f32x4>(sd.a_ar + u * 1024);
    pm[u] = cfma(W0[u], cpx{t.x, t.y}, cmul(W1[u], cpx{t.z, t.w}));
  }
  if (w == 7) {
#pragma unroll
    for (int u = 0; u < NU; ++u) {
      const cpx P = cfma(L32[u], cpx{sumq(pm[u].x), sumq(pm[u].y)}, tot[u]);
      st128(c, v4u{__float_as_uint(P.x), c.T, __float_as_uint(P.y), c.T2}, sd.v_pub + u * 256);
    }
  }
  SB0();
  if (TYPE == 0) {
    if (w == 7) waitv<1>(); else waitv<0>();
    st_flag(c, sd.v_fg);
    STAMP(4);
  }
  asm volatile("s_barrier" ::: "memory");
  auto ldwr = [&](auto A, auto B) {
    if constexpr (A.v <= 0 && 0 < B.v && LW <= 0) ldw<0>(c, c.wf[0]);
    if constexpr (A.v <= 1 && 1 < B.v && LW <= 1) ldw<1>(c, c.wf[1]);
    if constexpr (A.v <= 2 && 2 < B.v && LW <= 2) ldw<2>(c, c.wf[2]);
    if constexpr (A.v <= 3 && 3 < B.v) ldw<3>(c, c.wf[3]);
    SB0();
  };
  PRI(2);
  if (TYPE == 0) { if (IL == 0) w2c2(); ldwr(IC<0>{}, IC<NA>{}); }
  PRI(3);
  SB0();
  {
    const f32x2 st = ldsr<f32x2>(sd.a_bs);
    auto item = [&](int a) {
      const unsigned xo = sd.a_bx + a * 32;
      const v2u xv = ldsr<v2u>(xo < 8192 ? L_XT0 + xo : L_XT1 - 8192 + xo), dv = ldsr<v2u>(L_XT1 + 2048 + q * 8 + a * 32);
      const unsigned x0 = xv.x, x1 = xv.y, d0 = dv.x, d1 = dv.y;
      const float xf[4] = {bf2f(x0), bfhi(x0), bf2f(x1), bfhi(x1)}, df[4] = {bf2f(d0), bfhi(d0), bf2f(d1), bfhi(d1)};
      f32x4 r;
#pragma unroll
      for (int e = 0; e < 4; ++e) {
        float xn = rbf(rbf(xf[e] - st.x) * st.y);
        asm("" : "+v"(xn));
        r[e] = xf[e] + rbf(xn * df[e]);
      }
      return r;
    };
    const f32x4 z = {0.f, 0.f, 0.f, 0.f};
    if (wn == 0) { c.acc0[0] = item(0); c.acc0[1] = item(1); c.acc0[2] = item(2); c.acc0[3] = z; c.acc0[4] = z; }
    else { c.acc0[0] = z; c.acc0[1] = z; c.acc0[2] = z; c.acc0[3] = item(3); c.acc0[4] = item(4); }
#pragma unroll
    for (int a = 0; a < 5; ++a) PIN(c.acc0[a]);
    SB0();
  }
  if (NOJB) BAR();
  v4u lk0[NU], lk1[NU];
#pragma unroll
  for (int u = 0; u < NU; ++u) { lk0[u] = ld128(c, sd.v_lk0 + u * 256); lk1[u] = ld128(c, sd.v_lk1 + u * 256); }
  if (TYPE == 0) c.fl = ld64(c, c.vfl);
  PRI(1);
  if (TYPE == 0) ldwr(IC<NA>{}, IC<NA + NB>{}); else ldwr(IC<SA>{}, IC<4>{});
  PRI(3);
  auto hwait = [&]() {
    if ((__ballot(c.fl.x == c.T && c.fl.y == c.T2) & HM) != HM) {
      SPIN_LOOP {
        asm volatile("" ::: "memory");
        c.fl = ld64(c, c.vfl);
        if ((__ballot(c.fl.x == c.T && c.fl.y == c.T2) & HM) == HM) break;
      }
    }
    __builtin_amdgcn_fence(__ATOMIC_ACQUIRE, "wavefront");
    PRI(1);
    if (HF) c.fl = ld64(c, c.vfl);
    ldh<0>(c, c.hf[0]); ldh<1>(c, c.hf[1]);
    if constexpr (HP > 2) ldh<2>(c, c.hf[HP > 2 ? 2 : 0]);
    if constexpr (HP > 3) ldh<3>(c, c.hf[HP > 3 ? 3 : 0]);
    PRI(3);
    SB0();
  };
  if (TYPE == 0) hwait();
  auto lkok = [&]() {
    bool ok = true;
#pragma unroll
    for (int u = 0; u < NU; ++u)
      ok = ok && (sd.v_lk0 >= OOB || (lk0[u].y == c.T && lk0[u].w == c.T2)) && (sd.v_lk1 >= OOB || (lk1[u].y == c.T && lk1[u].w == c.T2));
    return __all(ok);
  };
  if (!lkok()) {
    SPIN_LOOP {
      asm volatile("" ::: "memory");
#pragma unroll
      for (int u = 0; u < NU; ++u) { lk0[u] = ld128(c, sd.v_lk0 + u * 256); lk1[u] = ld128(c, sd.v_lk1 + u * 256); }
      if (lkok()) break;
    }
  }
  if (TYPE == 1) c.fl = ld64(c, c.vfl);
  SB0();
  STAMP(5);
#pragma unroll
  for (int u = 0; u < NU; ++u) {
    const unsigned x0 = lk0[u].x, y0 = lk0[u].z, x1 = lk1[u].x, y1 = lk1[u].z;
    const cpx cy = cfma(wk1[u], cpx{__uint_as_float(x1), __uint_as_float(y1)}, cfma(wk0[u], cpx{__uint_as_float(x0), __uint_as_float(y0)}, pm[u]));
    cpx h = cfma(f1[u], cpx{sumq(cy.x), sumq(cy.y)}, eq[u]);
#pragma unroll
    for (int j = 0; j < 8; ++j) { h = cfma(lam[u], h, v[u][j]); v[u][j] = h; }
    ldsw<v4u>(sd.a_tw, v4u{pk2(v[u][0].x, v[u][1].x), pk2(v[u][2].x, v[u][3].x), pk2(v[u][4].x, v[u][5].x), pk2(v[u][6].x, v[u][7].x)});
    ldsw<v4u>(sd.a_tw + 16384, v4u{pk2(v[u][0].y, v[u][1].y), pk2(v[u][2].y, v[u][3].y), pk2(v[u][4].y, v[u][5].y), pk2(v[u][6].y, v[u][7].y)});
    asm volatile("s_waitcnt lgkmcnt(0)" ::: "memory");
#pragma unroll
    for (int mm = 0; mm < 2; ++mm) st128(c, __builtin_bit_cast(v4u, cat(trread(sd.a_tr + mm * 8), trread(sd.a_tr + mm * 8 + 16384))), sd.v_hs[u] + mm * 1024);
    if (u + 1 < NU) asm volatile("s_waitcnt lgkmcnt(0)" ::: "memory");
  }
  SB0();
  c.vfs = sd.v_fs;
  if (TYPE == 1) {
    if ((__ballot(c.fl.x == c.T && c.fl.y == c.T2) & HM) != HM) {
      waitv<0>();
      st_flag(c, sd.v_fs);
      c.vfs = OOB;
    }
    STAMP(4);
    hwait();
  }
  if (TYPE == 1) {
    if (si == 39) {
      SPIN_LOOP {
        bool ok = true;
        asm volatile("" ::: "memory");
#pragma unroll
        for (int k = 0; k < 4; ++k) {
          const v4u t = ld128(c, WS_STAT + (k * 512 + w * 64 + l) * 16);
          ok = ok && t.y == c.T && t.w == c.T2;
        }
        if (__all(ok)) break;
        __builtin_amdgcn_s_sleep(1);
      }
    }
  }
  c.bump = TYPE == 1 && si == 39;
  SB0();
}

DI void phase2(Ctx& c, const u16* dsk, float* out) {
  const int w = c.w, rt = c.rt, ct = c.ct;
  v4u wf[12][5], hf[12][4];
  f32x4 acc[4][5];
  v2u fl = c.fl;
  STAMP(6); CSTAMP(2);
#pragma unroll
  for (int a = 0; a < 5; ++a) acc[0][a] = c.acc0[a];
  auto body = [&](auto JJ) {
    constexpr int j = JJ.v;
    if constexpr (lds_chunk(j + 1) >= 0) {
      constexpr int ch = lds_chunk(j + 1);
      int l = threadIdx.x & 63; PIN(l);
      const unsigned a4 = tile_base(ch, 0) + w * 4096 + l * 16;
#pragma unroll
      for (int a = 0; a < 4; ++a) wf[j + 1][a] = ldsr<v4u>(a4 + a * 1024);
      wf[j + 1][4] = ldsr<v4u>(tile_base(ch, 4) + w * 1024 + l * 16);
    }
    if constexpr (j < 4) {
#pragma unroll
      for (int a = 0; a < 5; ++a) wf[j][a] = c.wf[j][a];
    }
    if constexpr (j < HP) {
#pragma unroll
      for (int m = 0; m < 4; ++m) hf[j][m] = c.hf[j][m];
    }
    if constexpr (j + WA < 12 && j + WA >= 4 && lds_chunk(j + WA) < 0) { ldw<j + WA>(c, wf[j + WA]); SB0(); }
    if constexpr (j + HA == 6 || j + HA == 8) {
      const unsigned long long msk = j + HA == 6 ? (15ull << 12) : ((c.wc >= 6 ? (1ull << 22) : (1ull << 24)) - (1ull << 16));
      if (__builtin_expect((__ballot(fl.x == c.T && fl.y == c.T2) & msk) != msk, 0)) {
        SPIN_LOOP {
          asm volatile("" ::: "memory");
          const v2u f = ld64(c, c.vfl);
          if ((__ballot(f.x == c.T && f.y == c.T2) & msk) == msk) break;
        }
      }
      __builtin_amdgcn_fence(__ATOMIC_ACQUIRE, "wavefront");
    }
    if constexpr (j + HA == 5 || j + HA == 7) fl = ld64(c, c.vfl);
    if constexpr (j + HA < 12 && j + HA >= HP) ldh<j + HA>(c, hf[j + HA]);
    SB0();
#pragma unroll
    for (int a = 0; a < 5; ++a)
#pragma unroll
      for (int m = 0; m < 4; ++m) {
        if (j == 0 && m > 0) mfz(acc[m][a], __builtin_bit_cast(bf16x8, wf[j][a]), __builtin_bit_cast(bf16x8, hf[j][m]));
        else mfa(acc[m][a], __builtin_bit_cast(bf16x8, wf[j][a]), __builtin_bit_cast(bf16x8, hf[j][m]));
      }
    if constexpr (j == JF) {
      if (c.cg < 27) waitv<(HA >= HP ? 4 : 0)>();
      PRI(3);
      st_flag(c, c.vfs);
      SB0();
      __builtin_amdgcn_s_setprio(FP ? 3 : 0);
    }
    if constexpr (FP && (j == 2 || j == 5 || j == 8)) __builtin_amdgcn_s_setprio(j == 2 ? 2 : (j == 5 ? 1 : 0));
    if constexpr (j == JB) {
      if (!NOJB || c.bump) BAR();
      __builtin_amdgcn_raw_buffer_store_b32(c.T, c.rws, (c.bump && threadIdx.x == 0) ? WS_EPOCH : OOB, 0, 16);
    }
    if (j == 1) STAMP(10);
    if (j == 3) STAMP(11);
    if (j == 4) STAMP(12);
    if (j == 7) STAMP(13);
    SB0();
  };
  body(IC<0>{}); body(IC<1>{}); body(IC<2>{}); body(IC<3>{}); body(IC<4>{}); body(IC<5>{});
  body(IC<6>{}); body(IC<7>{}); body(IC<8>{}); body(IC<9>{}); body(IC<10>{});
  int l = threadIdx.x & 63; PIN(l);
  unsigned b4 = w * 4096 + l * 16, b4h = b4 + 65536, b1 = 98304 + w * 1024 + l * 16, b2 = 147456 + w * 2048 + l * 16;
  PIN(b4); PIN(b4h); PIN(b1); PIN(b2);
  const int tl = ((w >> 1) & 1) * 32 + 8 * ((l & 15) >> 2) + 4 * (w & 1) + (l & 3);
  GAS float* op = (GAS float*)out + (size_t)(c.rb * 256 + rt * 64 + tl) * D_ + ct * 80 + (w >> 2) * 16 + 4 * (l >> 4);
  PIN(op);
  unsigned r4[4], r1[4], r2;
#pragma unroll
  for (int x = 0; x < 4; ++x) { r4[x] = ((w & 3) ^ x) * 4096 + l * 16 + (x == 1 ? 65536 : 0); r1[x] = 98304 + ((w & 3) ^ x) * 1024 + l * 16; PIN(r4[x]); PIN(r1[x]); }
  r2 = 147456 + ((w & 3) ^ 3) * 2048 + l * 16; PIN(r2);
  body(IC<11>{});
  MFMA_DRAIN();
  SB0();
  STAMP(7);
#pragma unroll
  for (int m = 0; m < 4; ++m)
#pragma unroll
    for (int a = 0; a < 5; ++a) {
      const unsigned tb = tile_base(m, a), ts = tile_wstride(m, a);
      ldsw<f32x4>(ts == 4096 ? (tb >= 65536 ? b4h + (tb - 65536) : b4 + tb) : (ts == 1024 ? b1 + (tb - 98304) : b2 + (tb - 147456)), acc[m][a]);
    }
  BAR();
  auto sum = [&](auto AA) {
    constexpr int a = AA.v;
    f32x4 p[8];
#pragma unroll
    for (int x = 0; x < 4; ++x)
#pragma unroll
      for (int hh = 0; hh < 2; ++hh) {
        const unsigned tb = tile_base(x, a), ts = tile_wstride(x, a);
        p[x * 2 + hh] = ldsr<f32x4>(ts == 4096 ? r4[x] + (tb - (x == 1 ? 65536 : 0)) + hh * 16384 : (ts == 1024 ? r1[x] + (tb - 98304) + hh * 4096 : r2 + (tb - 147456) + hh * 8192));
      }
    return ((p[0] + p[1]) + (p[2] + p[3])) + ((p[4] + p[5]) + (p[6] + p[7]));
  };
  if (w < 4) {
    const f32x4 s0 = sum(IC<0>{}), s1 = sum(IC<2>{}), s2 = sum(IC<4>{});
    *(GAS f32x4*)op = s0; *(GAS f32x4*)(op + 32) = s1; *(GAS f32x4*)(op + 64) = s2;
  } else {
    const f32x4 s0 = sum(IC<1>{}), s1 = sum(IC<3>{});
    *(GAS f32x4*)op = s0; *(GAS f32x4*)(op + 32) = s1;
  }
}

__global__ void __launch_bounds__(512, 2)
fused_kernel(const u16* x, const u16* lre, const u16* lim, const u16* bre, const u16* bim, const u16* cre, const u16* cim, const u16* ldt,
             const u16* dsk, const u16* wg, const u16* wu, const u16* wd, float* out, char* ws) {
  Ctx c;
  asm volatile("s_getpc_b64 %0" : "=s"(c.pc));
  c.dsk = dsk; c.x = x; c.bre = bre; c.bim = bim; c.cre = cre; c.cim = cim; c.wg = wg; c.wu = wu; c.wd = wd; c.ws = ws;
  c.lane = threadIdx.x & 63;
  c.w = __builtin_amdgcn_readfirstlane(threadIdx.x >> 6);
  const int xc = blockIdx.x & 7, ix = blockIdx.x >> 3;
  c.rb = (xc >> 1) * 2 + (ix >> 4); c.cg = (xc & 1) * 16 + (ix & 15);
  c.rt = c.cg >> 3; c.ct = c.cg & 7;
  c.shr = ix >> 3;
  c.wc = (c.w + (ROTC ? c.ct + 2 * c.shr : 0)) & 7;
  c.rws = __builtin_amdgcn_make_buffer_rsrc(ws, 0, WS_END, 0x00020000);
  STAMP(0);
  if (__builtin_expect(c.cg < 27, 1)) phase1<0>(c, lre, lim, ldt);
  else phase1<1>(c, lre, lim, ldt);
  phase2(c, dsk, out);
}

extern "C" void kernel_launch(void* const* d_in, const int* in_sizes, int n_in, void* d_out, int out_size, void* d_ws, size_t ws_size,
                              hipStream_t stream) {
  (void)in_sizes; (void)n_in; (void)out_size; (void)ws_size;
  (void)hipFuncSetAttribute((const void*)fused_kernel, hipFuncAttributeMaxDynamicSharedMemorySize, LDS_BYTES);
  fused_kernel<<<256, 512, LDS_BYTES, stream>>>((const u16*)d_in[0], (const u16*)d_in[1], (const u16*)d_in[2], (const u16*)d_in[3], (const u16*)d_in[4],
                                                (const u16*)d_in[5], (const u16*)d_in[6], (const u16*)d_in[7], (const u16*)d_in[8], (const u16*)d_in[10],
                                                (const u16*)d_in[11], (const u16*)d_in[12], (float*)d_out, (char*)d_ws);
}
```

```cpp
#include <hip/hip_runtime.h>
#include <stdint.h>

#define GAS __attribute__((address_space(1)))
#define LAS __attribute__((address_space(3)))
#define DI __device__ __forceinline__
typedef unsigned short u16;
typedef short bf16x8 __attribute__((ext_vector_type(8)));
typedef short s16x4 __attribute__((ext_vector_type(4)));
typedef __bf16 bf2 __attribute__((ext_vector_type(2)));
typedef float f32x4 __attribute__((ext_vector_type(4)));
typedef float f32x2 __attribute__((ext_vector_type(2)));
typedef unsigned v4u __attribute__((ext_vector_type(4)));
typedef unsigned v2u __attribute__((ext_vector_type(2)));

#define XMAP 2
#define SCHK 7
#define L2PF 0
#define PRE 8
#define ROTC 0
#define ORD 0
#define UO 1
#define JC 6
#define JE 8
#define L2P 0
#define NSHR (XMAP == 1 ? 8 : 4)
#define JF 0
#define P6BR
#define P6D 1
#define JS 2
#define IL 0
#define SA 2
#define CPF 50000
#define CPS 8
#define LP0 7
#define LP1 9
#define LP2 10
#define WA 3
#define FP 1
#define TPP 1
#define LW 1
#define HP 3
#define HA 2
#define NOJB 0
#define BEX 0
#define E0 0
#define WPF 1
#define SB1 1
#define GP 1
#define B2 1
#define TI 1
#define HF 0
#define HM (HF ? 0xfull : 0xfffull)
#define NA 4
#define NB 0
#define PRIO 2
#define JB 1
#define NWR 4
#define PH 2
#define NWP 7
#define PWD 3

constexpr int L_ = 2048, D_ = 640, F_ = 1728;
constexpr int ustep(int u) { return UO ? (u >= 27 ? 27 + u : 67 + u) : 54 + u; }
constexpr int NS2 = 94;
constexpr unsigned WS_EPOCH = 0;
constexpr unsigned WS_STAT = 4096;
constexpr unsigned WS_AGG = WS_STAT + 2048 * 16;
constexpr unsigned AGG_RB = 42 * 256;
constexpr unsigned WS_FLAGH = WS_AGG + 8 * AGG_RB;
constexpr unsigned WS_FLAGW = WS_FLAGH + 32 * 8 * 256;
constexpr unsigned WS_CTL_END = WS_FLAGW + 8 * 8 * 128;
constexpr unsigned WS_W2T = 262144;
constexpr unsigned W2T_CT = NS2 * 5 * 1024;
constexpr unsigned WS_H = WS_W2T + 8 * W2T_CT;
constexpr unsigned H_RT = NS2 * 4 * 1024;
constexpr unsigned WS_END = WS_H + 32 * H_RT;
constexpr unsigned OOB = 0x70000000u;
static_assert(WS_CTL_END <= WS_W2T, "ws map");
constexpr unsigned LX(int k) { return k * 32768; }
constexpr unsigned LWB(int k) { return 98304 + ((k + 1) % 3) * 16384; }
constexpr unsigned LWS(int k) { return 147456 + k * 4096; }
constexpr unsigned L_ST = 159744;
constexpr unsigned L_CS = L_ST + 2048;
constexpr unsigned L_CSS = L_CS + 512;
constexpr unsigned L_CS3 = LWB(2) + 8192;
constexpr unsigned L_COEF = L_CSS + 1024;
constexpr unsigned L_POW(int type, int u) { return type == 0 ? LWB(2) + 8192 : (u == 1 ? LWS(0) : LWB(u) + 12288); }
constexpr unsigned L_AGG = LWS(2);
constexpr unsigned L_DUMMY = LWS(0);
constexpr unsigned L_XT0 = LWB(1) + 8192;
constexpr unsigned L_XT1 = LWS(1);
constexpr unsigned LDS_BYTES = 163840;
static_assert(L_COEF + 512 <= LDS_BYTES && LWB(2) == LX(2) + 32768, "lds map");
constexpr unsigned tile_base(int ch, int j) {
  return ch < 3 ? (j < 4 ? LX((ch + 1) % 3) + j * 1024 : LWB((ch + 1) % 3)) : (j < 3 ? LWB(j) + 8192 : LWS(0) + (j - 3) * 1024);
}
constexpr unsigned tile_wstride(int ch, int j) { return ch < 3 ? (j < 4 ? 4096 : 1024) : (j < 3 ? 1024 : 2048); }
constexpr int SPIN_CAP = 1 << 15;

DI float bf2f(unsigned v) { return __uint_as_float(v << 16); }
DI float bfhi(unsigned v) { return __uint_as_float(v & 0xffff0000u); }
DI unsigned pk2(float a, float b) { bf2 v = {(__bf16)a, (__bf16)b}; return __builtin_bit_cast(unsigned, v); }
DI float rbf(float a) { return bf2f(pk2(a, 0.f) & 0xffffu); }
DI void mfa(f32x4& c, bf16x8 a, bf16x8 b) { asm volatile("v_mfma_f32_16x16x32_bf16 %0, %1, %2, %0" : "+v"(c) : "v"(a), "v"(b)); }
DI void mfz(f32x4& c, bf16x8 a, bf16x8 b) { asm volatile("v_mfma_f32_16x16x32_bf16 %0, %1, %2, 0" : "=&v"(c) : "v"(a), "v"(b)); }
template <bool Z> DI void mf(f32x4& c, bf16x8 a, bf16x8 b) { if (Z) mfz(c, a, b); else mfa(c, a, b); }
#define MFMA_DRAIN() asm volatile("s_nop 7\n\ts_nop 1" ::: "memory")
DI v4u mkrsrc(const void* p, unsigned bytes) {
  uint64_t a = (uint64_t)p;
  v4u r; r.x = (unsigned)a; r.y = (unsigned)(a >> 32) & 0xffffu; r.z = bytes; r.w = 0x00020000u;
  return r;
}
template <unsigned SI, unsigned LI> DI void glds16(v4u rs, unsigned voff, unsigned sb, unsigned lb) {
  unsigned t;
  asm volatile("s_add_u32 m0, %4, %6\n\ts_add_u32 %0, %3, %5\n\tbuffer_load_dwordx4 %1, %2, %0 offen lds"
               : "=&s"(t) : "v"(voff), "s"(rs), "s"(sb), "s"(lb), "i"(SI), "i"(LI) : "memory", "scc");
}
template <unsigned SI, unsigned LI> DI void glds16sc1(v4u rs, unsigned voff, unsigned lb) {
  unsigned t;
  asm volatile("s_add_u32 m0, %3, %5\n\ts_mov_b32 %0, %4\n\tbuffer_load_dwordx4 %1, %2, %0 offen sc1 lds"
               : "=&s"(t) : "v"(voff), "s"(rs), "s"(lb), "i"(SI), "i"(LI) : "memory", "scc");
}
template <class T> DI T ldsr(unsigned a) { return *(LAS T*)(uintptr_t)a; }
template <class T> DI void ldsw(unsigned a, T v) { *(LAS T*)(uintptr_t)a = v; }
DI s16x4 trread(unsigned a) { return __builtin_amdgcn_ds_read_tr16_b64_v4i16((LAS s16x4*)(uintptr_t)a); }
DI bf16x8 cat(s16x4 lo, s16x4 hi) { return __builtin_shufflevector(lo, hi, 0, 1, 2, 3, 4, 5, 6, 7); }
template <int N> DI void waitv() { asm volatile("s_waitcnt vmcnt(%0)" ::"n"(N) : "memory"); }
#define BAR() asm volatile("s_waitcnt lgkmcnt(0)\n\ts_barrier" ::: "memory")
#define LGKM0() __builtin_amdgcn_s_waitcnt(0xc07f)
#define PRI(n) do { if (PRIO == 2) { SB0(); __builtin_amdgcn_s_setprio(n); SB0(); } } while (0)
#define SB0() __builtin_amdgcn_sched_barrier(0)
#define PIN(x) asm volatile("" : "+v"(x))
#define SLPN 0
#define SLP() do { if (SLPN) __builtin_amdgcn_s_sleep(SLPN); } while (0)
#define SPIN_LOOP _Pragma("nounroll") for (int sp = 0; sp < SPIN_CAP; ++sp)
template <int N> DI float ror(float v) { return __builtin_bit_cast(float, __builtin_amdgcn_update_dpp(0, __builtin_bit_cast(int, v), 0x120 + N, 0xf, 0xf, false)); }
DI void sw16(float v, float& e, float& o) { unsigned u = __float_as_uint(v); auto r = __builtin_amdgcn_permlane16_swap(u, u, false, false); e = __uint_as_float(r[0]); o = __uint_as_float(r[1]); }
DI void sw32(float v, float& lo, float& hi) { unsigned u = __float_as_uint(v); auto r = __builtin_amdgcn_permlane32_swap(u, u, false, false); lo = __uint_as_float(r[0]); hi = __uint_as_float(r[1]); }
DI float sumq(float v) { float a, b; sw16(v, a, b); v = a + b; sw32(v, a, b); return a + b; }
DI float sumr(float v) { v += ror<8>(v); v += ror<4>(v); v += ror<2>(v); v += ror<1>(v); return v; }
DI void gatherq(float v, float (&a)[4]) { float e, o; sw16(v, e, o); sw32(e, a[0], a[2]); sw32(o, a[1], a[3]); }
typedef f32x2 cpx;
DI cpx cfma(cpx a, cpx b, cpx c) {
  cpx r;
  asm("v_pk_fma_f32 %0, %1, %2, %3 op_sel:[1,1,0] op_sel_hi:[1,0,1] neg_lo:[1,0,0]\n\tv_pk_fma_f32 %0, %1, %2, %0 op_sel_hi:[0,1,1]" : "=&v"(r) : "v"(a), "v"(b), "v"(c));
  return r;
}
DI cpx cmul(cpx a, cpx b) {
  cpx r;
  asm("v_pk_mul_f32 %0, %1, %2 op_sel:[1,1] op_sel_hi:[1,0] neg_lo:[1,0]\n\tv_pk_fma_f32 %0, %1, %2, %0 op_sel_hi:[0,1,1]" : "=&v"(r) : "v"(a), "v"(b));
  return r;
}
DI cpx csel(bool p, cpx a) { return cpx{p ? a.x : 1.f, p ? a.y : 0.f}; }
DI cpx czero(bool p, cpx a) { return cpx{p ? 0.f : a.x, p ? 0.f : a.y}; }
template <int V> struct IC { static constexpr int v = V; };

#define STAMP(k) do {} while (0)
#define CSTAMP(k) do {} while (0)
struct Ctx {
  const u16 *dsk, *x, *bre, *bim, *cre, *cim, *wg, *wu, *wd;
  char* ws;
  __amdgpu_buffer_rsrc_t rws;
  v2u tag;
  unsigned T, T2;
  int lane, w, wc, rb, cg, rt, ct, shr;
  f32x4 acc0[5];
  unsigned vw2, vh2, vfl;
  v4u wf[NWR][5], hf[HP][4];
  v2u fl;
  unsigned vfs;
  bool bump;
  unsigned long long pc;
};
DI v4u ld128(const Ctx& c, unsigned vo, unsigned so = 0) { return __builtin_amdgcn_raw_buffer_load_b128(c.rws, vo, so, 16); }
DI v2u ld64(const Ctx& c, unsigned vo) { return __builtin_amdgcn_raw_buffer_load_b64(c.rws, vo, 0, 16); }
DI void st128(const Ctx& c, v4u v, unsigned vo) { __builtin_amdgcn_raw_buffer_store_b128(v, c.rws, vo, 0, 16); }
DI void st_flag(const Ctx& c, unsigned vo) { __builtin_amdgcn_raw_buffer_store_b64(c.tag, c.rws, vo, 0, 16); }

DI float fexp(float x) {
  const float t = x * 1.44269502162933349609375f;
  const float r = fmaf(x, 1.925963033500011e-8f, fmaf(x, 1.44269502162933349609375f, -t));
  const float e = __builtin_amdgcn_exp2f(t);
  return fmaf(e, r * 0.693147182464599609375f, e);
}
DI void fsincos(float z, float& sn, float& cs) {
  const float k = rintf(z * 0.63661977236758134f);
  float r = fmaf(k, -1.5707963705062866f, z);
  r = fmaf(k, 4.3711388286737929e-08f, r);
  r = fmaf(k, 1.7151245100058819e-15f, r);
  const float r2 = r * r;
  const float sp = fmaf(r * r2, fmaf(r2, fmaf(r2, -1.9515295891e-4f, 8.3321608736e-3f), -1.6666654611e-1f), r);
  const float cp = fmaf(r2 * r2, fmaf(r2, fmaf(r2, 2.443315711809948e-5f, -1.388731625493765e-3f), 4.166664568298827e-2f), fmaf(r2, -0.5f, 1.f));
  const int n = (int)k;
  sn = (n & 1) ? cp : sp; cs = (n & 1) ? sp : cp;
  if (n & 2) sn = -sn;
  if ((n + 1) & 2) cs = -cs;
}
constexpr int LP(int ch) { return ch == 0 ? LP0 : (ch == 1 ? LP1 : LP2); }
constexpr int lds_chunk(int j) { return j == LP0 ? 0 : (j == LP1 ? 1 : (j == LP2 ? 2 : -1)); }
template <int J> DI void ldw(const Ctx& c, v4u (&d)[5]) {
  const unsigned vo = J == 11 ? (c.wc < 6 ? c.vw2 : OOB) : c.vw2;
#pragma unroll
  for (int a = 0; a < 4; ++a) d[a] = ld128(c, vo + a * 1024, J * 40960);
  d[4] = ld128(c, vo, J * 40960 + 4096);
}
template <int J> DI void ldh(const Ctx& c, v4u (&d)[4]) {
  const unsigned vo = J == 11 ? (c.wc < 6 ? c.vh2 : OOB) : c.vh2;
#pragma unroll
  for (int m = 0; m < 4; ++m) d[m] = ld128(c, vo ^ (m * 1024), J * 32768);
}

template <int TYPE> struct G1 {
  static constexpr int NU = TYPE == 0 ? 1 : 3;
  static constexpr int NG = TYPE == 0 ? 10 : 6;
  static constexpr int NR = TYPE == 0 ? 14 : 8;
  static constexpr int NP = (TYPE == 0 ? 7 : 6) - P6D;
  struct Frags { bf16x8 x[TYPE == 0 ? 4 : 2]; s16x4 wl[4], wh[4]; bf16x8 s[2 * NU]; };
  v4u rx, rw0, rw1, rs;
  unsigned vx0, vx1, vw, vs;
  unsigned sx, sw, ss, ss2;
  unsigned ax[2][2][2], aw[4], as[NU][2];
  f32x4 acc[4][4];
  f32x4 accs[2 * NU][2];
  f32x4 cs0, cs1, cs2, css[6];
  bf16x8 ones;
  int w;
  unsigned w1k, w4k, w1k6, vs6;

  DI void init_stage(const Ctx& c, int unit0) {
    const int l = c.lane;
    w = c.w; w1k = w * 1024; w4k = w * 4096;
    rx = mkrsrc(c.x, L_ * D_ * 2);
    const unsigned ch = (l & 7) ^ ((l >> 3) & 3);
    vx0 = (l >> 3) * 1280 + ch * 16;
    vx1 = (l >> 3) * 1280 + (ch ^ 4) * 16;
    sx = (c.rb * 256 + w * 32) * 1280;
    rs = mkrsrc((w & 2) ? c.bim : c.bre, 640 * 640 * 2);
    vs = (l >> 3) * 1280 + (((l & 7) ^ (l >> 3)) * 16);
    vs6 = w < 4 ? vs : OOB; w1k6 = w < 4 ? w1k : 65536;
    if (TYPE == 0) {
      rw0 = mkrsrc(c.wg, D_ * F_ * 2); rw1 = mkrsrc(c.wu, D_ * F_ * 2);
      const unsigned key2 = ((l >> 4) & 1) | ((w & 1) << 1);
      vw = (l >> 3) * 3456 + (((l & 7) ^ (key2 << 1)) * 16);
      sw = w * 8 * 3456 + c.cg * 128;
      ss2 = (unit0 * 16 + (w & 1) * 8) * 1280;
    } else {
      ss = (min(unit0 + (w >> 2), 39) * 16 + (w & 1) * 8) * 1280;
      ss2 = (min(unit0 + 2, 39) * 16 + (w & 1) * 8) * 1280;
    }
  }
  DI void init_read(const Ctx& c) {
    const int l = c.lane, i = l & 15, q = l >> 4;
    const int wt = w & 3, wn = w >> 2;
#pragma unroll
    for (int kk = 0; kk < 2; ++kk) {
      const unsigned ch = (((kk * 4 + q) ^ (i & 7)) * 16);
#pragma unroll
      for (int pr = 0; pr < (TYPE == 0 ? 2 : 1); ++pr) {
        ax[0][pr][kk] = (wt * 64 + (pr ^ wn) * 32 + 8 * (i >> 2) + (i & 3)) * 128 + ch;
        ax[1][pr][kk] = ax[0][pr][kk] + 65536;
        PIN(ax[0][pr][kk]); PIN(ax[1][pr][kk]);
      }
#pragma unroll
      for (int u = 0; u < 1; ++u) {
        as[u][kk] = (TYPE == 0 ? LWS(0) : LWB(2)) + i * 128 + ch;
        PIN(as[u][kk]);
      }
    }
    if (TYPE == 0) {
      const int r = (l >> 2) & 3, cc = l & 3;
      const unsigned key2 = ((r >> 1) & 1) | ((q & 1) << 1);
#pragma unroll
      for (int a = 0; a < 4; ++a) {
        const int act = a ^ wt;
        aw[a] = LWB(2) + (act >> 1) * 8192 + (8 * q + r) * 128 + (((2 * wn + (act & 1)) ^ key2) * 32) + cc * 8;
        PIN(aw[a]);
      }
      cs1 = f32x4{0.f, 0.f, 0.f, 0.f}; cs2 = f32x4{0.f, 0.f, 0.f, 0.f};
      PIN(cs1); PIN(cs2);
    }
    if (TYPE == 1) {
#pragma unroll
      for (int a = 0; a < 6; ++a) { css[a] = f32x4{0.f, 0.f, 0.f, 0.f}; PIN(css[a]); }
    }
    ones = bf16x8{0x3f80, 0x3f80, 0x3f80, 0x3f80, 0x3f80, 0x3f80, 0x3f80, 0x3f80};
    PIN(ones);
  }
  template <int P, int S> DI void piece() {
    constexpr int K = S % 3;
    if constexpr (P < 4) glds16<S * 128 + P * 10240, LX(K) + P * 1024>(rx, (P & 1) ? vx1 : vx0, sx, w4k);
    else if constexpr (P < 6) {
      if constexpr (TYPE == 0) glds16<S * (64 * 3456), LWB(K) + (P - 4) * 8192>(P == 4 ? rw0 : rw1, vw, sw, w1k);
      else if constexpr (P == 4) glds16<S * 128, LWB(K)>(rs, vs, ss, w1k);
    } else {
      if (w < 4)
      glds16<S * 128, (TYPE == 0 ? LWS(K) : LWB(K) + 8192)>(rs, vs6, ss2, w1k6);
    }
  }
  template <int S> DI void half0() { piece<6, S>(); piece<0, S>(); piece<4, S>(); piece<1, S>(); }
  template <int S> DI void half1() { piece<2, S>(); piece<5, S>(); piece<3, S>(); }

  template <int K, int KK, int R> DI void rd(Frags& f) {
    constexpr unsigned ow = LWB(K) - LWB(2), ox = K == 1 ? 32768 : 0;
    if constexpr (TYPE == 0) {
      if (R == 0) f.wl[0] = trread(aw[0] + (ow + KK * 4096));
      else if (R == 1) f.wh[0] = trread(aw[0] + (ow + KK * 4096 + 512));
      else if (R < 6) f.x[(R - 2) & 3] = ldsr<bf16x8>(ax[K == 2][((R - 2) >> 1) & 1][KK] + (ox + ((R - 2) & 1) * 512));
      else if (R < 12) {
        constexpr int a = ((R - 4) >> 1) & 3;
        if (R & 1) f.wh[a] = trread(aw[a] + (ow + KK * 4096 + 512)); else f.wl[a] = trread(aw[a] + (ow + KK * 4096));
      } else f.s[R & 1] = ldsr<bf16x8>(as[0][KK] + (K * 4096 + (R & 1) * 2048));
    } else {
      if (R == 1 || R == 2) f.x[(R - 1) & 1] = ldsr<bf16x8>(ax[K == 2][0][KK] + (ox + ((R - 1) & 1) * 512));
      else { constexpr int a = R == 0 ? 0 : R - 2; f.s[a] = ldsr<bf16x8>(as[0][KK] + (ow + a * 2048)); }
    }
  }
  template <bool Z, int M> DI void mm(const Frags& f) {
    if constexpr (TYPE == 0 && M < 16) mf<Z>(acc[M >> 2][M & 3], cat(f.wl[M >> 2], f.wh[M >> 2]), f.x[M & 3]);
    else { constexpr int a = (M - (TYPE == 0 ? 16 : 0)) >> 1; mf<Z>(accs[a][M & 1], f.x[M & 1], f.s[a]); }
  }
  template <int SUB, int G, class H> DI void group(const Frags& cur, Frags& nxt, H& hook) {
    constexpr int N = SUB + 1, K = (N >> 1) % 3, KK = N & 1;
    constexpr bool RD = N < 20;
    mm<SUB == 0, 2 * G>(cur);
    if constexpr (RD && 2 * G < NR) rd<K, KK, 2 * G>(nxt);
    mm<SUB == 0, 2 * G + 1>(cur);
    if constexpr (RD && 2 * G + 1 < NR) rd<K, KK, 2 * G + 1>(nxt);
    if constexpr (TYPE == 0) {
      if (G == 0) mf<SUB == 0>(cs0, ones, cat(cur.wl[0], cur.wh[0]));
      if (G == 2) { if (__builtin_expect(w == (SUB & 7), 0)) { mfa(cs1, ones, cur.s[0]); mfa(cs2, ones, cur.s[1]); } }
    } else {
      if (G == 0) {
        if (__builtin_expect(w == (SUB & 7), 0)) {
#pragma unroll
          for (int a = 0; a < 6; ++a) mfa(css[a], ones, cur.s[a]);
        }
      }
    }
    hook(IC<G>{});
    SB0();
    if constexpr (G + 1 < NG) group<SUB, G + 1>(cur, nxt, hook);
  }
  template <int SUB, class H> DI void part(const Frags& cur, Frags& nxt, H hook) { group<SUB, 0>(cur, nxt, hook); }
  template <int R> DI void rdall(Frags& f) { rd<0, 0, R>(f); if constexpr (R + 1 < NR) rdall<R + 1>(f); }
};

template <int NU> struct Side {
  unsigned xr[5], pa[NU], pb[NU], pd[NU];
  float v[10], s, e;
  unsigned a_mu, a_cs, v_hg, v_fg, a_st, a_css, a_aw, a_ar, v_pub, v_lk0, v_lk1, v_hs[NU], v_fs, a_tw, a_tr, v_x0, v_x1, a_bs, a_bx, a_p8, a_w0, a_w1, a_k0, a_k1;
  static constexpr int NCH = 8 + 8;
  DI void table(const Ctx& c, int u) const {
    if (c.w >= 6) return;
    const int k = threadIdx.x >> 4;
    const float fn = (float)(k < 7 ? 8 * k - 24 : (k < 16 ? 32 * k - 256 : (k < 23 ? 256 * k - 4096 : 1)));
    const float dt = rbf(fexp(bf2f(pd[u])));
    const float ea = dt * bf2f(pa[u]) * 1.44269504088896341f, eb = dt * bf2f(pb[u]) * 0.15915494309189535f;
    const float m = fn < 0.f ? 0.f : __builtin_amdgcn_exp2f(fn * ea);
    const float r = __builtin_amdgcn_fractf(fn * eb);
    ldsw<f32x2>(L_POW(NU == 1 ? 0 : 1, u) + threadIdx.x * 8, f32x2{m * __builtin_amdgcn_cosf(r), m * __builtin_amdgcn_sinf(r)});
  }
  template <int N> DI void tick(Ctx& c, int srow, int unit0) {
    const int l = c.lane, i = l & 15, q = l >> 4, w = c.w, wt = w & 3, wn = w >> 2, grp = wt * 2 + wn;
    if constexpr (N == 0) {
#pragma unroll
      for (int j = 0; j < 5; ++j) { v[2 * j] = bf2f(xr[j]); v[2 * j + 1] = bfhi(xr[j]); PIN(v[2 * j]); PIN(v[2 * j + 1]); }
    } else if constexpr (N == 1) {
      s = ((v[0] + v[1]) + (v[2] + v[3])) + ((v[4] + v[5]) + (v[6] + v[7])) + (v[8] + v[9]); PIN(s);
    } else if constexpr (N == 2) { s = sumr(s); PIN(s); }
    else if constexpr (N == 3) { s = sumq(s) * (1.f / 640.f); PIN(s); }
    else if constexpr (N == 4) {
#pragma unroll
      for (int j = 0; j < 10; ++j) { v[j] -= s; PIN(v[j]); }
    } else if constexpr (N == 5) {
      e = v[0] * v[0];
#pragma unroll
      for (int j = 1; j < 10; ++j) e = fmaf(v[j], v[j], e);
      PIN(e);
    } else if constexpr (N == 6) { e = sumr(e); PIN(e); }
    else if constexpr (N == 7) {
      const float var = rbf(sumq(e) * (1.f / 640.f));
      const float sd = rbf(__builtin_amdgcn_sqrtf(rbf(var + 1e-5f)));
      float rc = __builtin_amdgcn_rcpf(sd);
      rc = fmaf(fmaf(-sd, rc, 1.f), rc, rc);
      st128(c, v4u{__float_as_uint(rbf(s)), c.T, __float_as_uint(rc), c.T2}, WS_STAT + srow * 16);
    } else if constexpr (N < 11) {
      const int k0 = c.rb - 1 - q;
      if constexpr (N == 8) {
        a_css = (NU == 1 ? L_CSS : L_CS3) + q * 128 + i * 4;
        a_ar = L_AGG + i * 64 + q * 16;
        a_aw = a_ar + grp * 8 - 48;
#pragma unroll
        for (int u = 0; u < NU; ++u) { v_hs[u] = (unit0 + u < 40 ? WS_H + (c.rb * 4 + wt) * H_RT + ustep(unit0 + u) * 4096 + wn * 2048 : OOB) + l * 16; PIN(v_hs[u]); }
        PIN(a_css);
        PIN(a_ar); PIN(a_aw);
      } else if constexpr (N == 9) {
        const unsigned po = (WS_AGG + unit0 * 256) + i * 16;
        v_pub = q == 3 ? po + c.rb * AGG_RB : OOB;
        v_lk0 = k0 >= 0 ? po + q * AGG_RB : OOB;
        v_lk1 = k0 >= 4 ? po + (q + 4) * AGG_RB : OOB;
        PIN(v_pub); PIN(v_lk0); PIN(v_lk1);
      } else {
        const unsigned tb = L_POW(NU == 1 ? 0 : 1, 2) + i * 8;
        a_p8 = tb + q * 128;
        a_w0 = tb + (8 + max(grp - 1 - 2 * q, -1)) * 128;
        a_w1 = tb + (8 + max(grp - 2 - 2 * q, -1)) * 128;
        a_k0 = tb + (16 + max(k0, 0)) * 128;
        a_k1 = tb + (16 + max(k0 - 4, 0)) * 128;
        PIN(a_p8); PIN(a_w0); PIN(a_w1); PIN(a_k0); PIN(a_k1);
      }
    } else {
      constexpr int k = N - 11;
      const unsigned fbase = WS_FLAGH + (c.rb * 4 + wt) * 2048;
      const unsigned r8 = ((l & 12) << 4) | ((l & 3) << 3);
      if constexpr (k == 0) {
        if (NU == 1) {
          const int step = 2 * c.cg + wn;
          a_mu = (L_ST + wt * 512 + wn * 256) + r8;
          a_cs = (L_CS + wn * 256 + wt * 64) + q * 16;
          v_hg = (WS_H + (c.rb * 4 + wt) * H_RT + step * 4096 + wn * 2048) + l * 16;
          v_fg = (fbase + (step & 7) * 256 + (step >> 3) * 16) + (l & 1) * 8;
          PIN(a_mu); PIN(a_cs); PIN(v_hg); PIN(v_fg);
        }
      } else if constexpr (k == 1) {
        const int step = ustep(NU == 1 ? unit0 : min(unit0 + min(l, 2), 39));
        v_fs = fbase + (step & 7) * 256 + (step >> 3) * 16 + wn * 8;
        a_st = (L_ST + grp * 256) + q * 64;
        a_bs = (L_ST + c.rt * 512 + (wt >> 1) * 256 + (wt & 1) * 32) + r8;
        a_bx = ((wt >> 1) * 5120 + (wt & 1) * 640) + r8 * 20 + q * 8;
        PIN(v_fs); PIN(a_st); PIN(a_bs); PIN(a_bx);
      } else if constexpr (k == 2) {
        const unsigned tr = LWB(2) + 8192 + w * 1024;
        a_tw = tr + i * 64 + ((q ^ (i >> 1)) & 3) * 16;
        a_tr = tr + (l >> 2) * 64 + ((l ^ (l >> 3)) & 3) * 16;
        PIN(a_tw); PIN(a_tr);
      } else if constexpr (k == 3) {
        c.vh2 = (WS_H + (c.rb * 4 + c.rt) * H_RT + c.wc * 4096 + wt * 1024) + l * 16;
        c.vfl = (WS_FLAGH + (c.rb * 4 + c.rt) * 2048 + c.wc * 256) + (l & 31) * 8;
        c.vw2 = (WS_W2T + c.ct * W2T_CT + c.wc * 5120) + l * 16;
        PIN(c.vh2); PIN(c.vfl); PIN(c.vw2);
      } else {
        const unsigned c0 = w * 64 + l, r0 = (c0 * 6554) >> 16, c1 = c0 + 512, r1 = (c1 * 6554) >> 16, xb = (c.rb * 256 + c.rt * 64) * 1280 + c.ct * 160;
        v_x0 = xb + r0 * 1120 + c0 * 16;
        v_x1 = w < 2 ? xb + r1 * 1120 + c1 * 16 : c.ct * 160 + l * 16;
        PIN(v_x0); PIN(v_x1);
      }
    }
  }
};

template <int TYPE> DI void phase1(Ctx& c, const u16* lre, const u16* lim, const u16* ldt) {
  const int l = c.lane, i = l & 15, q = l >> 4, w = c.w, wt = w & 3, wn = w >> 2;
  const int rb = c.rb, cg = c.cg;
  constexpr int NU = G1<TYPE>::NU, NP = G1<TYPE>::NP;
  G1<TYPE> g;
  Side<NU> sd;
  const int srow = rb * 256 + cg * 8 + w;
  const int si = rb * 5 + (cg - 27), sj = si, mi = rb * 27 + cg;
  const int unit0 = TYPE == 0 ? cg : 27 + 3 * (cg - 27);
  __builtin_amdgcn_s_waitcnt(0x0070);
  unsigned cpf;
  {
    const unsigned n = ((blockIdx.x >> 3) & (CPS - 1)) * (512 / CPS) + (threadIdx.x & (512 / CPS - 1));
    if (w < 8 / CPS) asm volatile("global_load_dword %0, %1, %2" : "=v"(cpf) : "v"(min(n * 128u, (unsigned)CPF)), "s"(c.pc) : "memory");
  }
  STAMP(56);
  unsigned ep, qa, qb, qd;
  v4u xq;
  {
    const u16* xrow = c.x + (size_t)srow * D_;
    asm volatile("s_load_dword %2, %6, 0x0\n\ts_nop 4\n\tglobal_load_dwordx4 %0, %3, %5\n\tglobal_load_dword %1, %4, %5 offset:1024"
                 : "=&v"(xq), "=&v"(sd.xr[4]), "=&s"(ep) : "v"(l * 16), "v"(l * 4), "s"(xrow), "s"(c.ws) : "memory");
    if (TYPE == 1)
      asm volatile("s_nop 4\n\tglobal_load_ushort %0, %3, %4\n\tglobal_load_ushort %1, %3, %5\n\tglobal_load_ushort %2, %3, %6"
                   : "=&v"(qa), "=&v"(qb), "=&v"(qd) : "v"(((sj >> 2) * 64 + l) * 2), "s"(lre), "s"(lim), "s"(ldt) : "memory");
  }
  auto params = [&]() {
#pragma unroll
    for (int u = 0; u < NU; ++u) {
      const int ua = min(unit0 + u, 39);
      asm volatile("s_nop 4\n\tglobal_load_ushort %0, %3, %4\n\tglobal_load_ushort %1, %3, %5\n\tglobal_load_ushort %2, %3, %6"
                   : "=&v"(sd.pa[u]), "=&v"(sd.pb[u]), "=&v"(sd.pd[u]) : "v"((ua * 16 + i) * 2), "s"(lre), "s"(lim), "s"(ldt) : "memory");
    }
  };
  auto ticks = [&](auto self, auto N, auto E) { if constexpr (N.v < E.v) { sd.template tick<N.v>(c, srow, unit0); self(self, IC<N.v + 1>{}, E); } };
  g.init_stage(c, unit0);
  STAMP(57);
  unsigned wflag = OOB, ja = 0, jb = 0, jc = 0;
  if (TYPE == 0) {
    if (wn == 0) { g.template half0<0>(); g.template half1<0>(); }
    STAMP(58);
    g.init_read(c);
    ja = (l >> 2) * 320 + w * 32 + (l & 3) * 8;
    jb = ja + 5120; jc = ja + 5376;
    ja += LWB(2) + 8192; jb += jb < 8192 ? LWB(2) + 8192 : LWS(2) - 8192; jc += jc < 8192 ? LWB(2) + 8192 : LWS(2) - 8192;
    PIN(ja); PIN(jb); PIN(jc);
    ticks(ticks, IC<8>{}, IC<Side<NU>::NCH>{});
    STAMP(59);
    if (wn != 0) { g.template half0<0>(); g.template half1<0>(); }
    const v4u rj = mkrsrc(c.wd, F_ * D_ * 2);
    const unsigned c0 = w * 64 + l, r0 = (c0 * 3277) >> 16, c1 = c0 + 512, r1 = (c1 * 3277) >> 16;
    glds16<0, LWB(2) + 8192>(rj, r0 * 960 + c0 * 16, (mi >> 2) * 40960 + (mi & 3) * 320, g.w1k);
    if (w < 2) glds16<0, LWS(2)>(rj, r1 * 960 + c1 * 16, (mi >> 2) * 40960 + (mi & 3) * 320, g.w1k);
    STAMP(60);
  } else {
    params();
    auto req = [&]() {
      const v4u rj = mkrsrc(wn ? c.cim : c.cre, 640 * 640 * 2);
      const unsigned r0 = wt * 40 + (l >> 3);
#pragma unroll
      for (int j = 0; j < 5; ++j) {
        const unsigned row = r0 + j * 8;
        glds16<0, LX(2)>(rj, row * 1280 + (((l & 7) ^ ((row >> 1) & 7)) * 16), (sj & 3) * 160 * 1280 + (sj >> 2) * 128, (w * 5 + j) * 1024);
      }
    };
    if (wn == 0) req();
    g.init_read(c);
    ticks(ticks, IC<8>{}, IC<Side<NU>::NCH>{});
    if (wn != 0) req();
    g.template half0<0>(); g.template half1<0>();
  }
  asm volatile("s_waitcnt vmcnt(%3) lgkmcnt(0)" : "+v"(xq), "+v"(sd.xr[4]), "+s"(ep) : "n"(TYPE == 0 ? 7 : 10) : "memory");
  sd.xr[0] = xq.x; sd.xr[1] = xq.y; sd.xr[2] = xq.z; sd.xr[3] = xq.w;
  STAMP(1);
  asm volatile("" ::"v"(cpf));
  c.T = ep + 1u;
  c.T2 = (c.T * 0x9E3779B1u) ^ 0x5bd1e995u;
  c.tag = v2u{c.T, c.T2};
  PIN(c.tag);
  typename G1<TYPE>::Frags fa, fb;
  unsigned wpd = 0;
  if (TYPE == 0) {
    ticks(ticks, IC<0>{}, IC<(PRE < 4 ? PRE : 4)>{});
    if (SB1) __builtin_amdgcn_s_barrier();
    g.template half0<1>();
    STAMP(61);
    ticks(ticks, IC<4>{}, IC<(PRE < 7 ? PRE : 7)>{});
    g.template half1<1>();
    if (w >= 5) {
      const unsigned vo = w < 7 ? (l < 56 ? (192 + 56 * rb + l) * 3456 + cg * 128 : OOB) : (l < 28 ? (cg * 16 + (rb & 3) * 4 + (l & 3)) * 1280 + (3 + (l >> 2)) * 128 : OOB);
      const v4u rb2 = mkrsrc((rb & 4) ? c.bim : c.bre, 640 * 640 * 2);
      asm volatile("s_nop 4\n\tbuffer_load_dword %0, %1, %2, 0 offen" : "+v"(wpd) : "v"(vo), "s"(w == 5 ? g.rw0 : (w == 6 ? g.rw1 : rb2)) : "memory");
    }
    ticks(ticks, IC<7>{}, IC<PRE>{});
    STAMP(62);
    waitv<6 + (PRE >= 8)>();
    STAMP(63);
    BAR();
    g.template rdall<0>(fa);
  } else {
    asm volatile("" : "+v"(qa), "+v"(qb), "+v"(qd));
    if (w == 7) {
      const float ar = bf2f(qa), ai = bf2f(qb), dt = rbf(fexp(bf2f(qd))), e = fexp(dt * ar);
      float sn, cs;
      fsincos(dt * ai, sn, cs);
      const float inv = 2.f / (ar * ar + ai * ai);
      ldsw<f32x2>(L_COEF + l * 8, cmul(cpx{e * cs - 1.f, e * sn}, cpx{ar * inv, -ai * inv}));
    }
#pragma unroll
    for (int u = 0; u < NU; ++u) { asm volatile("" : "+v"(sd.pa[u]), "+v"(sd.pb[u]), "+v"(sd.pd[u])); sd.table(c, u); }
    g.template half0<1>(); g.template half1<1>();
    waitv<10>();
    BAR();
    const f32x4 k0 = ldsr<f32x4>(L_COEF + (16 * wt + 4 * q) * 8), k1 = ldsr<f32x4>(L_COEF + (16 * wt + 4 * q) * 8 + 16);
    const cpx kc[4] = {{k0.x, k0.y}, {k0.z, k0.w}, {k1.x, k1.y}, {k1.z, k1.w}};
    const int step = ustep(4 * (sj >> 2) + wt), ct = 2 * (sj & 3) + wn;
#pragma unroll
    for (int j = 0; j < 5; ++j) {
      const unsigned row = (wn * 5 + j) * 16 + i;
      const unsigned a = LX(2) + row * 128 + (((2 * wt + (q >> 1)) ^ ((row >> 1) & 7)) * 16) + (q & 1) * 8;
      const v2u cr = ldsr<v2u>(a), ci = ldsr<v2u>(a + 20480);
      const unsigned cr0 = cr.x, cr1 = cr.y, ci0 = ci.x, ci1 = ci.y;
      const cpx p0 = cmul(kc[0], cpx{bf2f(cr0), bf2f(ci0)}), p1 = cmul(kc[1], cpx{bfhi(cr0), bfhi(ci0)});
      const cpx p2 = cmul(kc[2], cpx{bf2f(cr1), bf2f(ci1)}), p3 = cmul(kc[3], cpx{bfhi(cr1), bfhi(ci1)});
      const v4u o = {pk2(p0.x, p1.x), pk2(p2.x, p3.x), pk2(-p0.y, -p1.y), pk2(-p2.y, -p3.y)};
      st128(c, o, l * 16 + j * 1024 + (WS_W2T + ct * W2T_CT + step * 5120));
    }
    wflag = WS_FLAGW + (ct * 8 + (step & 7)) * 128 + (step >> 3) * 8;
    SB0();
    waitv<10>();
    BAR();
    g.template rdall<0>(fa);
  }
  STAMP(2); CSTAMP(0);
  SB0();
  v4u sg; v2u fw; unsigned pfd = 0;
  bf16x8 jv0, jv1;
  const v4u rwsv = mkrsrc(c.ws, WS_END);
  auto w2l = [&](auto CH, auto A) { glds16sc1<LP(CH.v) * 40960 + A.v * 1024, tile_base(CH.v, A.v)>(rwsv, c.vw2, A.v < 4 ? g.w4k : g.w1k); };
  auto side = [&](auto SUB, auto G) {
    constexpr int k = TYPE == 0 ? (G.v >= 10 - TPP ? G.v - (10 - TPP) : -1) : G.v - 4;
    constexpr int n = TYPE == 0 ? PRE + SUB.v * TPP + k : SUB.v * 2 + k;
    if constexpr (k >= 0 && n < 8) sd.template tick<n>(c, srow, unit0);
    if constexpr (TYPE == 0 && SUB.v == 17 && G.v == 8) sd.table(c, 0);
    if constexpr (SUB.v >= 20 - LW) {
      constexpr int J = SUB.v - (20 - LW);
      constexpr int a = TYPE == 0 ? (G.v == 0 ? 0 : (G.v == 2 ? 1 : (G.v == 3 ? 2 : (G.v == 5 ? 3 : (G.v == 7 ? 4 : -1))))) : G.v - 1;
      if constexpr (a >= 0) c.wf[J][a] = a < 4 ? ld128(c, c.vw2 + a * 1024, J * 40960) : ld128(c, c.vw2, J * 40960 + 4096);
    }
  };
  constexpr int GA = TYPE == 0 ? 4 : 2, GB = TYPE == 0 ? 6 : 4;
  auto step = [&](auto SS) {
    constexpr int S = SS.v;
    LGKM0();
    g.template part<2 * S>(fa, fb, [&](auto G) {
      if constexpr (S == 0) {
        if constexpr (TYPE == 0) {
          if (G.v == 7) { jv0 = cat(trread(ja), trread(jb)); jv1 = cat(trread(ja + 256), trread(jc)); }
        }
        if (G.v == 0) g.template piece<0, 2>();
        if (G.v == 1) g.template piece<4, 2>();
        if (G.v == 2) g.template piece<1, 2>();
        if (G.v == 3) g.template piece<2, 2>();
        if (G.v == 4) g.template piece<3, 2>();
      } else if constexpr (S + 2 < 10) {
        if (G.v == 1) g.template piece<2, S + 2>();
        if (G.v == GA) g.template piece<5, S + 2>();
        if (G.v == GB) g.template piece<3, S + 2>();
      } else {
        if (G.v == 1) w2l(IC<S - 8>{}, IC<3>{});
        if (G.v == GB) w2l(IC<S - 8>{}, IC<4>{});
      }
      side(IC<2 * S>{}, G);
    });
    if constexpr (S < 9) {
      if (S == SCHK) asm volatile("s_waitcnt vmcnt(%2)" : "+v"(sg), "+v"(fw) : "n"(NP) : "memory");
      else if (S == 8) waitv<5>();
      else if (S == 0) waitv<TYPE == 0 ? 5 : 10>();
      else if (S == JS + 1 && TYPE == 0) waitv<NP + 1>();
      else waitv<NP>();
      LGKM0();
      asm volatile("s_barrier" ::: "memory");
    } else LGKM0();
    if (S == 0) { asm volatile("" ::"v"(wpd)); g.template piece<6, 2>(); g.template piece<5, 2>(); }
    if (S == JS && TYPE == 0) {
      const unsigned jo = WS_W2T + (mi & 3) * 2 * W2T_CT + (mi >> 2) * 5120;
      st128(c, __builtin_bit_cast(v4u, jv0), (jo + (w < 5 ? w * 1024 : W2T_CT + (w - 5) * 1024)) + l * 16);
      if (w < 2) st128(c, __builtin_bit_cast(v4u, jv1), (jo + W2T_CT + (3 + w) * 1024) + l * 16);
    }
    if (S == JS + 2 && TYPE == 0) { if (w == 0) st_flag(c, (WS_FLAGW + ((mi & 3) * 16 + ((mi >> 2) & 7)) * 128 + (mi >> 5) * 8) + (l & 1) * 1024); }
    if (S == 1 && TYPE == 1) st_flag(c, wflag);
    if (S == SCHK - 1) {
      if (TYPE == 0) params();
      const GAS char* sp = (const GAS char*)c.ws + WS_STAT + rb * 4096;
      const GAS char* fp = (const GAS char*)c.ws + WS_FLAGW + (c.ct * 8 + c.wc) * 128;
      asm volatile("s_nop 4\n\tglobal_load_dwordx4 %0, %2, %3 sc1\n\tglobal_load_dwordx2 %1, %4, %5 sc1"
                   : "=&v"(sg), "=&v"(fw) : "v"(wt * 1024 + l * 16), "s"(sp), "v"(i * 8), "s"(fp) : "memory");
    }
    if (S == SCHK) {
      if (TYPE == 0) asm volatile("" : "+v"(sd.pa[0]), "+v"(sd.pb[0]), "+v"(sd.pd[0]));
      const bool need = i < 12 && c.wc + 8 * i < NS2;
      SPIN_LOOP {
        if (__all((!need || (fw.x == c.T && fw.y == c.T2)) && sg.y == c.T && sg.w == c.T2)) break;
        __builtin_amdgcn_s_sleep(1);
        asm volatile("" ::: "memory");
        sg = ld128(c, WS_STAT + rb * 4096 + wt * 1024 + l * 16);
        fw = ld64(c, WS_FLAGW + (c.ct * 8 + c.wc) * 128 + i * 8);
      }
      ldsw<v2u>(L_ST + (wt * 64 + l) * 8, v2u{sg.x, sg.z});
    }
    if (S == 7) {
      glds16<0, L_XT0>(g.rx, sd.v_x0, 0, g.w1k);
      if (w < 3) glds16<0, L_XT1>(w == 2 ? mkrsrc(c.dsk, 1280) : g.rx, sd.v_x1, 0, g.w1k);
    }
    SB0();
    g.template part<2 * S + 1>(fb, fa, [&](auto G) {
      if constexpr (false) {
      } else if constexpr (S + 3 < 10) {
        if (G.v == 0) g.template piece<6, S + 3>();
        if (G.v == (TYPE == 0 ? 2 : 1)) g.template piece<0, S + 3>();
        if (G.v == GA) g.template piece<4, S + 3>();
        if (G.v == GB) g.template piece<1, S + 3>();
      } else if constexpr (S < 9) {
        if (G.v == 1) w2l(IC<S - 7>{}, IC<0>{});
        if (G.v == GA) w2l(IC<S - 7>{}, IC<1>{});
        if (G.v == GB) w2l(IC<S - 7>{}, IC<2>{});
      } else {
        if (G.v == G1<TYPE>::NG - 1) {
          if (TYPE == 0) ldsw<float>(L_CS + ((wn * 4 + wt) * 16 + i) * 4, g.cs0.x);
          if (TYPE == 0) { const unsigned ca = L_CSS + w * 128 + i * 4; ldsw<float>(ca, g.cs1.x); ldsw<float>(ca + 64, g.cs2.x); }
          else {
            const unsigned ca = L_CS3 + w * 128 + i * 4;
#pragma unroll
            for (int a = 0; a < 6; ++a) ldsw<float>(ca + (a >> 1) * 1024 + (a & 1) * 64, g.css[a].x);
          }
        }
      }
      side(IC<2 * S + 1>{}, G);
    });
  };
  step(IC<0>{}); step(IC<1>{}); step(IC<2>{}); step(IC<3>{}); step(IC<4>{}); step(IC<5>{}); step(IC<6>{});
  CSTAMP(1); STAMP(9);
  step(IC<7>{}); step(IC<8>{}); step(IC<9>{});
  asm volatile("" ::"v"(pfd));
  MFMA_DRAIN();
  BAR();
  SB0();
  STAMP(3);
  auto w2c2 = [&]() { w2l(IC<2>{}, IC<0>{}); w2l(IC<2>{}, IC<1>{}); w2l(IC<2>{}, IC<2>{}); w2l(IC<2>{}, IC<3>{}); w2l(IC<2>{}, IC<4>{}); };
  __builtin_amdgcn_s_setprio(3);
  if (TYPE == 0) {
    f32x4 cs[4];
    f32x2 mu[4];
#pragma unroll
    for (int a = 0; a < 4; ++a) cs[a] = ldsr<f32x4>(sd.a_cs ^ (a * 64));
#pragma unroll
    for (int m = 0; m < 4; ++m) mu[m] = ldsr<f32x2>((m < 2 ? sd.a_mu : (sd.a_mu ^ 256)) + (m & 1) * 32);
    auto glu = [&](auto ODD) {
#pragma unroll
      for (int m = 0; m < 4; ++m) {
        const f32x4 mv = {-mu[m].x, -mu[m].x, -mu[m].x, -mu[m].x};
        const float r2 = mu[m].y * mu[m].y;
        const f32x4 h0 = __builtin_elementwise_fma(mv, cs[ODD.v], g.acc[ODD.v][m]) * __builtin_elementwise_fma(mv, cs[2 + ODD.v], g.acc[2 + ODD.v][m]) * r2;
        const f32x4 h1 = __builtin_elementwise_fma(mv, cs[1 - ODD.v], g.acc[1 - ODD.v][m]) * __builtin_elementwise_fma(mv, cs[3 - ODD.v], g.acc[3 - ODD.v][m]) * r2;
        st128(c, v4u{pk2(h0[0], h0[1]), pk2(h0[2], h0[3]), pk2(h1[0], h1[1]), pk2(h1[2], h1[3])}, (m < 2 ? sd.v_hg : (sd.v_hg ^ 2048)) + (m & 1) * 1024);
        asm volatile("" ::: "memory");
        if (IL >= 1) {
          if (m == 0) { w2l(IC<2>{}, IC<0>{}); w2l(IC<2>{}, IC<1>{}); }
          if (m == 1) { w2l(IC<2>{}, IC<2>{}); w2l(IC<2>{}, IC<3>{}); }
          if (m == 2) { w2l(IC<2>{}, IC<4>{}); }
        }
      }
    };
    if (w & 1) glu(IC<1>{}); else glu(IC<0>{});

    SB0();
  }
  if (TYPE == 1) { w2c2(); if (SA > 0 && LW < 1) ldw<0>(c, c.wf[0]); if (SA > 1 && LW < 2) ldw<1>(c, c.wf[1]); SB0(); }
  if (GP) PRI(2);
  cpx v[NU][8], eq[NU], tot[NU], lam[NU], L32[NU], f1[NU], W0[NU], W1[NU], wk0[NU], wk1[NU];
  {
    f32x4 st[4];
#pragma unroll
    for (int e = 0; e < 4; ++e) st[e] = ldsr<f32x4>(sd.a_st + e * 16);
    const unsigned a_pi = sd.a_p8 & ~384u;
#pragma unroll
    for (int u = 0; u < NU; ++u) {
      const unsigned to = L_POW(TYPE, u) - L_POW(TYPE, 2);
      lam[u] = ldsr<cpx>(a_pi + to + 23 * 128);
      const cpx L8 = ldsr<cpx>(a_pi + to + 4 * 128), u2 = ldsr<cpx>(sd.a_p8 + to), u1 = ldsr<cpx>(sd.a_p8 + to + 128), u0 = ldsr<cpx>(sd.a_p8 + to + 256);
      f1[u] = ldsr<cpx>(sd.a_p8 + to + 384); L32[u] = ldsr<cpx>(a_pi + to + 9 * 128);
      W0[u] = ldsr<cpx>(sd.a_w0 + to); W1[u] = ldsr<cpx>(sd.a_w1 + to);
      const cpx lg = ldsr<cpx>(a_pi + (to + 8 * 128) + (wt * 2 + wn) * 128);
      wk0[u] = ldsr<cpx>(sd.a_k0 + to); wk1[u] = ldsr<cpx>(sd.a_k1 + to);
      const unsigned ac = sd.a_css + u * 1024;
      const float csx = sumq(ldsr<float>(ac) + ldsr<float>(ac + 512)), csy = sumq(ldsr<float>(ac + 64) + ldsr<float>(ac + 576));
#pragma unroll
      for (int j = 0; j < 8; ++j) {
        const float mu = (j & 1) ? st[j >> 1].z : st[j >> 1].x, rs = (j & 1) ? st[j >> 1].w : st[j >> 1].y;
        float a = fmaf(-mu, csx, g.accs[2 * u][j >> 2][j & 3]), b = fmaf(-mu, csy, g.accs[2 * u + 1][j >> 2][j & 3]);
        asm("" : "+v"(a), "+v"(b));
        v[u][j] = cpx{a * rs, b * rs};
        asm("" : "+v"(v[u][j]));
      }
      cpx h = v[u][0];
#pragma unroll
      for (int j = 1; j < 8; ++j) h = cfma(lam[u], h, v[u][j]);
      float ax[4], ay[4];
      gatherq(h.x, ax); gatherq(h.y, ay);
      eq[u] = cfma(u0, cpx{ax[0], ay[0]}, cfma(u1, cpx{ax[1], ay[1]}, cpx{u2.x * ax[2], u2.x * ay[2]}));
      tot[u] = cfma(L8, eq[u], h);
      if (l >= 48) ldsw<f32x2>(sd.a_aw + u * 1024, tot[u]);
      wk0[u] = cmul(wk0[u], lg); wk1[u] = cmul(wk1[u], lg);
    }
  }
  SB0();
  if (GP) PRI(3);
  BAR();
  cpx pm[NU];
#pragma unroll
  for (int u = 0; u < NU; ++u) {
    const f32x4 t = ldsr<f32x4>(sd.a_ar + u * 1024);
    pm[u] = cfma(W0[u], cpx{t.x, t.y}, cmul(W1[u], cpx{t.z, t.w}));
  }
  if (w == 7) {
#pragma unroll
    for (int u = 0; u < NU; ++u) {
      const cpx P = cfma(L32[u], cpx{sumq(pm[u].x), sumq(pm[u].y)}, tot[u]);
      st128(c, v4u{__float_as_uint(P.x), c.T, __float_as_uint(P.y), c.T2}, sd.v_pub + u * 256);
    }
  }
  SB0();
  if (TYPE == 0) {
    if (w == 7) waitv<1>(); else waitv<0>();
    st_flag(c, sd.v_fg);
    STAMP(4);
  }
  if (B2) asm volatile("s_barrier" ::: "memory");
  auto ldwr = [&](auto A, auto B) {
    if constexpr (A.v <= 0 && 0 < B.v && LW <= 0) ldw<0>(c, c.wf[0]);
    if constexpr (A.v <= 1 && 1 < B.v && LW <= 1) ldw<1>(c, c.wf[1]);
    if constexpr (A.v <= 2 && 2 < B.v && LW <= 2) ldw<2>(c, c.wf[2]);
    if constexpr (A.v <= 3 && 3 < B.v) ldw<3>(c, c.wf[3]);
    SB0();
  };
  PRI(2);
  if (TYPE == 0) { if (IL == 0) w2c2(); ldwr(IC<0>{}, IC<NA>{}); }
  PRI(3);
  SB0();
  {
    const f32x2 st = ldsr<f32x2>(sd.a_bs);
    const float nm = -st.x * st.y;
    auto item = [&](int a) {
      const unsigned xo = sd.a_bx + a * 32;
      const v2u xv = ldsr<v2u>(xo < 8192 ? L_XT0 + xo : L_XT1 - 8192 + xo), dv = ldsr<v2u>(L_XT1 + 2048 + q * 8 + a * 32);
      const unsigned x0 = xv.x, x1 = xv.y, d0 = dv.x, d1 = dv.y;
      const float xf[4] = {bf2f(x0), bfhi(x0), bf2f(x1), bfhi(x1)}, df[4] = {bf2f(d0), bfhi(d0), bf2f(d1), bfhi(d1)};
      f32x4 r;
#pragma unroll
      for (int e = 0; e < 4; ++e) {
        float xn = fmaf(xf[e], st.y, nm);
        asm("" : "+v"(xn));
        r[e] = fmaf(xn, df[e], xf[e]);
      }
      return r;
    };
    const f32x4 z = {0.f, 0.f, 0.f, 0.f};
    if (wn == 0) { c.acc0[0] = item(0); c.acc0[1] = item(1); c.acc0[2] = item(2); c.acc0[3] = z; c.acc0[4] = z; }
    else { c.acc0[0] = z; c.acc0[1] = z; c.acc0[2] = z; c.acc0[3] = item(3); c.acc0[4] = item(4); }
#pragma unroll
    for (int a = 0; a < 5; ++a) PIN(c.acc0[a]);
    SB0();
  }
  if (NOJB) BAR();
  v4u lk0[NU], lk1[NU];
#pragma unroll
  for (int u = 0; u < NU; ++u) { lk0[u] = ld128(c, sd.v_lk0 + u * 256); lk1[u] = ld128(c, sd.v_lk1 + u * 256); }
  if (TYPE == 0) c.fl = ld64(c, c.vfl);
  PRI(1);
  if (TYPE == 0) ldwr(IC<NA>{}, IC<NA + NB>{}); else ldwr(IC<SA>{}, IC<4>{});
  PRI(3);
  auto hwait = [&]() {
    if ((__ballot(c.fl.x == c.T && c.fl.y == c.T2) & HM) != HM) {
      SPIN_LOOP {
        asm volatile("" ::: "memory");
        c.fl = ld64(c, c.vfl);
        if ((__ballot(c.fl.x == c.T && c.fl.y == c.T2) & HM) == HM) break;
      }
    }
    __builtin_amdgcn_fence(__ATOMIC_ACQUIRE, "wavefront");
    PRI(1);
    if (HF) c.fl = ld64(c, c.vfl);
    ldh<0>(c, c.hf[0]); ldh<1>(c, c.hf[1]);
    if constexpr (HP > 2) ldh<2>(c, c.hf[HP > 2 ? 2 : 0]);
    if constexpr (HP > 3) ldh<3>(c, c.hf[HP > 3 ? 3 : 0]);
    PRI(3);
    SB0();
  };
  if (TYPE == 0) hwait();
  auto lkok = [&]() {
    bool ok = true;
#pragma unroll
    for (int u = 0; u < NU; ++u)
      ok = ok && (sd.v_lk0 >= OOB || (lk0[u].y == c.T && lk0[u].w == c.T2)) && (sd.v_lk1 >= OOB || (lk1[u].y == c.T && lk1[u].w == c.T2));
    return __all(ok);
  };
  if (!lkok()) {
    SPIN_LOOP {
      asm volatile("" ::: "memory");
#pragma unroll
      for (int u = 0; u < NU; ++u) { lk0[u] = ld128(c, sd.v_lk0 + u * 256); lk1[u] = ld128(c, sd.v_lk1 + u * 256); }
      if (lkok()) break;
    }
  }
  if (TYPE == 1) c.fl = ld64(c, c.vfl);
  SB0();
  STAMP(5);
#pragma unroll
  for (int u = 0; u < NU; ++u) {
    const unsigned x0 = lk0[u].x, y0 = lk0[u].z, x1 = lk1[u].x, y1 = lk1[u].z;
    const cpx cy = cfma(wk1[u], cpx{__uint_as_float(x1), __uint_as_float(y1)}, cfma(wk0[u], cpx{__uint_as_float(x0), __uint_as_float(y0)}, pm[u]));
    cpx h = cfma(f1[u], cpx{sumq(cy.x), sumq(cy.y)}, eq[u]);
#pragma unroll
    for (int j = 0; j < 8; ++j) { h = cfma(lam[u], h, v[u][j]); v[u][j] = h; }
    ldsw<v4u>(sd.a_tw, v4u{pk2(v[u][0].x, v[u][1].x), pk2(v[u][2].x, v[u][3].x), pk2(v[u][4].x, v[u][5].x), pk2(v[u][6].x, v[u][7].x)});
    ldsw<v4u>(sd.a_tw + 16384, v4u{pk2(v[u][0].y, v[u][1].y), pk2(v[u][2].y, v[u][3].y), pk2(v[u][4].y, v[u][5].y), pk2(v[u][6].y, v[u][7].y)});
    asm volatile("s_waitcnt lgkmcnt(0)" ::: "memory");
#pragma unroll
    for (int mm = 0; mm < 2; ++mm) st128(c, __builtin_bit_cast(v4u, cat(trread(sd.a_tr + mm * 8), trread(sd.a_tr + mm * 8 + 16384))), sd.v_hs[u] + mm * 1024);
    if (u + 1 < NU) asm volatile("s_waitcnt lgkmcnt(0)" ::: "memory");
  }
  SB0();
  c.vfs = sd.v_fs;
  if (TYPE == 1) {
    if ((__ballot(c.fl.x == c.T && c.fl.y == c.T2) & HM) != HM) {
      waitv<0>();
      st_flag(c, sd.v_fs);
      c.vfs = OOB;
    }
    STAMP(4);
    hwait();
  }
  if (TYPE == 1) {
    if (si == 39) {
      SPIN_LOOP {
        bool ok = true;
        asm volatile("" ::: "memory");
#pragma unroll
        for (int k = 0; k < 4; ++k) {
          const v4u t = ld128(c, WS_STAT + (k * 512 + w * 64 + l) * 16);
          ok = ok && t.y == c.T && t.w == c.T2;
        }
        if (__all(ok)) break;
        __builtin_amdgcn_s_sleep(1);
      }
    }
  }
  c.bump = TYPE == 1 && si == 39;
  SB0();
}

DI void phase2(Ctx& c, const u16* dsk, float* out) {
  const int w = c.w, rt = c.rt, ct = c.ct;
  v4u wf[12][5], hf[12][4];
  f32x4 acc[4][5];
  v2u fl = c.fl;
  STAMP(6); CSTAMP(2);
#pragma unroll
  for (int a = 0; a < 5; ++a) acc[0][a] = c.acc0[a];
  auto body = [&](auto JJ) {
    constexpr int j = JJ.v;
    if constexpr (lds_chunk(j + 1) >= 0) {
      constexpr int ch = lds_chunk(j + 1);
      int l = threadIdx.x & 63; PIN(l);
      const unsigned a4 = tile_base(ch, 0) + w * 4096 + l * 16;
#pragma unroll
      for (int a = 0; a < 4; ++a) wf[j + 1][a] = ldsr<v4u>(a4 + a * 1024);
      wf[j + 1][4] = ldsr<v4u>(tile_base(ch, 4) + w * 1024 + l * 16);
    }
    if constexpr (j < 4) {
#pragma unroll
      for (int a = 0; a < 5; ++a) wf[j][a] = c.wf[j][a];
    }
    if constexpr (j < HP) {
#pragma unroll
      for (int m = 0; m < 4; ++m) hf[j][m] = c.hf[j][m];
    }
    if constexpr (j + WA < 12 && j + WA >= 4 && lds_chunk(j + WA) < 0) { ldw<j + WA>(c, wf[j + WA]); SB0(); }
    if constexpr (j + HA == 6 || j + HA == 8) {
      const unsigned long long msk = j + HA == 6 ? (15ull << 12) : ((c.wc >= 6 ? (1ull << 22) : (1ull << 24)) - (1ull << 16));
      if (__builtin_expect((__ballot(fl.x == c.T && fl.y == c.T2) & msk) != msk, 0)) {
        SPIN_LOOP {
          asm volatile("" ::: "memory");
          const v2u f = ld64(c, c.vfl);
          if ((__ballot(f.x == c.T && f.y == c.T2) & msk) == msk) break;
        }
      }
      __builtin_amdgcn_fence(__ATOMIC_ACQUIRE, "wavefront");
    }
    if constexpr (j + HA == 5 || j + HA == 7) fl = ld64(c, c.vfl);
    if constexpr (j + HA < 12 && j + HA >= HP) ldh<j + HA>(c, hf[j + HA]);
    SB0();
#pragma unroll
    for (int a = 0; a < 5; ++a)
#pragma unroll
      for (int m = 0; m < 4; ++m) {
        if (j == 0 && m > 0) mfz(acc[m][a], __builtin_bit_cast(bf16x8, wf[j][a]), __builtin_bit_cast(bf16x8, hf[j][m]));
        else mfa(acc[m][a], __builtin_bit_cast(bf16x8, wf[j][a]), __builtin_bit_cast(bf16x8, hf[j][m]));
      }
    if constexpr (j == JF) {
      if (c.cg < 27) waitv<(HA >= HP ? 4 : 0)>();
      PRI(3);
      st_flag(c, c.vfs);
      SB0();
      __builtin_amdgcn_s_setprio(FP ? 3 : 0);
    }
    if constexpr (FP && (j == 2 || j == 5 || j == 8)) __builtin_amdgcn_s_setprio(j == 2 ? 2 : (j == 5 ? 1 : 0));
    if constexpr (j == JB) {
      if (!NOJB || c.bump) BAR();
      __builtin_amdgcn_raw_buffer_store_b32(c.T, c.rws, (c.bump && threadIdx.x == 0) ? WS_EPOCH : OOB, 0, 16);
    }
    if (j == 1) STAMP(10);
    if (j == 3) STAMP(11);
    if (j == 4) STAMP(12);
    if (j == 7) STAMP(13);
    SB0();
  };
  body(IC<0>{}); body(IC<1>{}); body(IC<2>{}); body(IC<3>{}); body(IC<4>{}); body(IC<5>{});
  body(IC<6>{}); body(IC<7>{}); body(IC<8>{}); body(IC<9>{}); body(IC<10>{});
  int l = threadIdx.x & 63; PIN(l);
  unsigned b4 = w * 4096 + l * 16, b4h = b4 + 65536, b1 = 98304 + w * 1024 + l * 16, b2 = 147456 + w * 2048 + l * 16;
  PIN(b4); PIN(b4h); PIN(b1); PIN(b2);
  const int tl = ((w >> 1) & 1) * 32 + 8 * ((l & 15) >> 2) + 4 * (w & 1) + (l & 3);
  GAS float* op = (GAS float*)out + (size_t)(c.rb * 256 + rt * 64 + tl) * D_ + ct * 80 + (w >> 2) * 16 + 4 * (l >> 4);
  PIN(op);
  unsigned r4[4], r1[4], r2;
#pragma unroll
  for (int x = 0; x < 4; ++x) { r4[x] = ((w & 3) ^ x) * 4096 + l * 16 + (x == 1 ? 65536 : 0); r1[x] = 98304 + ((w & 3) ^ x) * 1024 + l * 16; PIN(r4[x]); PIN(r1[x]); }
  r2 = 147456 + ((w & 3) ^ 3) * 2048 + l * 16; PIN(r2);
  auto redw = [&](int t) {
    const int a = t >> 2, m = t & 3;
    const unsigned tb = tile_base(m, a), ts = tile_wstride(m, a);
    ldsw<f32x4>(ts == 4096 ? (tb >= 65536 ? b4h + (tb - 65536) : b4 + tb) : (ts == 1024 ? b1 + (tb - 98304) : b2 + (tb - 147456)), acc[m][a]);
  };
  if (TI && c.wc < 6) {
#pragma unroll
    for (int t = 0; t < 20; ++t) {
      mfa(acc[t & 3][t >> 2], __builtin_bit_cast(bf16x8, wf[11][t >> 2]), __builtin_bit_cast(bf16x8, hf[11][t & 3]));
      if (t >= 8) redw(t - 8);
      SB0();
    }
    MFMA_DRAIN();
    SB0();
#pragma unroll
    for (int t = 12; t < 20; ++t) redw(t);
  } else {
    if (!TI && c.wc < 6) body(IC<11>{});
    MFMA_DRAIN();
    SB0();
#pragma unroll
    for (int t = 0; t < 20; ++t) redw(t);
  }
  STAMP(7);
  BAR();
  auto sum = [&](auto AA) {
    constexpr int a = AA.v;
    f32x4 p[8];
#pragma unroll
    for (int x = 0; x < 4; ++x)
#pragma unroll
      for (int hh = 0; hh < 2; ++hh) {
        const unsigned tb = tile_base(x, a), ts = tile_wstride(x, a);
        p[x * 2 + hh] = ldsr<f32x4>(ts == 4096 ? r4[x] + (tb - (x == 1 ? 65536 : 0)) + hh * 16384 : (ts == 1024 ? r1[x] + (tb - 98304) + hh * 4096 : r2 + (tb - 147456) + hh * 8192));
      }
    return ((p[0] + p[1]) + (p[2] + p[3])) + ((p[4] + p[5]) + (p[6] + p[7]));
  };
  if (w < 4) {
    const f32x4 s0 = sum(IC<0>{}), s1 = sum(IC<2>{}), s2 = sum(IC<4>{});
    *(GAS f32x4*)op = s0; *(GAS f32x4*)(op + 32) = s1; *(GAS f32x4*)(op + 64) = s2;
  } else {
    const f32x4 s0 = sum(IC<1>{}), s1 = sum(IC<3>{});
    *(GAS f32x4*)op = s0; *(GAS f32x4*)(op + 32) = s1;
  }
}

__global__ void __launch_bounds__(512, 2)
fused_kernel(const u16* x, const u16* lre, const u16* lim, const u16* bre, const u16* bim, const u16* cre, const u16* cim, const u16* ldt,
             const u16* dsk, const u16* wg, const u16* wu, const u16* wd, float* out, char* ws) {
  Ctx c;
  asm volatile("s_getpc_b64 %0" : "=s"(c.pc));
  c.dsk = dsk; c.x = x; c.bre = bre; c.bim = bim; c.cre = cre; c.cim = cim; c.wg = wg; c.wu = wu; c.wd = wd; c.ws = ws;
  c.lane = threadIdx.x & 63;
  c.w = __builtin_amdgcn_readfirstlane(threadIdx.x >> 6);
  const int xc = blockIdx.x & 7, ix = blockIdx.x >> 3;
  c.rb = (xc >> 1) * 2 + (ix >> 4); c.cg = (xc & 1) * 16 + (ix & 15);
  c.rt = c.cg >> 3; c.ct = c.cg & 7;
  c.shr = ix >> 3;
  c.wc = (c.w + (ROTC ? c.ct + 2 * c.shr : 0)) & 7;
  c.rws = __builtin_amdgcn_make_buffer_rsrc(ws, 0, WS_END, 0x00020000);
  STAMP(0);
  if (__builtin_expect(c.cg < 27, 1)) phase1<0>(c, lre, lim, ldt);
  else phase1<1>(c, lre, lim, ldt);
  phase2(c, dsk, out);
}

extern "C" void kernel_launch(void* const* d_in, const int* in_sizes, int n_in, void* d_out, int out_size, void* d_ws, size_t ws_size,
                              hipStream_t stream) {
  (void)in_sizes; (void)n_in; (void)out_size; (void)ws_size;
  (void)hipFuncSetAttribute((const void*)fused_kernel, hipFuncAttributeMaxDynamicSharedMemorySize, LDS_BYTES);
  fused_kernel<<<256, 512, LDS_BYTES, stream>>>((const u16*)d_in[0], (const u16*)d_in[1], (const u16*)d_in[2], (const u16*)d_in[3], (const u16*)d_in[4],
                                                (const u16*)d_in[5], (const u16*)d_in[6], (const u16*)d_in[7], (const u16*)d_in[8], (const u16*)d_in[10],
                                                (const u16*)d_in[11], (const u16*)d_in[12], (float*)d_out, (char*)d_ws + ((ws_size / 2) & ~(size_t)0x1fffff));
}
```

```cpp
#include <hip/hip_runtime.h>
#include <stdint.h>

#define GAS __attribute__((address_space(1)))
#define LAS __attribute__((address_space(3)))
#define DI __device__ __forceinline__
typedef unsigned short u16;
typedef short bf16x8 __attribute__((ext_vector_type(8)));
typedef short s16x4 __attribute__((ext_vector_type(4)));
typedef __bf16 bf2 __attribute__((ext_vector_type(2)));
typedef float f32x4 __attribute__((ext_vector_type(4)));
typedef float f32x2 __attribute__((ext_vector_type(2)));
typedef unsigned v4u __attribute__((ext_vector_type(4)));
typedef unsigned v2u __attribute__((ext_vector_type(2)));

constexpr int L_ = 2048, D_ = 640, F_ = 1728;
constexpr int NS2 = 94;
constexpr int ustep(int u) { return u >= 27 ? 27 + u : 67 + u; }
constexpr unsigned WS_EPOCH = 0;
constexpr unsigned WS_STAT = 4096;
constexpr unsigned WS_AGG = WS_STAT + 2048 * 16;
constexpr unsigned AGG_RB = 42 * 256;
constexpr unsigned WS_FLAGH = WS_AGG + 8 * AGG_RB;
constexpr unsigned WS_FLAGW = WS_FLAGH + 32 * 8 * 256;
constexpr unsigned WS_CTL_END = WS_FLAGW + 8 * 8 * 128;
constexpr unsigned WS_W2T = 262144;
constexpr unsigned W2T_CT = NS2 * 5 * 1024;
constexpr unsigned WS_H = WS_W2T + 8 * W2T_CT;
constexpr unsigned H_RT = NS2 * 4 * 1024;
constexpr unsigned WS_END = WS_H + 32 * H_RT;
constexpr unsigned OOB = 0x70000000u;
static_assert(WS_CTL_END <= WS_W2T && WS_H % 4096 == 0, "ws map");
constexpr unsigned LX(int k) { return k * 32768; }
constexpr unsigned LWB(int k) { return 98304 + ((k + 1) % 3) * 16384; }
constexpr unsigned LWS(int k) { return 147456 + k * 4096; }
constexpr unsigned L_ST = 159744;
constexpr unsigned L_CS = L_ST + 2048;
constexpr unsigned L_CSS = L_CS + 512;
constexpr unsigned L_CS3 = LWB(2) + 8192;
constexpr unsigned L_COEF = L_CSS + 1024;
constexpr unsigned L_POW(int type, int u) { return type == 0 ? LWB(2) + 8192 : (u == 1 ? LWS(0) : LWB(u) + 12288); }
constexpr unsigned L_AGG = LWS(2);
constexpr unsigned L_XT0 = LWB(1) + 8192;
constexpr unsigned L_XT1 = LWS(1);
constexpr unsigned LDS_BYTES = 163840;
static_assert(L_COEF + 512 <= LDS_BYTES && LWB(2) == LX(2) + 32768, "lds map");
constexpr unsigned tile_base(int ch, int j) {
  return ch < 3 ? (j < 4 ? LX((ch + 1) % 3) + j * 1024 : LWB((ch + 1) % 3)) : (j < 3 ? LWB(j) + 8192 : LWS(0) + (j - 3) * 1024);
}
constexpr unsigned tile_wstride(int ch, int j) { return ch < 3 ? (j < 4 ? 4096 : 1024) : (j < 3 ? 1024 : 2048); }
constexpr int SPIN_CAP = 1 << 16;
constexpr unsigned CODE_PF = 48000;

DI float bf2f(unsigned v) { return __uint_as_float(v << 16); }
DI float bfhi(unsigned v) { return __uint_as_float(v & 0xffff0000u); }
DI unsigned pk2(float a, float b) { bf2 v = {(__bf16)a, (__bf16)b}; return __builtin_bit_cast(unsigned, v); }
DI float rbf(float a) { return bf2f(pk2(a, 0.f) & 0xffffu); }
DI void mfa(f32x4& c, bf16x8 a, bf16x8 b) { asm volatile("v_mfma_f32_16x16x32_bf16 %0, %1, %2, %0" : "+v"(c) : "v"(a), "v"(b)); }
DI void mfz(f32x4& c, bf16x8 a, bf16x8 b) { asm volatile("v_mfma_f32_16x16x32_bf16 %0, %1, %2, 0" : "=&v"(c) : "v"(a), "v"(b)); }
template <bool Z> DI void mf(f32x4& c, bf16x8 a, bf16x8 b) { if (Z) mfz(c, a, b); else mfa(c, a, b); }
#define MFMA_DRAIN() asm volatile("s_nop 7\n\ts_nop 1" ::: "memory")
DI v4u mkrsrc(const void* p, unsigned bytes) {
  uint64_t a = (uint64_t)p;
  v4u r; r.x = (unsigned)a; r.y = (unsigned)(a >> 32) & 0xffffu; r.z = bytes; r.w = 0x00020000u;
  return r;
}
template <unsigned SI, unsigned LI> DI void glds16(v4u rs, unsigned voff, unsigned sb, unsigned lb) {
  unsigned t;
  asm volatile("s_add_u32 m0, %4, %6\n\ts_add_u32 %0, %3, %5\n\tbuffer_load_dwordx4 %1, %2, %0 offen lds"
               : "=&s"(t) : "v"(voff), "s"(rs), "s"(sb), "s"(lb), "i"(SI), "i"(LI) : "memory", "scc");
}
template <unsigned SI, unsigned LI> DI void glds16sc1(v4u rs, unsigned voff, unsigned lb) {
  unsigned t;
  asm volatile("s_add_u32 m0, %3, %5\n\ts_mov_b32 %0, %4\n\tbuffer_load_dwordx4 %1, %2, %0 offen sc1 lds"
               : "=&s"(t) : "v"(voff), "s"(rs), "s"(lb), "i"(SI), "i"(LI) : "memory", "scc");
}
template <class T> DI T ldsr(unsigned a) { return *(LAS T*)(uintptr_t)a; }
template <class T> DI void ldsw(unsigned a, T v) { *(LAS T*)(uintptr_t)a = v; }
DI s16x4 trread(unsigned a) { return __builtin_amdgcn_ds_read_tr16_b64_v4i16((LAS s16x4*)(uintptr_t)a); }
DI bf16x8 cat(s16x4 lo, s16x4 hi) { return __builtin_shufflevector(lo, hi, 0, 1, 2, 3, 4, 5, 6, 7); }
template <int N> DI void waitv() { asm volatile("s_waitcnt vmcnt(%0)" ::"n"(N) : "memory"); }
#define BAR() asm volatile("s_waitcnt lgkmcnt(0)\n\ts_barrier" ::: "memory")
#define LGKM0() __builtin_amdgcn_s_waitcnt(0xc07f)
#define SB0() __builtin_amdgcn_sched_barrier(0)
#define PRI(n) do { SB0(); __builtin_amdgcn_s_setprio(n); SB0(); } while (0)
#define PIN(x) asm volatile("" : "+v"(x))
#define SPIN_LOOP _Pragma("nounroll") for (int sp = 0; sp < SPIN_CAP; ++sp)
template <int N> DI float ror(float v) { return __builtin_bit_cast(float, __builtin_amdgcn_update_dpp(0, __builtin_bit_cast(int, v), 0x120 + N, 0xf, 0xf, false)); }
DI void sw16(float v, float& e, float& o) { unsigned u = __float_as_uint(v); auto r = __builtin_amdgcn_permlane16_swap(u, u, false, false); e = __uint_as_float(r[0]); o = __uint_as_float(r[1]); }
DI void sw32(float v, float& lo, float& hi) { unsigned u = __float_as_uint(v); auto r = __builtin_amdgcn_permlane32_swap(u, u, false, false); lo = __uint_as_float(r[0]); hi = __uint_as_float(r[1]); }
DI float sumq(float v) { float a, b; sw16(v, a, b); v = a + b; sw32(v, a, b); return a + b; }
DI float sumr(float v) { v += ror<8>(v); v += ror<4>(v); v += ror<2>(v); v += ror<1>(v); return v; }
DI void gatherq(float v, float (&a)[4]) { float e, o; sw16(v, e, o); sw32(e, a[0], a[2]); sw32(o, a[1], a[3]); }
typedef f32x2 cpx;
DI cpx cfma(cpx a, cpx b, cpx c) {
  cpx r;
  asm("v_pk_fma_f32 %0, %1, %2, %3 op_sel:[1,1,0] op_sel_hi:[1,0,1] neg_lo:[1,0,0]\n\tv_pk_fma_f32 %0, %1, %2, %0 op_sel_hi:[0,1,1]" : "=&v"(r) : "v"(a), "v"(b), "v"(c));
  return r;
}
DI cpx cmul(cpx a, cpx b) {
  cpx r;
  asm("v_pk_mul_f32 %0, %1, %2 op_sel:[1,1] op_sel_hi:[1,0] neg_lo:[1,0]\n\tv_pk_fma_f32 %0, %1, %2, %0 op_sel_hi:[0,1,1]" : "=&v"(r) : "v"(a), "v"(b));
  return r;
}
template <int V> struct IC { static constexpr int v = V; };

struct Ctx {
  const u16 *dsk, *x, *bre, *bim, *cre, *cim, *wg, *wu, *wd;
  char* ws;
  __amdgpu_buffer_rsrc_t rws;
  v2u tag;
  unsigned T, T2;
  int lane, w, rb, cg, rt, ct;
  f32x4 acc0[5];
  unsigned vw2, vh2, vfl;
  v4u wf[4][5], hf[3][4];
  v2u fl;
  unsigned vfs;
  unsigned long long pc;
};
DI v4u ld128(const Ctx& c, unsigned vo, unsigned so = 0) { return __builtin_amdgcn_raw_buffer_load_b128(c.rws, vo, so, 16); }
DI v2u ld64(const Ctx& c, unsigned vo) { return __builtin_amdgcn_raw_buffer_load_b64(c.rws, vo, 0, 16); }
DI void st128(const Ctx& c, v4u v, unsigned vo) { __builtin_amdgcn_raw_buffer_store_b128(v, c.rws, vo, 0, 16); }
DI void st_flag(const Ctx& c, unsigned vo) { __builtin_amdgcn_raw_buffer_store_b64(c.tag, c.rws, vo, 0, 16); }
DI unsigned long long fresh(const Ctx& c, v2u f) { return __ballot(f.x == c.T && f.y == c.T2); }

DI float fexp(float x) {
  const float t = x * 1.44269502162933349609375f;
  const float r = fmaf(x, 1.925963033500011e-8f, fmaf(x, 1.44269502162933349609375f, -t));
  const float e = __builtin_amdgcn_exp2f(t);
  return fmaf(e, r * 0.693147182464599609375f, e);
}
DI void fsincos(float z, float& sn, float& cs) {
  const float k = rintf(z * 0.63661977236758134f);
  float r = fmaf(k, -1.5707963705062866f, z);
  r = fmaf(k, 4.3711388286737929e-08f, r);
  r = fmaf(k, 1.7151245100058819e-15f, r);
  const float r2 = r * r;
  const float sp = fmaf(r * r2, fmaf(r2, fmaf(r2, -1.9515295891e-4f, 8.3321608736e-3f), -1.6666654611e-1f), r);
  const float cp = fmaf(r2 * r2, fmaf(r2, fmaf(r2, 2.443315711809948e-5f, -1.388731625493765e-3f), 4.166664568298827e-2f), fmaf(r2, -0.5f, 1.f));
  const int n = (int)k;
  sn = (n & 1) ? cp : sp; cs = (n & 1) ? sp : cp;
  if (n & 2) sn = -sn;
  if ((n + 1) & 2) cs = -cs;
}
constexpr int LP(int ch) { return ch == 0 ? 7 : (ch == 1 ? 9 : 10); }
constexpr int lds_chunk(int j) { return j == 7 ? 0 : (j == 9 ? 1 : (j == 10 ? 2 : -1)); }
template <int J> DI void ldw(const Ctx& c, v4u (&d)[5]) {
  const unsigned vo = J == 11 ? (c.w < 6 ? c.vw2 : OOB) : c.vw2;
#pragma unroll
  for (int a = 0; a < 4; ++a) d[a] = ld128(c, vo + a * 1024, J * 40960);
  d[4] = ld128(c, vo, J * 40960 + 4096);
}
template <int J> DI void ldh(const Ctx& c, v4u (&d)[4]) {
  const unsigned vo = J == 11 ? (c.w < 6 ? c.vh2 : OOB) : c.vh2;
#pragma unroll
  for (int m = 0; m < 4; ++m) d[m] = ld128(c, vo ^ (m * 1024), J * 32768);
}

template <int TYPE> struct G1 {
  static constexpr int NU = TYPE == 0 ? 1 : 3;
  static constexpr int NG = TYPE == 0 ? 10 : 6;
  static constexpr int NR = TYPE == 0 ? 14 : 8;
  static constexpr int NP = TYPE == 0 ? 6 : 5;
  struct Frags { bf16x8 x[TYPE == 0 ? 4 : 2]; s16x4 wl[4], wh[4]; bf16x8 s[2 * NU]; };
  v4u rx, rw0, rw1, rs;
  unsigned vx0, vx1, vw, vs;
  unsigned sx, sw, ss, ss2;
  unsigned ax[2][2][2], aw[4], as[2];
  f32x4 acc[4][4];
  f32x4 accs[2 * NU][2];
  f32x4 cs0, css[2 * NU];
  bf16x8 ones;
  int w;
  unsigned w1k, w4k;

  DI void init_stage(const Ctx& c, int unit0) {
    const int l = c.lane;
    w = c.w; w1k = w * 1024; w4k = w * 4096;
    rx = mkrsrc(c.x, L_ * D_ * 2);
    const unsigned ch = (l & 7) ^ ((l >> 3) & 3);
    vx0 = (l >> 3) * 1280 + ch * 16;
    vx1 = (l >> 3) * 1280 + (ch ^ 4) * 16;
    sx = (c.rb * 256 + w * 32) * 1280;
    rs = mkrsrc((w & 2) ? c.bim : c.bre, 640 * 640 * 2);
    vs = (l >> 3) * 1280 + (((l & 7) ^ (l >> 3)) * 16);
    if (TYPE == 0) {
      rw0 = mkrsrc(c.wg, D_ * F_ * 2); rw1 = mkrsrc(c.wu, D_ * F_ * 2);
      const unsigned key2 = ((l >> 4) & 1) | ((w & 1) << 1);
      vw = (l >> 3) * 3456 + (((l & 7) ^ (key2 << 1)) * 16);
      sw = w * 8 * 3456 + c.cg * 128;
      ss2 = (unit0 * 16 + (w & 1) * 8) * 1280;
    } else {
      ss = (min(unit0 + (w >> 2), 39) * 16 + (w & 1) * 8) * 1280;
      ss2 = (min(unit0 + 2, 39) * 16 + (w & 1) * 8) * 1280;
    }
  }
  DI void init_read(const Ctx& c) {
    const int l = c.lane, i = l & 15, q = l >> 4;
    const int wt = w & 3, wn = w >> 2;
#pragma unroll
    for (int kk = 0; kk < 2; ++kk) {
      const unsigned ch = (((kk * 4 + q) ^ (i & 7)) * 16);
#pragma unroll
      for (int pr = 0; pr < (TYPE == 0 ? 2 : 1); ++pr) {
        ax[0][pr][kk] = (wt * 64 + (pr ^ wn) * 32 + 8 * (i >> 2) + (i & 3)) * 128 + ch;
        ax[1][pr][kk] = ax[0][pr][kk] + 65536;
        PIN(ax[0][pr][kk]); PIN(ax[1][pr][kk]);
      }
      as[kk] = (TYPE == 0 ? LWS(0) : LWB(2)) + i * 128 + ch;
      PIN(as[kk]);
    }
    if (TYPE == 0) {
      const int r = (l >> 2) & 3, cc = l & 3;
      const unsigned key2 = ((r >> 1) & 1) | ((q & 1) << 1);
#pragma unroll
      for (int a = 0; a < 4; ++a) {
        const int act = a ^ wt;
        aw[a] = LWB(2) + (act >> 1) * 8192 + (8 * q + r) * 128 + (((2 * wn + (act & 1)) ^ key2) * 32) + cc * 8;
        PIN(aw[a]);
      }
    }
#pragma unroll
    for (int a = 0; a < 2 * NU; ++a) { css[a] = f32x4{0.f, 0.f, 0.f, 0.f}; PIN(css[a]); }
    ones = bf16x8{0x3f80, 0x3f80, 0x3f80, 0x3f80, 0x3f80, 0x3f80, 0x3f80, 0x3f80};
    PIN(ones);
  }
  template <int P, int S> DI void piece() {
    constexpr int K = S % 3;
    if constexpr (P < 4) glds16<S * 128 + P * 10240, LX(K) + P * 1024>(rx, (P & 1) ? vx1 : vx0, sx, w4k);
    else if constexpr (P < 6) {
      if constexpr (TYPE == 0) glds16<S * (64 * 3456), LWB(K) + (P - 4) * 8192>(P == 4 ? rw0 : rw1, vw, sw, w1k);
      else if constexpr (P == 4) glds16<S * 128, LWB(K)>(rs, vs, ss, w1k);
    } else {
      if (w < 4) glds16<S * 128, (TYPE == 0 ? LWS(K) : LWB(K) + 8192)>(rs, vs, ss2, w1k);
    }
  }
  template <int S> DI void half0() { piece<6, S>(); piece<0, S>(); piece<4, S>(); piece<1, S>(); }
  template <int S> DI void half1() { piece<2, S>(); piece<5, S>(); piece<3, S>(); }

  template <int K, int KK, int R> DI void rd(Frags& f) {
    constexpr unsigned ow = LWB(K) - LWB(2), ox = K == 1 ? 32768 : 0;
    if constexpr (TYPE == 0) {
      if (R == 0) f.wl[0] = trread(aw[0] + (ow + KK * 4096));
      else if (R == 1) f.wh[0] = trread(aw[0] + (ow + KK * 4096 + 512));
      else if (R < 6) f.x[(R - 2) & 3] = ldsr<bf16x8>(ax[K == 2][((R - 2) >> 1) & 1][KK] + (ox + ((R - 2) & 1) * 512));
      else if (R < 12) {
        constexpr int a = ((R - 4) >> 1) & 3;
        if (R & 1) f.wh[a] = trread(aw[a] + (ow + KK * 4096 + 512)); else f.wl[a] = trread(aw[a] + (ow + KK * 4096));
      } else f.s[R & 1] = ldsr<bf16x8>(as[KK] + (K * 4096 + (R & 1) * 2048));
    } else {
      if (R == 1 || R == 2) f.x[(R - 1) & 1] = ldsr<bf16x8>(ax[K == 2][0][KK] + (ox + ((R - 1) & 1) * 512));
      else { constexpr int a = R == 0 ? 0 : R - 2; f.s[a] = ldsr<bf16x8>(as[KK] + (ow + a * 2048)); }
    }
  }
  template <bool Z, int M> DI void mm(const Frags& f) {
    if constexpr (TYPE == 0 && M < 16) mf<Z>(acc[M >> 2][M & 3], cat(f.wl[M >> 2], f.wh[M >> 2]), f.x[M & 3]);
    else { constexpr int a = (M - (TYPE == 0 ? 16 : 0)) >> 1; mf<Z>(accs[a][M & 1], f.x[M & 1], f.s[a]); }
  }
  template <int SUB, int G, class H> DI void group(const Frags& cur, Frags& nxt, H& hook) {
    constexpr int N = SUB + 1, K = (N >> 1) % 3, KK = N & 1;
    constexpr bool RD = N < 20;
    mm<SUB == 0, 2 * G>(cur);
    if constexpr (RD && 2 * G < NR) rd<K, KK, 2 * G>(nxt);
    mm<SUB == 0, 2 * G + 1>(cur);
    if constexpr (RD && 2 * G + 1 < NR) rd<K, KK, 2 * G + 1>(nxt);
    if constexpr (TYPE == 0 && G == 0) mf<SUB == 0>(cs0, ones, cat(cur.wl[0], cur.wh[0]));
    if constexpr (G == (TYPE == 0 ? 2 : 0)) {
      if (__builtin_expect(w == (SUB & 7), 0)) {
#pragma unroll
        for (int a = 0; a < 2 * NU; ++a) mfa(css[a], ones, cur.s[a]);
      }
    }
    hook(IC<G>{});
    SB0();
    if constexpr (G + 1 < NG) group<SUB, G + 1>(cur, nxt, hook);
  }
  template <int SUB, class H> DI void part(const Frags& cur, Frags& nxt, H hook) { group<SUB, 0>(cur, nxt, hook); }
  template <int R> DI void rdall(Frags& f) { rd<0, 0, R>(f); if constexpr (R + 1 < NR) rdall<R + 1>(f); }
};

template <int NU> struct Side {
  unsigned xr[5], pa[NU], pb[NU], pd[NU];
  float v[10], s, e;
  unsigned a_mu, a_cs, v_hg, v_fg, a_st, a_css, a_aw, a_ar, v_pub, v_lk0, v_lk1, v_hs[NU], v_fs, a_tw, a_tr, v_x0, v_x1, a_bs, a_bx, a_p8, a_w0, a_w1, a_k0, a_k1;
  static constexpr int NCH = 16;
  DI void table(const Ctx& c, int u) const {
    if (c.w >= 6) return;
    const int k = threadIdx.x >> 4;
    const float fn = (float)(k < 7 ? 8 * k - 24 : (k < 16 ? 32 * k - 256 : (k < 23 ? 256 * k - 4096 : 1)));
    const float dt = rbf(fexp(bf2f(pd[u])));
    const float ea = dt * bf2f(pa[u]) * 1.44269504088896341f, eb = dt * bf2f(pb[u]) * 0.15915494309189535f;
    const float m = fn < 0.f ? 0.f : __builtin_amdgcn_exp2f(fn * ea);
    const float r = __builtin_amdgcn_fractf(fn * eb);
    ldsw<f32x2>(L_POW(NU == 1 ? 0 : 1, u) + threadIdx.x * 8, f32x2{m * __builtin_amdgcn_cosf(r), m * __builtin_amdgcn_sinf(r)});
  }
  template <int N> DI void tick(Ctx& c, int srow, int unit0) {
    const int l = c.lane, i = l & 15, q = l >> 4, w = c.w, wt = w & 3, wn = w >> 2, grp = wt * 2 + wn;
    if constexpr (N == 0) {
#pragma unroll
      for (int j = 0; j < 5; ++j) { v[2 * j] = bf2f(xr[j]); v[2 * j + 1] = bfhi(xr[j]); PIN(v[2 * j]); PIN(v[2 * j + 1]); }
    } else if constexpr (N == 1) {
      s = ((v[0] + v[1]) + (v[2] + v[3])) + ((v[4] + v[5]) + (v[6] + v[7])) + (v[8] + v[9]); PIN(s);
    } else if constexpr (N == 2) { s = sumr(s); PIN(s); }
    else if constexpr (N == 3) { s = sumq(s) * (1.f / 640.f); PIN(s); }
    else if constexpr (N == 4) {
#pragma unroll
      for (int j = 0; j < 10; ++j) { v[j] -= s; PIN(v[j]); }
    } else if constexpr (N == 5) {
      e = v[0] * v[0];
#pragma unroll
      for (int j = 1; j < 10; ++j) e = fmaf(v[j], v[j], e);
      PIN(e);
    } else if constexpr (N == 6) { e = sumr(e); PIN(e); }
    else if constexpr (N == 7) {
      const float var = rbf(sumq(e) * (1.f / 640.f));
      const float sd = rbf(__builtin_amdgcn_sqrtf(rbf(var + 1e-5f)));
      float rc = __builtin_amdgcn_rcpf(sd);
      rc = fmaf(fmaf(-sd, rc, 1.f), rc, rc);
      st128(c, v4u{__float_as_uint(rbf(s)), c.T, __float_as_uint(rc), c.T2}, WS_STAT + srow * 16);
    } else {
      const int k0 = c.rb - 1 - q;
      const unsigned fbase = WS_FLAGH + (c.rb * 4 + wt) * 2048;
      const unsigned r8 = ((l & 12) << 4) | ((l & 3) << 3);
      if constexpr (N == 8) {
        a_css = (NU == 1 ? L_CSS : L_CS3) + q * 128 + i * 4;
        a_ar = L_AGG + i * 64 + q * 16;
        a_aw = a_ar + grp * 8 - 48;
#pragma unroll
        for (int u = 0; u < NU; ++u) { v_hs[u] = (unit0 + u < 40 ? WS_H + (c.rb * 4 + wt) * H_RT + ustep(unit0 + u) * 4096 + wn * 2048 : OOB) + l * 16; PIN(v_hs[u]); }
        PIN(a_css); PIN(a_ar); PIN(a_aw);
      } else if constexpr (N == 9) {
        const unsigned po = (WS_AGG + unit0 * 256) + i * 16;
        v_pub = q == 3 ? po + c.rb * AGG_RB : OOB;
        v_lk0 = k0 >= 0 ? po + q * AGG_RB : OOB;
        v_lk1 = k0 >= 4 ? po + (q + 4) * AGG_RB : OOB;
        PIN(v_pub); PIN(v_lk0); PIN(v_lk1);
      } else if constexpr (N == 10) {
        const unsigned tb = L_POW(NU == 1 ? 0 : 1, 2) + i * 8;
        a_p8 = tb + q * 128;
        a_w0 = tb + (8 + max(grp - 1 - 2 * q, -1)) * 128;
        a_w1 = tb + (8 + max(grp - 2 - 2 * q, -1)) * 128;
        a_k0 = tb + (16 + max(k0, 0)) * 128;
        a_k1 = tb + (16 + max(k0 - 4, 0)) * 128;
        PIN(a_p8); PIN(a_w0); PIN(a_w1); PIN(a_k0); PIN(a_k1);
      } else if constexpr (N == 11) {
        if (NU == 1) {
          const int step = 2 * c.cg + wn;
          a_mu = (L_ST + wt * 512 + wn * 256) + r8;
          a_cs = (L_CS + wn * 256 + wt * 64) + q * 16;
          v_hg = (WS_H + (c.rb * 4 + wt) * H_RT + step * 4096 + wn * 2048) + l * 16;
          v_fg = (fbase + (step & 7) * 256 + (step >> 3) * 16) + (l & 1) * 8;
          PIN(a_mu); PIN(a_cs); PIN(v_hg); PIN(v_fg);
        }
      } else if constexpr (N == 12) {
        const int step = ustep(NU == 1 ? unit0 : min(unit0 + min(l, 2), 39));
        v_fs = fbase + (step & 7) * 256 + (step >> 3) * 16 + wn * 8;
        a_st = (L_ST + grp * 256) + q * 64;
        a_bs = (L_ST + c.rt * 512 + (wt >> 1) * 256 + (wt & 1) * 32) + r8;
        a_bx = ((wt >> 1) * 5120 + (wt & 1) * 640) + r8 * 20 + q * 8;
        PIN(v_fs); PIN(a_st); PIN(a_bs); PIN(a_bx);
      } else if constexpr (N == 13) {
        const unsigned tr = LWB(2) + 8192 + w * 1024;
        a_tw = tr + i * 64 + ((q ^ (i >> 1)) & 3) * 16;
        a_tr = tr + (l >> 2) * 64 + ((l ^ (l >> 3)) & 3) * 16;
        PIN(a_tw); PIN(a_tr);
      } else if constexpr (N == 14) {
        c.vh2 = (WS_H + (c.rb * 4 + c.rt) * H_RT + w * 4096 + wt * 1024) + l * 16;
        c.vfl = (WS_FLAGH + (c.rb * 4 + c.rt) * 2048 + w * 256) + (l & 31) * 8;
        c.vw2 = (WS_W2T + c.ct * W2T_CT + w * 5120) + l * 16;
        PIN(c.vh2); PIN(c.vfl); PIN(c.vw2);
      } else {
        const unsigned c0 = w * 64 + l, r0 = (c0 * 6554) >> 16, c1 = c0 + 512, r1 = (c1 * 6554) >> 16, xb = (c.rb * 256 + c.rt * 64) * 1280 + c.ct * 160;
        v_x0 = xb + r0 * 1120 + c0 * 16;
        v_x1 = w < 2 ? xb + r1 * 1120 + c1 * 16 : c.ct * 160 + l * 16;
        PIN(v_x0); PIN(v_x1);
      }
    }
  }
};

template <int TYPE> DI void phase1(Ctx& c, const u16* lre, const u16* lim, const u16* ldt) {
  const int l = c.lane, i = l & 15, q = l >> 4, w = c.w, wt = w & 3, wn = w >> 2;
  const int rb = c.rb, cg = c.cg;
  constexpr int NU = G1<TYPE>::NU, NP = G1<TYPE>::NP;
  G1<TYPE> g;
  Side<NU> sd;
  const int srow = rb * 256 + cg * 8 + w;
  const int si = rb * 5 + (cg - 27), mi = rb * 27 + cg;
  const int unit0 = TYPE == 0 ? cg : 27 + 3 * (cg - 27);
  __builtin_amdgcn_s_waitcnt(0x0070);
  unsigned cpf;
  {
    const unsigned n = ((blockIdx.x >> 3) & 7) * 64 + l;
    if (w == 0) asm volatile("global_load_dword %0, %1, %2" : "=v"(cpf) : "v"(min(n * 128u, CODE_PF)), "s"(c.pc) : "memory");
  }
  unsigned ep, qa, qb, qd;
  v4u xq;
  {
    const u16* xrow = c.x + (size_t)srow * D_;
    asm volatile("s_load_dword %2, %6, 0x0\n\ts_nop 4\n\tglobal_load_dwordx4 %0, %3, %5\n\tglobal_load_dword %1, %4, %5 offset:1024"
                 : "=&v"(xq), "=&v"(sd.xr[4]), "=&s"(ep) : "v"(l * 16), "v"(l * 4), "s"(xrow), "s"(c.ws) : "memory");
    if (TYPE == 1)
      asm volatile("s_nop 4\n\tglobal_load_ushort %0, %3, %4\n\tglobal_load_ushort %1, %3, %5\n\tglobal_load_ushort %2, %3, %6"
                   : "=&v"(qa), "=&v"(qb), "=&v"(qd) : "v"(((si >> 2) * 64 + l) * 2), "s"(lre), "s"(lim), "s"(ldt) : "memory");
  }
  auto params = [&]() {
#pragma unroll
    for (int u = 0; u < NU; ++u) {
      const int ua = min(unit0 + u, 39);
      asm volatile("s_nop 4\n\tglobal_load_ushort %0, %3, %4\n\tglobal_load_ushort %1, %3, %5\n\tglobal_load_ushort %2, %3, %6"
                   : "=&v"(sd.pa[u]), "=&v"(sd.pb[u]), "=&v"(sd.pd[u]) : "v"((ua * 16 + i) * 2), "s"(lre), "s"(lim), "s"(ldt) : "memory");
    }
  };
  auto ticks = [&](auto self, auto N, auto E) { if constexpr (N.v < E.v) { sd.template tick<N.v>(c, srow, unit0); self(self, IC<N.v + 1>{}, E); } };
  g.init_stage(c, unit0);
  unsigned wflag = OOB, ja = 0, jb = 0, jc = 0;
  if (TYPE == 0) {
    if (wn == 0) { g.template half0<0>(); g.template half1<0>(); }
    g.init_read(c);
    ja = (l >> 2) * 320 + w * 32 + (l & 3) * 8;
    jb = ja + 5120; jc = ja + 5376;
    ja += LWB(2) + 8192; jb += jb < 8192 ? LWB(2) + 8192 : LWS(2) - 8192; jc += jc < 8192 ? LWB(2) + 8192 : LWS(2) - 8192;
    PIN(ja); PIN(jb); PIN(jc);
    ticks(ticks, IC<8>{}, IC<Side<NU>::NCH>{});
    if (wn != 0) { g.template half0<0>(); g.template half1<0>(); }
    const v4u rj = mkrsrc(c.wd, F_ * D_ * 2);
    const unsigned c0 = w * 64 + l, r0 = (c0 * 3277) >> 16, c1 = c0 + 512, r1 = (c1 * 3277) >> 16;
    glds16<0, LWB(2) + 8192>(rj, r0 * 960 + c0 * 16, (mi >> 2) * 40960 + (mi & 3) * 320, g.w1k);
    if (w < 2) glds16<0, LWS(2)>(rj, r1 * 960 + c1 * 16, (mi >> 2) * 40960 + (mi & 3) * 320, g.w1k);
  } else {
    params();
    auto req = [&]() {
      const v4u rj = mkrsrc(wn ? c.cim : c.cre, 640 * 640 * 2);
      const unsigned r0 = wt * 40 + (l >> 3);
#pragma unroll
      for (int j = 0; j < 5; ++j) {
        const unsigned row = r0 + j * 8;
        glds16<0, LX(2)>(rj, row * 1280 + (((l & 7) ^ ((row >> 1) & 7)) * 16), (si & 3) * 160 * 1280 + (si >> 2) * 128, (w * 5 + j) * 1024);
      }
    };
    if (wn == 0) req();
    g.init_read(c);
    ticks(ticks, IC<8>{}, IC<Side<NU>::NCH>{});
    if (wn != 0) req();
    g.template half0<0>(); g.template half1<0>();
  }
  asm volatile("s_waitcnt vmcnt(%3) lgkmcnt(0)" : "+v"(xq), "+v"(sd.xr[4]), "+s"(ep) : "n"(TYPE == 0 ? 7 : 10) : "memory");
  sd.xr[0] = xq.x; sd.xr[1] = xq.y; sd.xr[2] = xq.z; sd.xr[3] = xq.w;
  asm volatile("" ::"v"(cpf));
  c.T = ep + 1u;
  c.T2 = (c.T * 0x9E3779B1u) ^ 0x5bd1e995u;
  c.tag = v2u{c.T, c.T2};
  PIN(c.tag);
  typename G1<TYPE>::Frags fa, fb;
  unsigned wpd = 0;
  if (TYPE == 0) {
    ticks(ticks, IC<0>{}, IC<4>{});
    __builtin_amdgcn_s_barrier();
    g.template half0<1>();
    ticks(ticks, IC<4>{}, IC<7>{});
    g.template half1<1>();
    if (w >= 5) {
      const unsigned vo = w < 7 ? (l < 56 ? (192 + 56 * rb + l) * 3456 + cg * 128 : OOB) : (l < 28 ? (cg * 16 + (rb & 3) * 4 + (l & 3)) * 1280 + (3 + (l >> 2)) * 128 : OOB);
      const v4u rb2 = mkrsrc((rb & 4) ? c.bim : c.bre, 640 * 640 * 2);
      asm volatile("s_nop 4\n\tbuffer_load_dword %0, %1, %2, 0 offen" : "+v"(wpd) : "v"(vo), "s"(w == 5 ? g.rw0 : (w == 6 ? g.rw1 : rb2)) : "memory");
    }
    ticks(ticks, IC<7>{}, IC<8>{});
    waitv<7>();
    BAR();
    g.template rdall<0>(fa);
  } else {
    asm volatile("" : "+v"(qa), "+v"(qb), "+v"(qd));
    if (w == 7) {
      const float ar = bf2f(qa), ai = bf2f(qb), dt = rbf(fexp(bf2f(qd))), e = fexp(dt * ar);
      float sn, cs;
      fsincos(dt * ai, sn, cs);
      const float inv = 2.f / (ar * ar + ai * ai);
      ldsw<f32x2>(L_COEF + l * 8, cmul(cpx{e * cs - 1.f, e * sn}, cpx{ar * inv, -ai * inv}));
    }
#pragma unroll
    for (int u = 0; u < NU; ++u) { asm volatile("" : "+v"(sd.pa[u]), "+v"(sd.pb[u]), "+v"(sd.pd[u])); sd.table(c, u); }
    g.template half0<1>(); g.template half1<1>();
    waitv<10>();
    BAR();
    const f32x4 k0 = ldsr<f32x4>(L_COEF + (16 * wt + 4 * q) * 8), k1 = ldsr<f32x4>(L_COEF + (16 * wt + 4 * q) * 8 + 16);
    const cpx kc[4] = {{k0.x, k0.y}, {k0.z, k0.w}, {k1.x, k1.y}, {k1.z, k1.w}};
    const int step = ustep(4 * (si >> 2) + wt), ct = 2 * (si & 3) + wn;
#pragma unroll
    for (int j = 0; j < 5; ++j) {
      const unsigned row = (wn * 5 + j) * 16 + i;
      const unsigned a = LX(2) + row * 128 + (((2 * wt + (q >> 1)) ^ ((row >> 1) & 7)) * 16) + (q & 1) * 8;
      const v2u cr = ldsr<v2u>(a), ci = ldsr<v2u>(a + 20480);
      const unsigned cr0 = cr.x, cr1 = cr.y, ci0 = ci.x, ci1 = ci.y;
      const cpx p0 = cmul(kc[0], cpx{bf2f(cr0), bf2f(ci0)}), p1 = cmul(kc[1], cpx{bfhi(cr0), bfhi(ci0)});
      const cpx p2 = cmul(kc[2], cpx{bf2f(cr1), bf2f(ci1)}), p3 = cmul(kc[3], cpx{bfhi(cr1), bfhi(ci1)});
      const v4u o = {pk2(p0.x, p1.x), pk2(p2.x, p3.x), pk2(-p0.y, -p1.y), pk2(-p2.y, -p3.y)};
      st128(c, o, l * 16 + j * 1024 + (WS_W2T + ct * W2T_CT + step * 5120));
    }
    wflag = WS_FLAGW + (ct * 8 + (step & 7)) * 128 + (step >> 3) * 8;
    SB0();
    waitv<10>();
    BAR();
    g.template rdall<0>(fa);
  }
  SB0();
  v4u sg; v2u fw;
  bf16x8 jv0, jv1;
  const v4u rwsv = mkrsrc(c.ws, WS_END);
  auto w2l = [&](auto CH, auto A) { glds16sc1<LP(CH.v) * 40960 + A.v * 1024, tile_base(CH.v, A.v)>(rwsv, c.vw2, A.v < 4 ? g.w4k : g.w1k); };
  auto side = [&](auto SUB, auto G) {
    if constexpr (TYPE == 1 && G.v >= 4 && SUB.v * 2 + G.v - 4 < 8) sd.template tick<SUB.v * 2 + G.v - 4>(c, srow, unit0);
    if constexpr (TYPE == 0 && SUB.v == 17 && G.v == 8) sd.table(c, 0);
    if constexpr (SUB.v == 19) {
      constexpr int a = TYPE == 0 ? (G.v == 0 ? 0 : (G.v == 2 ? 1 : (G.v == 3 ? 2 : (G.v == 5 ? 3 : (G.v == 7 ? 4 : -1))))) : G.v - 1;
      if constexpr (a >= 0) c.wf[0][a] = a < 4 ? ld128(c, c.vw2 + a * 1024) : ld128(c, c.vw2, 4096);
    }
  };
  constexpr int GA = TYPE == 0 ? 4 : 2, GB = TYPE == 0 ? 6 : 4;
  auto step = [&](auto SS) {
    constexpr int S = SS.v;
    LGKM0();
    g.template part<2 * S>(fa, fb, [&](auto G) {
      if constexpr (S == 0) {
        if constexpr (TYPE == 0) {
          if (G.v == 7) { jv0 = cat(trread(ja), trread(jb)); jv1 = cat(trread(ja + 256), trread(jc)); }
        }
        if (G.v == 0) g.template piece<0, 2>();
        if (G.v == 1) g.template piece<4, 2>();
        if (G.v == 2) g.template piece<1, 2>();
        if (G.v == 3) g.template piece<2, 2>();
        if (G.v == 4) g.template piece<3, 2>();
      } else if constexpr (S + 2 < 10) {
        if (G.v == 1) g.template piece<2, S + 2>();
        if (G.v == GA) g.template piece<5, S + 2>();
        if (G.v == GB) g.template piece<3, S + 2>();
      } else {
        if (G.v == 1) w2l(IC<S - 8>{}, IC<3>{});
        if (G.v == GB) w2l(IC<S - 8>{}, IC<4>{});
      }
      side(IC<2 * S>{}, G);
    });
    if constexpr (S < 9) {
      if (S == 7) asm volatile("s_waitcnt vmcnt(%2)" : "+v"(sg), "+v"(fw) : "n"(NP) : "memory");
      else if (S == 8) waitv<5>();
      else if (S == 0) waitv<TYPE == 0 ? 5 : 10>();
      else if (S == 3 && TYPE == 0) waitv<NP + 1>();
      else waitv<NP>();
      LGKM0();
      asm volatile("s_barrier" ::: "memory");
    } else LGKM0();
    if (S == 0) { asm volatile("" ::"v"(wpd)); g.template piece<6, 2>(); g.template piece<5, 2>(); }
    if (S == 2 && TYPE == 0) {
      const unsigned jo = WS_W2T + (mi & 3) * 2 * W2T_CT + (mi >> 2) * 5120;
      st128(c, __builtin_bit_cast(v4u, jv0), (jo + (w < 5 ? w * 1024 : W2T_CT + (w - 5) * 1024)) + l * 16);
      if (w < 2) st128(c, __builtin_bit_cast(v4u, jv1), (jo + W2T_CT + (3 + w) * 1024) + l * 16);
    }
    if (S == 4 && TYPE == 0) { if (w == 0) st_flag(c, (WS_FLAGW + ((mi & 3) * 16 + ((mi >> 2) & 7)) * 128 + (mi >> 5) * 8) + (l & 1) * 1024); }
    if (S == 1 && TYPE == 1) st_flag(c, wflag);
    if (S == 6) {
      if (TYPE == 0) params();
      const GAS char* sp = (const GAS char*)c.ws + WS_STAT + rb * 4096;
      const GAS char* fp = (const GAS char*)c.ws + WS_FLAGW + (c.ct * 8 + w) * 128;
      asm volatile("s_nop 4\n\tglobal_load_dwordx4 %0, %2, %3 sc1\n\tglobal_load_dwordx2 %1, %4, %5 sc1"
                   : "=&v"(sg), "=&v"(fw) : "v"(wt * 1024 + l * 16), "s"(sp), "v"(i * 8), "s"(fp) : "memory");
    }
    if (S == 7) {
      if (TYPE == 0) asm volatile("" : "+v"(sd.pa[0]), "+v"(sd.pb[0]), "+v"(sd.pd[0]));
      const bool need = i < 12 && w + 8 * i < NS2;
      SPIN_LOOP {
        if (__all((!need || (fw.x == c.T && fw.y == c.T2)) && sg.y == c.T && sg.w == c.T2)) break;
        __builtin_amdgcn_s_sleep(1);
        asm volatile("" ::: "memory");
        sg = ld128(c, WS_STAT + rb * 4096 + wt * 1024 + l * 16);
        fw = ld64(c, WS_FLAGW + (c.ct * 8 + w) * 128 + i * 8);
      }
      ldsw<v2u>(L_ST + (wt * 64 + l) * 8, v2u{sg.x, sg.z});
      glds16<0, L_XT0>(g.rx, sd.v_x0, 0, g.w1k);
      if (w < 3) glds16<0, L_XT1>(w == 2 ? mkrsrc(c.dsk, 1280) : g.rx, sd.v_x1, 0, g.w1k);
    }
    SB0();
    g.template part<2 * S + 1>(fb, fa, [&](auto G) {
      if constexpr (S + 3 < 10) {
        if (G.v == 0) g.template piece<6, S + 3>();
        if (G.v == (TYPE == 0 ? 2 : 1)) g.template piece<0, S + 3>();
        if (G.v == GA) g.template piece<4, S + 3>();
        if (G.v == GB) g.template piece<1, S + 3>();
      } else if constexpr (S < 9) {
        if (G.v == 1) w2l(IC<S - 7>{}, IC<0>{});
        if (G.v == GA) w2l(IC<S - 7>{}, IC<1>{});
        if (G.v == GB) w2l(IC<S - 7>{}, IC<2>{});
      } else {
        if (G.v == G1<TYPE>::NG - 1) {
          if (TYPE == 0) ldsw<float>(L_CS + ((wn * 4 + wt) * 16 + i) * 4, g.cs0.x);
          const unsigned ca = (TYPE == 0 ? L_CSS : L_CS3) + w * 128 + i * 4;
#pragma unroll
          for (int a = 0; a < 2 * NU; ++a) ldsw<float>(ca + (a >> 1) * 1024 + (a & 1) * 64, g.css[a].x);
        }
      }
      side(IC<2 * S + 1>{}, G);
    });
  };
  step(IC<0>{}); step(IC<1>{}); step(IC<2>{}); step(IC<3>{}); step(IC<4>{});
  step(IC<5>{}); step(IC<6>{}); step(IC<7>{}); step(IC<8>{}); step(IC<9>{});
  MFMA_DRAIN();
  BAR();
  SB0();
  auto w2c2 = [&]() { w2l(IC<2>{}, IC<0>{}); w2l(IC<2>{}, IC<1>{}); w2l(IC<2>{}, IC<2>{}); w2l(IC<2>{}, IC<3>{}); w2l(IC<2>{}, IC<4>{}); };
  __builtin_amdgcn_s_setprio(3);
  if (TYPE == 0) {
    f32x4 cs[4];
    f32x2 mu[4];
#pragma unroll
    for (int a = 0; a < 4; ++a) cs[a] = ldsr<f32x4>(sd.a_cs ^ (a * 64));
#pragma unroll
    for (int m = 0; m < 4; ++m) mu[m] = ldsr<f32x2>((m < 2 ? sd.a_mu : (sd.a_mu ^ 256)) + (m & 1) * 32);
    auto glu = [&](auto ODD) {
#pragma unroll
      for (int m = 0; m < 4; ++m) {
        const f32x4 mv = {-mu[m].x, -mu[m].x, -mu[m].x, -mu[m].x};
        const float r2 = mu[m].y * mu[m].y;
        const f32x4 h0 = __builtin_elementwise_fma(mv, cs[ODD.v], g.acc[ODD.v][m]) * __builtin_elementwise_fma(mv, cs[2 + ODD.v], g.acc[2 + ODD.v][m]) * r2;
        const f32x4 h1 = __builtin_elementwise_fma(mv, cs[1 - ODD.v], g.acc[1 - ODD.v][m]) * __builtin_elementwise_fma(mv, cs[3 - ODD.v], g.acc[3 - ODD.v][m]) * r2;
        st128(c, v4u{pk2(h0[0], h0[1]), pk2(h0[2], h0[3]), pk2(h1[0], h1[1]), pk2(h1[2], h1[3])}, (m < 2 ? sd.v_hg : (sd.v_hg ^ 2048)) + (m & 1) * 1024);
        asm volatile("" ::: "memory");
      }
    };
    if (w & 1) glu(IC<1>{}); else glu(IC<0>{});
    SB0();
  } else {
    if (si == 39) {
      SPIN_LOOP {
        bool ok = true;
        asm volatile("" ::: "memory");
#pragma unroll
        for (int k = 0; k < 4; ++k) {
          const v4u t = ld128(c, WS_STAT + (k * 512 + w * 64 + l) * 16);
          ok = ok && t.y == c.T && t.w == c.T2;
        }
        if (__all(ok)) break;
        __builtin_amdgcn_s_sleep(1);
      }
    }
    w2c2(); ldw<1>(c, c.wf[1]);
    SB0();
  }
  PRI(2);
  cpx v[NU][8], eq[NU], tot[NU], lam[NU], L32[NU], f1[NU], W0[NU], W1[NU], wk0[NU], wk1[NU];
  {
    f32x4 st[4];
#pragma unroll
    for (int e = 0; e < 4; ++e) st[e] = ldsr<f32x4>(sd.a_st + e * 16);
    const unsigned a_pi = sd.a_p8 & ~384u;
#pragma unroll
    for (int u = 0; u < NU; ++u) {
      const unsigned to = L_POW(TYPE, u) - L_POW(TYPE, 2);
      lam[u] = ldsr<cpx>(a_pi + to + 23 * 128);
      const cpx L8 = ldsr<cpx>(a_pi + to + 4 * 128), u2 = ldsr<cpx>(sd.a_p8 + to), u1 = ldsr<cpx>(sd.a_p8 + to + 128), u0 = ldsr<cpx>(sd.a_p8 + to + 256);
      f1[u] = ldsr<cpx>(sd.a_p8 + to + 384); L32[u] = ldsr<cpx>(a_pi + to + 9 * 128);
      W0[u] = ldsr<cpx>(sd.a_w0 + to); W1[u] = ldsr<cpx>(sd.a_w1 + to);
      const cpx lg = ldsr<cpx>(a_pi + (to + 8 * 128) + (wt * 2 + wn) * 128);
      wk0[u] = ldsr<cpx>(sd.a_k0 + to); wk1[u] = ldsr<cpx>(sd.a_k1 + to);
      const unsigned ac = sd.a_css + u * 1024;
      const float csx = sumq(ldsr<float>(ac) + ldsr<float>(ac + 512)), csy = sumq(ldsr<float>(ac + 64) + ldsr<float>(ac + 576));
#pragma unroll
      for (int j = 0; j < 8; ++j) {
        const float mu = (j & 1) ? st[j >> 1].z : st[j >> 1].x, rs = (j & 1) ? st[j >> 1].w : st[j >> 1].y;
        float a = fmaf(-mu, csx, g.accs[2 * u][j >> 2][j & 3]), b = fmaf(-mu, csy, g.accs[2 * u + 1][j >> 2][j & 3]);
        asm("" : "+v"(a), "+v"(b));
        v[u][j] = cpx{a * rs, b * rs};
        asm("" : "+v"(v[u][j]));
      }
      cpx h = v[u][0];
#pragma unroll
      for (int j = 1; j < 8; ++j) h = cfma(lam[u], h, v[u][j]);
      float ax[4], ay[4];
      gatherq(h.x, ax); gatherq(h.y, ay);
      eq[u] = cfma(u0, cpx{ax[0], ay[0]}, cfma(u1, cpx{ax[1], ay[1]}, cpx{u2.x * ax[2], u2.x * ay[2]}));
      tot[u] = cfma(L8, eq[u], h);
      if (l >= 48) ldsw<f32x2>(sd.a_aw + u * 1024, tot[u]);
      wk0[u] = cmul(wk0[u], lg); wk1[u] = cmul(wk1[u], lg);
    }
  }
  PRI(3);
  BAR();
  cpx pm[NU];
#pragma unroll
  for (int u = 0; u < NU; ++u) {
    const f32x4 t = ldsr<f32x4>(sd.a_ar + u * 1024);
    pm[u] = cfma(W0[u], cpx{t.x, t.y}, cmul(W1[u], cpx{t.z, t.w}));
  }
  if (w == 7) {
#pragma unroll
    for (int u = 0; u < NU; ++u) {
      const cpx P = cfma(L32[u], cpx{sumq(pm[u].x), sumq(pm[u].y)}, tot[u]);
      st128(c, v4u{__float_as_uint(P.x), c.T, __float_as_uint(P.y), c.T2}, sd.v_pub + u * 256);
    }
  }
  SB0();
  if (TYPE == 0) {
    if (w == 7) waitv<1>(); else waitv<0>();
    st_flag(c, sd.v_fg);
  } else {
    if (si == 39 && w == 0) __builtin_amdgcn_raw_buffer_store_b32(c.T, c.rws, WS_EPOCH, 0, 16);
  }
  asm volatile("s_barrier" ::: "memory");
  PRI(2);
  if (TYPE == 0) { w2c2(); ldw<1>(c, c.wf[1]); ldw<2>(c, c.wf[2]); ldw<3>(c, c.wf[3]); }
  PRI(3);
  {
    const f32x2 st = ldsr<f32x2>(sd.a_bs);
    const float nm = -st.x * st.y;
    auto item = [&](int a) {
      const unsigned xo = sd.a_bx + a * 32;
      const v2u xv = ldsr<v2u>(xo < 8192 ? L_XT0 + xo : L_XT1 - 8192 + xo), dv = ldsr<v2u>(L_XT1 + 2048 + q * 8 + a * 32);
      const unsigned x0 = xv.x, x1 = xv.y, d0 = dv.x, d1 = dv.y;
      const float xf[4] = {bf2f(x0), bfhi(x0), bf2f(x1), bfhi(x1)}, df[4] = {bf2f(d0), bfhi(d0), bf2f(d1), bfhi(d1)};
      f32x4 r;
#pragma unroll
      for (int e = 0; e < 4; ++e) {
        float xn = fmaf(xf[e], st.y, nm);
        asm("" : "+v"(xn));
        r[e] = fmaf(xn, df[e], xf[e]);
      }
      return r;
    };
    const f32x4 z = {0.f, 0.f, 0.f, 0.f};
    if (wn == 0) { c.acc0[0] = item(0); c.acc0[1] = item(1); c.acc0[2] = item(2); c.acc0[3] = z; c.acc0[4] = z; }
    else { c.acc0[0] = z; c.acc0[1] = z; c.acc0[2] = z; c.acc0[3] = item(3); c.acc0[4] = item(4); }
#pragma unroll
    for (int a = 0; a < 5; ++a) PIN(c.acc0[a]);
    SB0();
  }
  v4u lk0[NU], lk1[NU];
#pragma unroll
  for (int u = 0; u < NU; ++u) { lk0[u] = ld128(c, sd.v_lk0 + u * 256); lk1[u] = ld128(c, sd.v_lk1 + u * 256); }
  if (TYPE == 0) c.fl = ld64(c, c.vfl);
  else { PRI(1); ldw<2>(c, c.wf[2]); ldw<3>(c, c.wf[3]); PRI(3); }
  auto hwait = [&]() {
    if ((fresh(c, c.fl) & 0xfffull) != 0xfffull) {
      SPIN_LOOP {
        asm volatile("" ::: "memory");
        c.fl = ld64(c, c.vfl);
        if ((fresh(c, c.fl) & 0xfffull) == 0xfffull) break;
      }
    }
    __builtin_amdgcn_fence(__ATOMIC_ACQUIRE, "wavefront");
    PRI(1);
    ldh<0>(c, c.hf[0]); ldh<1>(c, c.hf[1]); ldh<2>(c, c.hf[2]);
    PRI(3);
  };
  if (TYPE == 0) hwait();
  auto lkok = [&]() {
    bool ok = true;
#pragma unroll
    for (int u = 0; u < NU; ++u)
      ok = ok && (sd.v_lk0 >= OOB || (lk0[u].y == c.T && lk0[u].w == c.T2)) && (sd.v_lk1 >= OOB || (lk1[u].y == c.T && lk1[u].w == c.T2));
    return __all(ok);
  };
  if (!lkok()) {
    SPIN_LOOP {
      asm volatile("" ::: "memory");
#pragma unroll
      for (int u = 0; u < NU; ++u) { lk0[u] = ld128(c, sd.v_lk0 + u * 256); lk1[u] = ld128(c, sd.v_lk1 + u * 256); }
      if (lkok()) break;
    }
  }
  if (TYPE == 1) c.fl = ld64(c, c.vfl);
  SB0();
#pragma unroll
  for (int u = 0; u < NU; ++u) {
    const unsigned x0 = lk0[u].x, y0 = lk0[u].z, x1 = lk1[u].x, y1 = lk1[u].z;
    const cpx cy = cfma(wk1[u], cpx{__uint_as_float(x1), __uint_as_float(y1)}, cfma(wk0[u], cpx{__uint_as_float(x0), __uint_as_float(y0)}, pm[u]));
    cpx h = cfma(f1[u], cpx{sumq(cy.x), sumq(cy.y)}, eq[u]);
#pragma unroll
    for (int j = 0; j < 8; ++j) { h = cfma(lam[u], h, v[u][j]); v[u][j] = h; }
    ldsw<v4u>(sd.a_tw, v4u{pk2(v[u][0].x, v[u][1].x), pk2(v[u][2].x, v[u][3].x), pk2(v[u][4].x, v[u][5].x), pk2(v[u][6].x, v[u][7].x)});
    ldsw<v4u>(sd.a_tw + 16384, v4u{pk2(v[u][0].y, v[u][1].y), pk2(v[u][2].y, v[u][3].y), pk2(v[u][4].y, v[u][5].y), pk2(v[u][6].y, v[u][7].y)});
    asm volatile("s_waitcnt lgkmcnt(0)" ::: "memory");
#pragma unroll
    for (int mm = 0; mm < 2; ++mm) st128(c, __builtin_bit_cast(v4u, cat(trread(sd.a_tr + mm * 8), trread(sd.a_tr + mm * 8 + 16384))), sd.v_hs[u] + mm * 1024);
    if (u + 1 < NU) asm volatile("s_waitcnt lgkmcnt(0)" ::: "memory");
  }
  SB0();
  c.vfs = sd.v_fs;
  if (TYPE == 1) {
    if ((fresh(c, c.fl) & 0xfffull) != 0xfffull) {
      waitv<0>();
      st_flag(c, sd.v_fs);
      c.vfs = OOB;
    }
    hwait();
  }
  SB0();
}

DI void phase2(Ctx& c, float* out) {
  const int w = c.w, rt = c.rt, ct = c.ct;
  v4u wf[12][5], hf[12][4];
  f32x4 acc[4][5];
  v2u fl = c.fl;
#pragma unroll
  for (int a = 0; a < 5; ++a) acc[0][a] = c.acc0[a];
  auto body = [&](auto JJ) {
    constexpr int j = JJ.v;
    if constexpr (lds_chunk(j + 1) >= 0) {
      constexpr int ch = lds_chunk(j + 1);
      int l = threadIdx.x & 63; PIN(l);
      const unsigned a4 = tile_base(ch, 0) + w * 4096 + l * 16;
#pragma unroll
      for (int a = 0; a < 4; ++a) wf[j + 1][a] = ldsr<v4u>(a4 + a * 1024);
      wf[j + 1][4] = ldsr<v4u>(tile_base(ch, 4) + w * 1024 + l * 16);
    }
    if constexpr (j < 4) {
#pragma unroll
      for (int a = 0; a < 5; ++a) wf[j][a] = c.wf[j][a];
    }
    if constexpr (j < 3) {
#pragma unroll
      for (int m = 0; m < 4; ++m) hf[j][m] = c.hf[j][m];
    }
    if constexpr (j + 3 < 12 && j + 3 >= 4 && lds_chunk(j + 3) < 0) { ldw<j + 3>(c, wf[j + 3]); SB0(); }
    if constexpr (j == 4 || j == 6) {
      const unsigned long long msk = j == 4 ? (15ull << 12) : ((w >= 6 ? (1ull << 22) : (1ull << 24)) - (1ull << 16));
      if (__builtin_expect((fresh(c, fl) & msk) != msk, 0)) {
        SPIN_LOOP {
          asm volatile("" ::: "memory");
          const v2u f = ld64(c, c.vfl);
          if ((fresh(c, f) & msk) == msk) break;
        }
      }
      __builtin_amdgcn_fence(__ATOMIC_ACQUIRE, "wavefront");
    }
    if constexpr (j == 3 || j == 5) fl = ld64(c, c.vfl);
    if constexpr (j + 2 < 12 && j + 2 >= 3) ldh<j + 2>(c, hf[j + 2]);
    SB0();
#pragma unroll
    for (int a = 0; a < 5; ++a)
#pragma unroll
      for (int m = 0; m < 4; ++m) {
        if (j == 0 && m > 0) mfz(acc[m][a], __builtin_bit_cast(bf16x8, wf[j][a]), __builtin_bit_cast(bf16x8, hf[j][m]));
        else mfa(acc[m][a], __builtin_bit_cast(bf16x8, wf[j][a]), __builtin_bit_cast(bf16x8, hf[j][m]));
      }
    if constexpr (j == 0) {
      if (c.cg < 27) waitv<0>(); else waitv<8>();
      st_flag(c, c.vfs);
    }
    if constexpr (j == 2 || j == 5 || j == 8) __builtin_amdgcn_s_setprio(j == 2 ? 2 : (j == 5 ? 1 : 0));
    if constexpr (j == 1) BAR();
    SB0();
  };
  body(IC<0>{}); body(IC<1>{}); body(IC<2>{}); body(IC<3>{}); body(IC<4>{}); body(IC<5>{});
  body(IC<6>{}); body(IC<7>{}); body(IC<8>{}); body(IC<9>{}); body(IC<10>{});
  int l = threadIdx.x & 63; PIN(l);
  unsigned b4 = w * 4096 + l * 16, b4h = b4 + 65536, b1 = 98304 + w * 1024 + l * 16, b2 = 147456 + w * 2048 + l * 16;
  PIN(b4); PIN(b4h); PIN(b1); PIN(b2);
  const int tl = ((w >> 1) & 1) * 32 + 8 * ((l & 15) >> 2) + 4 * (w & 1) + (l & 3);
  GAS float* op = (GAS float*)out + (size_t)(c.rb * 256 + rt * 64 + tl) * D_ + ct * 80 + (w >> 2) * 16 + 4 * (l >> 4);
  PIN(op);
  unsigned r4[4], r1[4], r2;
#pragma unroll
  for (int x = 0; x < 4; ++x) { r4[x] = ((w & 3) ^ x) * 4096 + l * 16 + (x == 1 ? 65536 : 0); r1[x] = 98304 + ((w & 3) ^ x) * 1024 + l * 16; PIN(r4[x]); PIN(r1[x]); }
  r2 = 147456 + ((w & 3) ^ 3) * 2048 + l * 16; PIN(r2);
  auto redw = [&](int t) {
    const int a = t >> 2, m = t & 3;
    const unsigned tb = tile_base(m, a), ts = tile_wstride(m, a);
    ldsw<f32x4>(ts == 4096 ? (tb >= 65536 ? b4h + (tb - 65536) : b4 + tb) : (ts == 1024 ? b1 + (tb - 98304) : b2 + (tb - 147456)), acc[m][a]);
  };
  if (w < 6) {
#pragma unroll
    for (int t = 0; t < 20; ++t) {
      mfa(acc[t & 3][t >> 2], __builtin_bit_cast(bf16x8, wf[11][t >> 2]), __builtin_bit_cast(bf16x8, hf[11][t & 3]));
      if (t >= 8) redw(t - 8);
      SB0();
    }
    MFMA_DRAIN();
    SB0();
#pragma unroll
    for (int t = 12; t < 20; ++t) redw(t);
  } else {
    MFMA_DRAIN();
    SB0();
#pragma unroll
    for (int t = 0; t < 20; ++t) redw(t);
  }
  BAR();
  auto sum = [&](auto AA) {
    constexpr int a = AA.v;
    f32x4 p[8];
#pragma unroll
    for (int x = 0; x < 4; ++x)
#pragma unroll
      for (int hh = 0; hh < 2; ++hh) {
        const unsigned tb = tile_base(x, a), ts = tile_wstride(x, a);
        p[x * 2 + hh] = ldsr<f32x4>(ts == 4096 ? r4[x] + (tb - (x == 1 ? 65536 : 0)) + hh * 16384 : (ts == 1024 ? r1[x] + (tb - 98304) + hh * 4096 : r2 + (tb - 147456) + hh * 8192));
      }
    return ((p[0] + p[1]) + (p[2] + p[3])) + ((p[4] + p[5]) + (p[6] + p[7]));
  };
  if (w < 4) {
    const f32x4 s0 = sum(IC<0>{}), s1 = sum(IC<2>{}), s2 = sum(IC<4>{});
    *(GAS f32x4*)op = s0; *(GAS f32x4*)(op + 32) = s1; *(GAS f32x4*)(op + 64) = s2;
  } else {
    const f32x4 s0 = sum(IC<1>{}), s1 = sum(IC<3>{});
    *(GAS f32x4*)op = s0; *(GAS f32x4*)(op + 32) = s1;
  }
}

__global__ void __launch_bounds__(512, 2)
fused_kernel(const u16* x, const u16* lre, const u16* lim, const u16* bre, const u16* bim, const u16* cre, const u16* cim, const u16* ldt,
             const u16* dsk, const u16* wg, const u16* wu, const u16* wd, float* out, char* ws) {
  Ctx c;
  asm volatile("s_getpc_b64 %0" : "=s"(c.pc));
  c.dsk = dsk; c.x = x; c.bre = bre; c.bim = bim; c.cre = cre; c.cim = cim; c.wg = wg; c.wu = wu; c.wd = wd; c.ws = ws;
  c.lane = threadIdx.x & 63;
  c.w = __builtin_amdgcn_readfirstlane(threadIdx.x >> 6);
  const int xc = blockIdx.x & 7, ix = blockIdx.x >> 3;
  c.rb = (xc >> 1) * 2 + (ix >> 4); c.cg = (xc & 1) * 16 + (ix & 15);
  c.rt = c.cg >> 3; c.ct = c.cg & 7;
  c.rws = __builtin_amdgcn_make_buffer_rsrc(ws, 0, WS_END, 0x00020000);
  if (__builtin_expect(c.cg < 27, 1)) phase1<0>(c, lre, lim, ldt);
  else phase1<1>(c, lre, lim, ldt);
  phase2(c, out);
}

extern "C" void kernel_launch(void* const* d_in, const int* in_sizes, int n_in, void* d_out, int out_size, void* d_ws, size_t ws_size,
                              hipStream_t stream) {
  (void)in_sizes; (void)n_in; (void)out_size; (void)ws_size;
  (void)hipFuncSetAttribute((const void*)fused_kernel, hipFuncAttributeMaxDynamicSharedMemorySize, LDS_BYTES);
  fused_kernel<<<256, 512, LDS_BYTES, stream>>>((const u16*)d_in[0], (const u16*)d_in[1], (const u16*)d_in[2], (const u16*)d_in[3], (const u16*)d_in[4],
                                                (const u16*)d_in[5], (const u16*)d_in[6], (const u16*)d_in[7], (const u16*)d_in[8], (const u16*)d_in[10],
                                                (const u16*)d_in[11], (const u16*)d_in[12], (float*)d_out, (char*)d_ws);
}
```

```cpp
#include <hip/hip_runtime.h>
#include <stdint.h>

#define GAS __attribute__((address_space(1)))
#define LAS __attribute__((address_space(3)))
#define DI __device__ __forceinline__
typedef unsigned short u16;
typedef short bf16x8 __attribute__((ext_vector_type(8)));
typedef short s16x4 __attribute__((ext_vector_type(4)));
typedef __bf16 bf2 __attribute__((ext_vector_type(2)));
typedef float f32x4 __attribute__((ext_vector_type(4)));
typedef float f32x2 __attribute__((ext_vector_type(2)));
typedef unsigned v4u __attribute__((ext_vector_type(4)));
typedef unsigned v2u __attribute__((ext_vector_type(2)));

constexpr int L_ = 2048, D_ = 640, F_ = 1728;
constexpr int NS2 = 94;
constexpr int ustep(int u) { return u >= 27 ? 27 + u : 67 + u; }
constexpr unsigned WS_EPOCH = 0;
constexpr unsigned WS_STAT = 4096;
constexpr unsigned WS_AGG = WS_STAT + 2048 * 16;
constexpr unsigned AGG_RB = 42 * 256;
constexpr unsigned WS_FLAGH = WS_AGG + 8 * AGG_RB;
constexpr unsigned WS_FLAGW = WS_FLAGH + 32 * 8 * 256;
constexpr unsigned WS_CTL_END = WS_FLAGW + 8 * 8 * 128;
constexpr unsigned WS_W2T = 262144;
constexpr unsigned W2T_CT = NS2 * 5 * 1024;
constexpr unsigned WS_H = WS_W2T + 8 * W2T_CT;
constexpr unsigned H_RT = NS2 * 4 * 1024;
constexpr unsigned WS_END = WS_H + 32 * H_RT;
constexpr unsigned OOB = 0x70000000u;
static_assert(WS_CTL_END <= WS_W2T && WS_H % 4096 == 0, "ws map");
constexpr unsigned LX(int k) { return k * 32768; }
constexpr unsigned LWB(int k) { return 98304 + ((k + 1) % 3) * 16384; }
constexpr unsigned LWS(int k) { return 147456 + k * 4096; }
constexpr unsigned L_ST = 159744;
constexpr unsigned L_CS = L_ST + 2048;
constexpr unsigned L_CSS = L_CS + 512;
constexpr unsigned L_CS3 = LWB(2) + 8192;
constexpr unsigned L_COEF = L_CSS + 1024;
constexpr unsigned L_POW(int type, int u) { return type == 0 ? LWB(2) + 8192 : (u == 1 ? LWS(0) : LWB(u) + 12288); }
constexpr unsigned L_AGG = LWS(2);
constexpr unsigned L_XT0 = LWB(1) + 8192;
constexpr unsigned L_XT1 = LWS(1);
constexpr unsigned LDS_BYTES = 163840;
static_assert(L_COEF + 512 <= LDS_BYTES && LWB(2) == LX(2) + 32768, "lds map");
constexpr unsigned tile_base(int ch, int j) {
  return ch < 3 ? (j < 4 ? LX((ch + 1) % 3) + j * 1024 : LWB((ch + 1) % 3)) : (j < 3 ? LWB(j) + 8192 : LWS(0) + (j - 3) * 1024);
}
constexpr unsigned tile_wstride(int ch, int j) { return ch < 3 ? (j < 4 ? 4096 : 1024) : (j < 3 ? 1024 : 2048); }
constexpr int SPIN_CAP = 1 << 16;
constexpr unsigned CODE_PF = 48000;

DI float bf2f(unsigned v) { return __uint_as_float(v << 16); }
DI float bfhi(unsigned v) { return __uint_as_float(v & 0xffff0000u); }
DI unsigned pk2(float a, float b) { bf2 v = {(__bf16)a, (__bf16)b}; return __builtin_bit_cast(unsigned, v); }
DI float rbf(float a) { return bf2f(pk2(a, 0.f) & 0xffffu); }
DI void mfa(f32x4& c, bf16x8 a, bf16x8 b) { asm volatile("v_mfma_f32_16x16x32_bf16 %0, %1, %2, %0" : "+v"(c) : "v"(a), "v"(b)); }
DI void mfz(f32x4& c, bf16x8 a, bf16x8 b) { asm volatile("v_mfma_f32_16x16x32_bf16 %0, %1, %2, 0" : "=&v"(c) : "v"(a), "v"(b)); }
template <bool Z> DI void mf(f32x4& c, bf16x8 a, bf16x8 b) { if (Z) mfz(c, a, b); else mfa(c, a, b); }
#define MFMA_DRAIN() asm volatile("s_nop 7\n\ts_nop 1" ::: "memory")
DI v4u mkrsrc(const void* p, unsigned bytes) {
  uint64_t a = (uint64_t)p;
  v4u r; r.x = (unsigned)a; r.y = (unsigned)(a >> 32) & 0xffffu; r.z = bytes; r.w = 0x00020000u;
  return r;
}
template <unsigned SI, unsigned LI> DI void glds16(v4u rs, unsigned voff, unsigned sb, unsigned lb) {
  unsigned t;
  asm volatile("s_add_u32 m0, %4, %6\n\ts_add_u32 %0, %3, %5\n\tbuffer_load_dwordx4 %1, %2, %0 offen lds"
               : "=&s"(t) : "v"(voff), "s"(rs), "s"(sb), "s"(lb), "i"(SI), "i"(LI) : "memory", "scc");
}
template <unsigned SI, unsigned LI> DI void glds16sc1(v4u rs, unsigned voff, unsigned lb) {
  unsigned t;
  asm volatile("s_add_u32 m0, %3, %5\n\ts_mov_b32 %0, %4\n\tbuffer_load_dwordx4 %1, %2, %0 offen sc1 lds"
               : "=&s"(t) : "v"(voff), "s"(rs), "s"(lb), "i"(SI), "i"(LI) : "memory", "scc");
}
template <class T> DI T ldsr(unsigned a) { return *(LAS T*)(uintptr_t)a; }
template <class T> DI void ldsw(unsigned a, T v) { *(LAS T*)(uintptr_t)a = v; }
DI s16x4 trread(unsigned a) { return __builtin_amdgcn_ds_read_tr16_b64_v4i16((LAS s16x4*)(uintptr_t)a); }
DI bf16x8 cat(s16x4 lo, s16x4 hi) { return __builtin_shufflevector(lo, hi, 0, 1, 2, 3, 4, 5, 6, 7); }
template <int N> DI void waitv() { asm volatile("s_waitcnt vmcnt(%0)" ::"n"(N) : "memory"); }
#define BAR() asm volatile("s_waitcnt lgkmcnt(0)\n\ts_barrier" ::: "memory")
#define LGKM0() __builtin_amdgcn_s_waitcnt(0xc07f)
#define SB0() __builtin_amdgcn_sched_barrier(0)
#define PRI(n) do { SB0(); __builtin_amdgcn_s_setprio(n); SB0(); } while (0)
#define PIN(x) asm volatile("" : "+v"(x))
#define SPIN_LOOP _Pragma("nounroll") for (int sp = 0; sp < SPIN_CAP; ++sp)
template <int N> DI float ror(float v) { return __builtin_bit_cast(float, __builtin_amdgcn_update_dpp(0, __builtin_bit_cast(int, v), 0x120 + N, 0xf, 0xf, false)); }
DI void sw16(float v, float& e, float& o) { unsigned u = __float_as_uint(v); auto r = __builtin_amdgcn_permlane16_swap(u, u, false, false); e = __uint_as_float(r[0]); o = __uint_as_float(r[1]); }
DI void sw32(float v, float& lo, float& hi) { unsigned u = __float_as_uint(v); auto r = __builtin_amdgcn_permlane32_swap(u, u, false, false); lo = __uint_as_float(r[0]); hi = __uint_as_float(r[1]); }
DI float sumq(float v) { float a, b; sw16(v, a, b); v = a + b; sw32(v, a, b); return a + b; }
DI float sumr(float v) { v += ror<8>(v); v += ror<4>(v); v += ror<2>(v); v += ror<1>(v); return v; }
DI void gatherq(float v, float (&a)[4]) { float e, o; sw16(v, e, o); sw32(e, a[0], a[2]); sw32(o, a[1], a[3]); }
typedef f32x2 cpx;
DI cpx cfma(cpx a, cpx b, cpx c) {
  cpx r;
  asm("v_pk_fma_f32 %0, %1, %2, %3 op_sel:[1,1,0] op_sel_hi:[1,0,1] neg_lo:[1,0,0]\n\tv_pk_fma_f32 %0, %1, %2, %0 op_sel_hi:[0,1,1]" : "=&v"(r) : "v"(a), "v"(b), "v"(c));
  return r;
}
DI cpx cmul(cpx a, cpx b) {
  cpx r;
  asm("v_pk_mul_f32 %0, %1, %2 op_sel:[1,1] op_sel_hi:[1,0] neg_lo:[1,0]\n\tv_pk_fma_f32 %0, %1, %2, %0 op_sel_hi:[0,1,1]" : "=&v"(r) : "v"(a), "v"(b));
  return r;
}
template <int V> struct IC { static constexpr int v = V; };

struct Ctx {
  const u16 *dsk, *x, *bre, *bim, *cre, *cim, *wg, *wu, *wd;
  char* ws;
  __amdgpu_buffer_rsrc_t rws;
  v2u tag;
  unsigned T, T2;
  int lane, w, rb, cg, rt, ct;
  f32x4 acc0[5];
  unsigned vw2, vh2, vfl;
  v4u wf[4][5], hf[3][4];
  v2u fl;
  unsigned vfs;
  unsigned long long pc;
};
DI v4u ld128(const Ctx& c, unsigned vo, unsigned so = 0) { return __builtin_amdgcn_raw_buffer_load_b128(c.rws, vo, so, 16); }
DI v2u ld64(const Ctx& c, unsigned vo) { return __builtin_amdgcn_raw_buffer_load_b64(c.rws, vo, 0, 16); }
DI void st128(const Ctx& c, v4u v, unsigned vo) { __builtin_amdgcn_raw_buffer_store_b128(v, c.rws, vo, 0, 16); }
DI void st_flag(const Ctx& c, unsigned vo) { __builtin_amdgcn_raw_buffer_store_b64(c.tag, c.rws, vo, 0, 16); }
DI unsigned long long fresh(const Ctx& c, v2u f) { return __ballot(f.x == c.T && f.y == c.T2); }

DI float fexp(float x) {
  const float t = x * 1.44269502162933349609375f;
  const float r = fmaf(x, 1.925963033500011e-8f, fmaf(x, 1.44269502162933349609375f, -t));
  const float e = __builtin_amdgcn_exp2f(t);
  return fmaf(e, r * 0.693147182464599609375f, e);
}
DI void fsincos(float z, float& sn, float& cs) {
  const float k = rintf(z * 0.63661977236758134f);
  float r = fmaf(k, -1.5707963705062866f, z);
  r = fmaf(k, 4.3711388286737929e-08f, r);
  r = fmaf(k, 1.7151245100058819e-15f, r);
  const float r2 = r * r;
  const float sp = fmaf(r * r2, fmaf(r2, fmaf(r2, -1.9515295891e-4f, 8.3321608736e-3f), -1.6666654611e-1f), r);
  const float cp = fmaf(r2 * r2, fmaf(r2, fmaf(r2, 2.443315711809948e-5f, -1.388731625493765e-3f), 4.166664568298827e-2f), fmaf(r2, -0.5f, 1.f));
  const int n = (int)k;
  sn = (n & 1) ? cp : sp; cs = (n & 1) ? sp : cp;
  if (n & 2) sn = -sn;
  if ((n + 1) & 2) cs = -cs;
}
constexpr int LP(int ch) { return ch == 0 ? 7 : (ch == 1 ? 9 : 10); }
constexpr int lds_chunk(int j) { return j == 7 ? 0 : (j == 9 ? 1 : (j == 10 ? 2 : -1)); }
template <int J> DI void ldw(const Ctx& c, v4u (&d)[5]) {
  const unsigned vo = J == 11 ? (c.w < 6 ? c.vw2 : OOB) : c.vw2;
#pragma unroll
  for (int a = 0; a < 4; ++a) d[a] = ld128(c, vo + a * 1024, J * 40960);
  d[4] = ld128(c, vo, J * 40960 + 4096);
}
template <int J> DI void ldh(const Ctx& c, v4u (&d)[4]) {
  const unsigned vo = J == 11 ? (c.w < 6 ? c.vh2 : OOB) : c.vh2;
#pragma unroll
  for (int m = 0; m < 4; ++m) d[m] = ld128(c, vo ^ (m * 1024), J * 32768);
}

template <int TYPE> struct G1 {
  static constexpr int NU = TYPE == 0 ? 1 : 3;
  static constexpr int NG = TYPE == 0 ? 10 : 6;
  static constexpr int NR = TYPE == 0 ? 14 : 8;
  static constexpr int NP = TYPE == 0 ? 6 : 5;
  struct Frags { bf16x8 x[TYPE == 0 ? 4 : 2]; s16x4 wl[4], wh[4]; bf16x8 s[2 * NU]; };
  v4u rx, rw0, rw1, rs;
  unsigned vx0, vx1, vw, vs;
  unsigned sx, sw, ss, ss2;
  unsigned ax[2][2][2], aw[4], as[2];
  f32x4 acc[4][4];
  f32x4 accs[2 * NU][2];
  f32x4 cs0, css[2 * NU];
  bf16x8 ones;
  int w;
  unsigned w1k, w4k;

  DI void init_stage(const Ctx& c, int unit0) {
    const int l = c.lane;
    w = c.w; w1k = w * 1024; w4k = w * 4096;
    rx = mkrsrc(c.x, L_ * D_ * 2);
    const unsigned ch = (l & 7) ^ ((l >> 3) & 3);
    vx0 = (l >> 3) * 1280 + ch * 16;
    vx1 = (l >> 3) * 1280 + (ch ^ 4) * 16;
    sx = (c.rb * 256 + w * 32) * 1280;
    rs = mkrsrc((w & 2) ? c.bim : c.bre, 640 * 640 * 2);
    vs = (l >> 3) * 1280 + (((l & 7) ^ (l >> 3)) * 16);
    if (TYPE == 0) {
      rw0 = mkrsrc(c.wg, D_ * F_ * 2); rw1 = mkrsrc(c.wu, D_ * F_ * 2);
      const unsigned key2 = ((l >> 4) & 1) | ((w & 1) << 1);
      vw = (l >> 3) * 3456 + (((l & 7) ^ (key2 << 1)) * 16);
      sw = w * 8 * 3456 + c.cg * 128;
      ss2 = (unit0 * 16 + (w & 1) * 8) * 1280;
    } else {
      ss = (min(unit0 + (w >> 2), 39) * 16 + (w & 1) * 8) * 1280;
      ss2 = (min(unit0 + 2, 39) * 16 + (w & 1) * 8) * 1280;
    }
  }
  DI void init_read(const Ctx& c) {
    const int l = c.lane, i = l & 15, q = l >> 4;
    const int wt = w & 3, wn = w >> 2;
#pragma unroll
    for (int kk = 0; kk < 2; ++kk) {
      const unsigned ch = (((kk * 4 + q) ^ (i & 7)) * 16);
#pragma unroll
      for (int pr = 0; pr < (TYPE == 0 ? 2 : 1); ++pr) {
        ax[0][pr][kk] = (wt * 64 + (pr ^ wn) * 32 + 8 * (i >> 2) + (i & 3)) * 128 + ch;
        ax[1][pr][kk] = ax[0][pr][kk] + 65536;
        PIN(ax[0][pr][kk]); PIN(ax[1][pr][kk]);
      }
      as[kk] = (TYPE == 0 ? LWS(0) : LWB(2)) + i * 128 + ch;
      PIN(as[kk]);
    }
    if (TYPE == 0) {
      const int r = (l >> 2) & 3, cc = l & 3;
      const unsigned key2 = ((r >> 1) & 1) | ((q & 1) << 1);
#pragma unroll
      for (int a = 0; a < 4; ++a) {
        const int act = a ^ wt;
        aw[a] = LWB(2) + (act >> 1) * 8192 + (8 * q + r) * 128 + (((2 * wn + (act & 1)) ^ key2) * 32) + cc * 8;
        PIN(aw[a]);
      }
    }
#pragma unroll
    for (int a = 0; a < 2 * NU; ++a) { css[a] = f32x4{0.f, 0.f, 0.f, 0.f}; PIN(css[a]); }
    ones = bf16x8{0x3f80, 0x3f80, 0x3f80, 0x3f80, 0x3f80, 0x3f80, 0x3f80, 0x3f80};
    PIN(ones);
  }
  template <int P, int S> DI void piece() {
    constexpr int K = S % 3;
    if constexpr (P < 4) glds16<S * 128 + P * 10240, LX(K) + P * 1024>(rx, (P & 1) ? vx1 : vx0, sx, w4k);
    else if constexpr (P < 6) {
      if constexpr (TYPE == 0) glds16<S * (64 * 3456), LWB(K) + (P - 4) * 8192>(P == 4 ? rw0 : rw1, vw, sw, w1k);
      else if constexpr (P == 4) glds16<S * 128, LWB(K)>(rs, vs, ss, w1k);
    } else {
      if (w < 4) glds16<S * 128, (TYPE == 0 ? LWS(K) : LWB(K) + 8192)>(rs, vs, ss2, w1k);
    }
  }
  template <int S> DI void half0() { piece<6, S>(); piece<0, S>(); piece<4, S>(); piece<1, S>(); }
  template <int S> DI void half1() { piece<2, S>(); piece<5, S>(); piece<3, S>(); }

  template <int K, int KK, int R> DI void rd(Frags& f) {
    constexpr unsigned ow = LWB(K) - LWB(2), ox = K == 1 ? 32768 : 0;
    if constexpr (TYPE == 0) {
      if (R == 0) f.wl[0] = trread(aw[0] + (ow + KK * 4096));
      else if (R == 1) f.wh[0] = trread(aw[0] + (ow + KK * 4096 + 512));
      else if (R < 6) f.x[(R - 2) & 3] = ldsr<bf16x8>(ax[K == 2][((R - 2) >> 1) & 1][KK] + (ox + ((R - 2) & 1) * 512));
      else if (R < 12) {
        constexpr int a = ((R - 4) >> 1) & 3;
        if (R & 1) f.wh[a] = trread(aw[a] + (ow + KK * 4096 + 512)); else f.wl[a] = trread(aw[a] + (ow + KK * 4096));
      } else f.s[R & 1] = ldsr<bf16x8>(as[KK] + (K * 4096 + (R & 1) * 2048));
    } else {
      if (R == 1 || R == 2) f.x[(R - 1) & 1] = ldsr<bf16x8>(ax[K == 2][0][KK] + (ox + ((R - 1) & 1) * 512));
      else { constexpr int a = R == 0 ? 0 : R - 2; f.s[a] = ldsr<bf16x8>(as[KK] + (ow + a * 2048)); }
    }
  }
  template <bool Z, int M> DI void mm(const Frags& f) {
    if constexpr (TYPE == 0 && M < 16) mf<Z>(acc[M >> 2][M & 3], cat(f.wl[M >> 2], f.wh[M >> 2]), f.x[M & 3]);
    else { constexpr int a = (M - (TYPE == 0 ? 16 : 0)) >> 1; mf<Z>(accs[a][M & 1], f.x[M & 1], f.s[a]); }
  }
  template <int SUB, int G, class H> DI void group(const Frags& cur, Frags& nxt, H& hook) {
    constexpr int N = SUB + 1, K = (N >> 1) % 3, KK = N & 1;
    constexpr bool RD = N < 20;
    mm<SUB == 0, 2 * G>(cur);
    if constexpr (RD && 2 * G < NR) rd<K, KK, 2 * G>(nxt);
    mm<SUB == 0, 2 * G + 1>(cur);
    if constexpr (RD && 2 * G + 1 < NR) rd<K, KK, 2 * G + 1>(nxt);
    if constexpr (TYPE == 0 && G == 0) mf<SUB == 0>(cs0, ones, cat(cur.wl[0], cur.wh[0]));
    if constexpr (G == (TYPE == 0 ? 2 : 0)) {
      if (__builtin_expect(w == (SUB & 7), 0)) {
#pragma unroll
        for (int a = 0; a < 2 * NU; ++a) mfa(css[a], ones, cur.s[a]);
      }
    }
    hook(IC<G>{});
    SB0();
    if constexpr (G + 1 < NG) group<SUB, G + 1>(cur, nxt, hook);
  }
  template <int SUB, class H> DI void part(const Frags& cur, Frags& nxt, H hook) { group<SUB, 0>(cur, nxt, hook); }
  template <int R> DI void rdall(Frags& f) { rd<0, 0, R>(f); if constexpr (R + 1 < NR) rdall<R + 1>(f); }
};

template <int NU> struct Side {
  unsigned xr[5], pa[NU], pb[NU], pd[NU];
  float v[10], s, e;
  unsigned a_mu, a_cs, v_hg, v_fg, a_st, a_css, a_aw, a_ar, v_pub, v_lk0, v_lk1, v_hs[NU], v_fs, a_tw, a_tr, v_x0, v_x1, a_bs, a_bx, a_p8, a_w0, a_w1, a_k0, a_k1;
  static constexpr int NCH = 16;
  DI void table(const Ctx& c, int u) const {
    if (c.w >= 6) return;
    const int k = threadIdx.x >> 4;
    const float fn = (float)(k < 7 ? 8 * k - 24 : (k < 16 ? 32 * k - 256 : (k < 23 ? 256 * k - 4096 : 1)));
    const float dt = rbf(fexp(bf2f(pd[u])));
    const float ea = dt * bf2f(pa[u]) * 1.44269504088896341f, eb = dt * bf2f(pb[u]) * 0.15915494309189535f;
    const float m = fn < 0.f ? 0.f : __builtin_amdgcn_exp2f(fn * ea);
    const float r = __builtin_amdgcn_fractf(fn * eb);
    ldsw<f32x2>(L_POW(NU == 1 ? 0 : 1, u) + threadIdx.x * 8, f32x2{m * __builtin_amdgcn_cosf(r), m * __builtin_amdgcn_sinf(r)});
  }
  template <int N> DI void tick(Ctx& c, int srow, int unit0) {
    const int l = c.lane, i = l & 15, q = l >> 4, w = c.w, wt = w & 3, wn = w >> 2, grp = wt * 2 + wn;
    if constexpr (N == 0) {
#pragma unroll
      for (int j = 0; j < 5; ++j) { v[2 * j] = bf2f(xr[j]); v[2 * j + 1] = bfhi(xr[j]); PIN(v[2 * j]); PIN(v[2 * j + 1]); }
    } else if constexpr (N == 1) {
      s = ((v[0] + v[1]) + (v[2] + v[3])) + ((v[4] + v[5]) + (v[6] + v[7])) + (v[8] + v[9]); PIN(s);
    } else if constexpr (N == 2) { s = sumr(s); PIN(s); }
    else if constexpr (N == 3) { s = sumq(s) * (1.f / 640.f); PIN(s); }
    else if constexpr (N == 4) {
#pragma unroll
      for (int j = 0; j < 10; ++j) { v[j] -= s; PIN(v[j]); }
    } else if constexpr (N == 5) {
      e = v[0] * v[0];
#pragma unroll
      for (int j = 1; j < 10; ++j) e = fmaf(v[j], v[j], e);
      PIN(e);
    } else if constexpr (N == 6) { e = sumr(e); PIN(e); }
    else if constexpr (N == 7) {
      const float var = rbf(sumq(e) * (1.f / 640.f));
      const float sd = rbf(__builtin_amdgcn_sqrtf(rbf(var + 1e-5f)));
      float rc = __builtin_amdgcn_rcpf(sd);
      rc = fmaf(fmaf(-sd, rc, 1.f), rc, rc);
      st128(c, v4u{__float_as_uint(rbf(s)), c.T, __float_as_uint(rc), c.T2}, WS_STAT + srow * 16);
    } else {
      const int k0 = c.rb - 1 - q;
      const unsigned fbase = WS_FLAGH + (c.rb * 4 + wt) * 2048;
      const unsigned r8 = ((l & 12) << 4) | ((l & 3) << 3);
      if constexpr (N == 8) {
        a_css = (NU == 1 ? L_CSS : L_CS3) + q * 128 + i * 4;
        a_ar = L_AGG + i * 64 + q * 16;
        a_aw = a_ar + grp * 8 - 48;
#pragma unroll
        for (int u = 0; u < NU; ++u) { v_hs[u] = (unit0 + u < 40 ? WS_H + (c.rb * 4 + wt) * H_RT + ustep(unit0 + u) * 4096 + wn * 2048 : OOB) + l * 16; PIN(v_hs[u]); }
        PIN(a_css); PIN(a_ar); PIN(a_aw);
      } else if constexpr (N == 9) {
        const unsigned po = (WS_AGG + unit0 * 256) + i * 16;
        v_pub = q == 3 ? po + c.rb * AGG_RB : OOB;
        v_lk0 = k0 >= 0 ? po + q * AGG_RB : OOB;
        v_lk1 = k0 >= 4 ? po + (q + 4) * AGG_RB : OOB;
        PIN(v_pub); PIN(v_lk0); PIN(v_lk1);
      } else if constexpr (N == 10) {
        const unsigned tb = L_POW(NU == 1 ? 0 : 1, 2) + i * 8;
        a_p8 = tb + q * 128;
        a_w0 = tb + (8 + max(grp - 1 - 2 * q, -1)) * 128;
        a_w1 = tb + (8 + max(grp - 2 - 2 * q, -1)) * 128;
        a_k0 = tb + (16 + max(k0, 0)) * 128;
        a_k1 = tb + (16 + max(k0 - 4, 0)) * 128;
        PIN(a_p8); PIN(a_w0); PIN(a_w1); PIN(a_k0); PIN(a_k1);
      } else if constexpr (N == 11) {
        if (NU == 1) {
          const int step = 2 * c.cg + wn;
          a_mu = (L_ST + wt * 512 + wn * 256) + r8;
          a_cs = (L_CS + wn * 256 + wt * 64) + q * 16;
          v_hg = (WS_H + (c.rb * 4 + wt) * H_RT + step * 4096 + wn * 2048) + l * 16;
          v_fg = (fbase + (step & 7) * 256 + (step >> 3) * 16) + (l & 1) * 8;
          PIN(a_mu); PIN(a_cs); PIN(v_hg); PIN(v_fg);
        }
      } else if constexpr (N == 12) {
        const int step = ustep(NU == 1 ? unit0 : min(unit0 + min(l, 2), 39));
        v_fs = fbase + (step & 7) * 256 + (step >> 3) * 16 + wn * 8;
        a_st = (L_ST + grp * 256) + q * 64;
        a_bs = (L_ST + c.rt * 512 + (wt >> 1) * 256 + (wt & 1) * 32) + r8;
        a_bx = ((wt >> 1) * 5120 + (wt & 1) * 640) + r8 * 20 + q * 8;
        PIN(v_fs); PIN(a_st); PIN(a_bs); PIN(a_bx);
      } else if constexpr (N == 13) {
        const unsigned tr = LWB(2) + 8192 + w * 1024;
        a_tw = tr + i * 64 + ((q ^ (i >> 1)) & 3) * 16;
        a_tr = tr + (l >> 2) * 64 + ((l ^ (l >> 3)) & 3) * 16;
        PIN(a_tw); PIN(a_tr);
      } else if constexpr (N == 14) {
        c.vh2 = (WS_H + (c.rb * 4 + c.rt) * H_RT + w * 4096 + wt * 1024) + l * 16;
        c.vfl = (WS_FLAGH + (c.rb * 4 + c.rt) * 2048 + w * 256) + (l & 31) * 8;
        c.vw2 = (WS_W2T + c.ct * W2T_CT + w * 5120) + l * 16;
        PIN(c.vh2); PIN(c.vfl); PIN(c.vw2);
      } else {
        const unsigned c0 = w * 64 + l, r0 = (c0 * 6554) >> 16, c1 = c0 + 512, r1 = (c1 * 6554) >> 16, xb = (c.rb * 256 + c.rt * 64) * 1280 + c.ct * 160;
        v_x0 = xb + r0 * 1120 + c0 * 16;
        v_x1 = w < 2 ? xb + r1 * 1120 + c1 * 16 : c.ct * 160 + l * 16;
        PIN(v_x0); PIN(v_x1);
      }
    }
  }
};

template <int TYPE> DI void phase1(Ctx& c, const u16* lre, const u16* lim, const u16* ldt) {
  const int l = c.lane, i = l & 15, q = l >> 4, w = c.w, wt = w & 3, wn = w >> 2;
  const int rb = c.rb, cg = c.cg;
  constexpr int NU = G1<TYPE>::NU, NP = G1<TYPE>::NP;
  G1<TYPE> g;
  Side<NU> sd;
  const int srow = rb * 256 + cg * 8 + w;
  const int si = rb * 5 + (cg - 27), mi = rb * 27 + cg;
  const int unit0 = TYPE == 0 ? cg : 27 + 3 * (cg - 27);
  __builtin_amdgcn_s_waitcnt(0x0070);
  unsigned cpf;
  {
    const unsigned n = ((blockIdx.x >> 3) & 7) * 64 + l;
    if (w == 0) asm volatile("global_load_dword %0, %1, %2" : "=v"(cpf) : "v"(min(n * 128u, CODE_PF)), "s"(c.pc) : "memory");
  }
  unsigned ep, qa, qb, qd;
  v4u xq;
  {
    const u16* xrow = c.x + (size_t)srow * D_;
    asm volatile("s_load_dword %2, %6, 0x0\n\ts_nop 4\n\tglobal_load_dwordx4 %0, %3, %5\n\tglobal_load_dword %1, %4, %5 offset:1024"
                 : "=&v"(xq), "=&v"(sd.xr[4]), "=&s"(ep) : "v"(l * 16), "v"(l * 4), "s"(xrow), "s"(c.ws) : "memory");
    if (TYPE == 1)
      asm volatile("s_nop 4\n\tglobal_load_ushort %0, %3, %4\n\tglobal_load_ushort %1, %3, %5\n\tglobal_load_ushort %2, %3, %6"
                   : "=&v"(qa), "=&v"(qb), "=&v"(qd) : "v"(((si >> 2) * 64 + l) * 2), "s"(lre), "s"(lim), "s"(ldt) : "memory");
  }
  auto params = [&]() {
#pragma unroll
    for (int u = 0; u < NU; ++u) {
      const int ua = min(unit0 + u, 39);
      asm volatile("s_nop 4\n\tglobal_load_ushort %0, %3, %4\n\tglobal_load_ushort %1, %3, %5\n\tglobal_load_ushort %2, %3, %6"
                   : "=&v"(sd.pa[u]), "=&v"(sd.pb[u]), "=&v"(sd.pd[u]) : "v"((ua * 16 + i) * 2), "s"(lre), "s"(lim), "s"(ldt) : "memory");
    }
  };
  auto ticks = [&](auto self, auto N, auto E) { if constexpr (N.v < E.v) { sd.template tick<N.v>(c, srow, unit0); self(self, IC<N.v + 1>{}, E); } };
  g.init_stage(c, unit0);
  unsigned wflag = OOB, ja = 0, jb = 0, jc = 0;
  if (TYPE == 0) {
    if (wn == 0) { g.template half0<0>(); g.template half1<0>(); }
    g.init_read(c);
    ja = (l >> 2) * 320 + w * 32 + (l & 3) * 8;
    jb = ja + 5120; jc = ja + 5376;
    ja += LWB(2) + 8192; jb += jb < 8192 ? LWB(2) + 8192 : LWS(2) - 8192; jc += jc < 8192 ? LWB(2) + 8192 : LWS(2) - 8192;
    PIN(ja); PIN(jb); PIN(jc);
    ticks(ticks, IC<8>{}, IC<Side<NU>::NCH>{});
    if (wn != 0) { g.template half0<0>(); g.template half1<0>(); }
    const v4u rj = mkrsrc(c.wd, F_ * D_ * 2);
    const unsigned c0 = w * 64 + l, r0 = (c0 * 3277) >> 16, c1 = c0 + 512, r1 = (c1 * 3277) >> 16;
    glds16<0, LWB(2) + 8192>(rj, r0 * 960 + c0 * 16, (mi >> 2) * 40960 + (mi & 3) * 320, g.w1k);
    if (w < 2) glds16<0, LWS(2)>(rj, r1 * 960 + c1 * 16, (mi >> 2) * 40960 + (mi & 3) * 320, g.w1k);
  } else {
    params();
    auto req = [&]() {
      const v4u rj = mkrsrc(wn ? c.cim : c.cre, 640 * 640 * 2);
      const unsigned r0 = wt * 40 + (l >> 3);
#pragma unroll
      for (int j = 0; j < 5; ++j) {
        const unsigned row = r0 + j * 8;
        glds16<0, LX(2)>(rj, row * 1280 + (((l & 7) ^ ((row >> 1) & 7)) * 16), (si & 3) * 160 * 1280 + (si >> 2) * 128, (w * 5 + j) * 1024);
      }
    };
    if (wn == 0) req();
    g.init_read(c);
    ticks(ticks, IC<8>{}, IC<Side<NU>::NCH>{});
    if (wn != 0) req();
    g.template half0<0>(); g.template half1<0>();
  }
  asm volatile("s_waitcnt vmcnt(%3) lgkmcnt(0)" : "+v"(xq), "+v"(sd.xr[4]), "+s"(ep) : "n"(TYPE == 0 ? 7 : 10) : "memory");
  sd.xr[0] = xq.x; sd.xr[1] = xq.y; sd.xr[2] = xq.z; sd.xr[3] = xq.w;
  asm volatile("" ::"v"(cpf));
  c.T = ep + 1u;
  c.T2 = (c.T * 0x9E3779B1u) ^ 0x5bd1e995u;
  c.tag = v2u{c.T, c.T2};
  PIN(c.tag);
  typename G1<TYPE>::Frags fa, fb;
  unsigned wpd = 0;
  if (TYPE == 0) {
    ticks(ticks, IC<0>{}, IC<4>{});
    __builtin_amdgcn_s_barrier();
    g.template half0<1>();
    ticks(ticks, IC<4>{}, IC<7>{});
    g.template half1<1>();
    if (w >= 5) {
      const unsigned vo = w < 7 ? (l < 56 ? (192 + 56 * rb + l) * 3456 + cg * 128 : OOB) : (l < 28 ? (cg * 16 + (rb & 3) * 4 + (l & 3)) * 1280 + (3 + (l >> 2)) * 128 : OOB);
      const v4u rb2 = mkrsrc((rb & 4) ? c.bim : c.bre, 640 * 640 * 2);
      asm volatile("s_nop 4\n\tbuffer_load_dword %0, %1, %2, 0 offen" : "+v"(wpd) : "v"(vo), "s"(w == 5 ? g.rw0 : (w == 6 ? g.rw1 : rb2)) : "memory");
    }
    ticks(ticks, IC<7>{}, IC<8>{});
    waitv<7>();
    BAR();
    g.template rdall<0>(fa);
  } else {
    asm volatile("" : "+v"(qa), "+v"(qb), "+v"(qd));
    if (w == 7) {
      const float ar = bf2f(qa), ai = bf2f(qb), dt = rbf(fexp(bf2f(qd))), e = fexp(dt * ar);
      float sn, cs;
      fsincos(dt * ai, sn, cs);
      const float inv = 2.f / (ar * ar + ai * ai);
      ldsw<f32x2>(L_COEF + l * 8, cmul(cpx{e * cs - 1.f, e * sn}, cpx{ar * inv, -ai * inv}));
    }
#pragma unroll
    for (int u = 0; u < NU; ++u) { asm volatile("" : "+v"(sd.pa[u]), "+v"(sd.pb[u]), "+v"(sd.pd[u])); sd.table(c, u); }
    g.template half0<1>(); g.template half1<1>();
    waitv<10>();
    BAR();
    const f32x4 k0 = ldsr<f32x4>(L_COEF + (16 * wt + 4 * q) * 8), k1 = ldsr<f32x4>(L_COEF + (16 * wt + 4 * q) * 8 + 16);
    const cpx kc[4] = {{k0.x, k0.y}, {k0.z, k0.w}, {k1.x, k1.y}, {k1.z, k1.w}};
    const int step = ustep(4 * (si >> 2) + wt), ct = 2 * (si & 3) + wn;
#pragma unroll
    for (int j = 0; j < 5; ++j) {
      const unsigned row = (wn * 5 + j) * 16 + i;
      const unsigned a = LX(2) + row * 128 + (((2 * wt + (q >> 1)) ^ ((row >> 1) & 7)) * 16) + (q & 1) * 8;
      const v2u cr = ldsr<v2u>(a), ci = ldsr<v2u>(a + 20480);
      const unsigned cr0 = cr.x, cr1 = cr.y, ci0 = ci.x, ci1 = ci.y;
      const cpx p0 = cmul(kc[0], cpx{bf2f(cr0), bf2f(ci0)}), p1 = cmul(kc[1], cpx{bfhi(cr0), bfhi(ci0)});
      const cpx p2 = cmul(kc[2], cpx{bf2f(cr1), bf2f(ci1)}), p3 = cmul(kc[3], cpx{bfhi(cr1), bfhi(ci1)});
      const v4u o = {pk2(p0.x, p1.x), pk2(p2.x, p3.x), pk2(-p0.y, -p1.y), pk2(-p2.y, -p3.y)};
      st128(c, o, l * 16 + j * 1024 + (WS_W2T + ct * W2T_CT + step * 5120));
    }
    wflag = WS_FLAGW + (ct * 8 + (step & 7)) * 128 + (step >> 3) * 8;
    SB0();
    waitv<10>();
    BAR();
    g.template rdall<0>(fa);
  }
  SB0();
  v4u sg; v2u fw;
  bf16x8 jv0, jv1;
  const v4u rwsv = mkrsrc(c.ws, WS_END);
  auto w2l = [&](auto CH, auto A) { glds16sc1<LP(CH.v) * 40960 + A.v * 1024, tile_base(CH.v, A.v)>(rwsv, c.vw2, A.v < 4 ? g.w4k : g.w1k); };
  auto side = [&](auto SUB, auto G) {
    if constexpr (TYPE == 1 && G.v >= 4 && SUB.v * 2 + G.v - 4 < 8) sd.template tick<SUB.v * 2 + G.v - 4>(c, srow, unit0);
    if constexpr (TYPE == 0 && SUB.v == 17 && G.v == 8) sd.table(c, 0);
    if constexpr (SUB.v == 19) {
      constexpr int a = TYPE == 0 ? (G.v == 0 ? 0 : (G.v == 2 ? 1 : (G.v == 3 ? 2 : (G.v == 5 ? 3 : (G.v == 7 ? 4 : -1))))) : G.v - 1;
      if constexpr (a >= 0) c.wf[0][a] = a < 4 ? ld128(c, c.vw2 + a * 1024) : ld128(c, c.vw2, 4096);
    }
  };
  constexpr int GA = TYPE == 0 ? 4 : 2, GB = TYPE == 0 ? 6 : 4;
  auto step = [&](auto SS) {
    constexpr int S = SS.v;
    LGKM0();
    g.template part<2 * S>(fa, fb, [&](auto G) {
      if constexpr (S == 0) {
        if constexpr (TYPE == 0) {
          if (G.v == 7) { jv0 = cat(trread(ja), trread(jb)); jv1 = cat(trread(ja + 256), trread(jc)); }
        }
        if (G.v == 0) g.template piece<0, 2>();
        if (G.v == 1) g.template piece<4, 2>();
        if (G.v == 2) g.template piece<1, 2>();
        if (G.v == 3) g.template piece<2, 2>();
        if (G.v == 4) g.template piece<3, 2>();
      } else if constexpr (S + 2 < 10) {
        if (G.v == 1) g.template piece<2, S + 2>();
        if (G.v == GA) g.template piece<5, S + 2>();
        if (G.v == GB) g.template piece<3, S + 2>();
      } else {
        if (G.v == 1) w2l(IC<S - 8>{}, IC<3>{});
        if (G.v == GB) w2l(IC<S - 8>{}, IC<4>{});
      }
      side(IC<2 * S>{}, G);
    });
    if constexpr (S < 9) {
      if (S == 7) asm volatile("s_waitcnt vmcnt(%2)" : "+v"(sg), "+v"(fw) : "n"(NP) : "memory");
      else if (S == 8) waitv<5>();
      else if (S == 0) waitv<TYPE == 0 ? 5 : 10>();
      else if (S == 3 && TYPE == 0) waitv<NP + 1>();
      else waitv<NP>();
      LGKM0();
      asm volatile("s_barrier" ::: "memory");
    } else LGKM0();
    if (S == 0) { asm volatile("" ::"v"(wpd)); g.template piece<6, 2>(); g.template piece<5, 2>(); }
    if (S == 2 && TYPE == 0) {
      const unsigned jo = WS_W2T + (mi & 3) * 2 * W2T_CT + (mi >> 2) * 5120;
      st128(c, __builtin_bit_cast(v4u, jv0), (jo + (w < 5 ? w * 1024 : W2T_CT + (w - 5) * 1024)) + l * 16);
      if (w < 2) st128(c, __builtin_bit_cast(v4u, jv1), (jo + W2T_CT + (3 + w) * 1024) + l * 16);
    }
    if (S == 4 && TYPE == 0) { if (w == 0) st_flag(c, (WS_FLAGW + ((mi & 3) * 16 + ((mi >> 2) & 7)) * 128 + (mi >> 5) * 8) + (l & 1) * 1024); }
    if (S == 1 && TYPE == 1) st_flag(c, wflag);
    if (S == 6) {
      if (TYPE == 0) params();
      const GAS char* sp = (const GAS char*)c.ws + WS_STAT + rb * 4096;
      const GAS char* fp = (const GAS char*)c.ws + WS_FLAGW + (c.ct * 8 + w) * 128;
      asm volatile("s_nop 4\n\tglobal_load_dwordx4 %0, %2, %3 sc1\n\tglobal_load_dwordx2 %1, %4, %5 sc1"
                   : "=&v"(sg), "=&v"(fw) : "v"(wt * 1024 + l * 16), "s"(sp), "v"(i * 8), "s"(fp) : "memory");
    }
    if (S == 7) {
      if (TYPE == 0) asm volatile("" : "+v"(sd.pa[0]), "+v"(sd.pb[0]), "+v"(sd.pd[0]));
      const bool need = i < 12 && w + 8 * i < NS2;
      SPIN_LOOP {
        if (__all((!need || (fw.x == c.T && fw.y == c.T2)) && sg.y == c.T && sg.w == c.T2)) break;
        __builtin_amdgcn_s_sleep(1);
        asm volatile("" ::: "memory");
        sg = ld128(c, WS_STAT + rb * 4096 + wt * 1024 + l * 16);
        fw = ld64(c, WS_FLAGW + (c.ct * 8 + w) * 128 + i * 8);
      }
      ldsw<v2u>(L_ST + (wt * 64 + l) * 8, v2u{sg.x, sg.z});
      glds16<0, L_XT0>(g.rx, sd.v_x0, 0, g.w1k);
      if (w < 3) glds16<0, L_XT1>(w == 2 ? mkrsrc(c.dsk, 1280) : g.rx, sd.v_x1, 0, g.w1k);
    }
    SB0();
    g.template part<2 * S + 1>(fb, fa, [&](auto G) {
      if constexpr (S + 3 < 10) {
        if (G.v == 0) g.template piece<6, S + 3>();
        if (G.v == (TYPE == 0 ? 2 : 1)) g.template piece<0, S + 3>();
        if (G.v == GA) g.template piece<4, S + 3>();
        if (G.v == GB) g.template piece<1, S + 3>();
      } else if constexpr (S < 9) {
        if (G.v == 1) w2l(IC<S - 7>{}, IC<0>{});
        if (G.v == GA) w2l(IC<S - 7>{}, IC<1>{});
        if (G.v == GB) w2l(IC<S - 7>{}, IC<2>{});
      } else {
        if (G.v == G1<TYPE>::NG - 1) {
          if (TYPE == 0) ldsw<float>(L_CS + ((wn * 4 + wt) * 16 + i) * 4, g.cs0.x);
          const unsigned ca = (TYPE == 0 ? L_CSS : L_CS3) + w * 128 + i * 4;
#pragma unroll
          for (int a = 0; a < 2 * NU; ++a) ldsw<float>(ca + (a >> 1) * 1024 + (a & 1) * 64, g.css[a].x);
        }
      }
      side(IC<2 * S + 1>{}, G);
    });
  };
  step(IC<0>{}); step(IC<1>{}); step(IC<2>{}); step(IC<3>{}); step(IC<4>{});
  step(IC<5>{}); step(IC<6>{}); step(IC<7>{}); step(IC<8>{}); step(IC<9>{});
  MFMA_DRAIN();
  BAR();
  SB0();
  auto w2c2 = [&]() { w2l(IC<2>{}, IC<0>{}); w2l(IC<2>{}, IC<1>{}); w2l(IC<2>{}, IC<2>{}); w2l(IC<2>{}, IC<3>{}); w2l(IC<2>{}, IC<4>{}); };
  __builtin_amdgcn_s_setprio(3);
  if (TYPE == 0) {
    f32x4 cs[4];
    f32x2 mu[4];
#pragma unroll
    for (int a = 0; a < 4; ++a) cs[a] = ldsr<f32x4>(sd.a_cs ^ (a * 64));
#pragma unroll
    for (int m = 0; m < 4; ++m) mu[m] = ldsr<f32x2>((m < 2 ? sd.a_mu : (sd.a_mu ^ 256)) + (m & 1) * 32);
    auto glu = [&](auto ODD) {
#pragma unroll
      for (int m = 0; m < 4; ++m) {
        const f32x4 mv = {-mu[m].x, -mu[m].x, -mu[m].x, -mu[m].x};
        const float r2 = mu[m].y * mu[m].y;
        const f32x4 h0 = __builtin_elementwise_fma(mv, cs[ODD.v], g.acc[ODD.v][m]) * __builtin_elementwise_fma(mv, cs[2 + ODD.v], g.acc[2 + ODD.v][m]) * r2;
        const f32x4 h1 = __builtin_elementwise_fma(mv, cs[1 - ODD.v], g.acc[1 - ODD.v][m]) * __builtin_elementwise_fma(mv, cs[3 - ODD.v], g.acc[3 - ODD.v][m]) * r2;
        st128(c, v4u{pk2(h0[0], h0[1]), pk2(h0[2], h0[3]), pk2(h1[0], h1[1]), pk2(h1[2], h1[3])}, (m < 2 ? sd.v_hg : (sd.v_hg ^ 2048)) + (m & 1) * 1024);
        asm volatile("" ::: "memory");
      }
    };
    if (w & 1) glu(IC<1>{}); else glu(IC<0>{});
    SB0();
  } else {
    if (si == 39) {
      SPIN_LOOP {
        bool ok = true;
        asm volatile("" ::: "memory");
#pragma unroll
        for (int k = 0; k < 4; ++k) {
          const v4u t = ld128(c, WS_STAT + (k * 512 + w * 64 + l) * 16);
          ok = ok && t.y == c.T && t.w == c.T2;
        }
        if (__all(ok)) break;
        __builtin_amdgcn_s_sleep(1);
      }
    }
    w2c2(); ldw<1>(c, c.wf[1]);
    SB0();
  }
  PRI(2);
  cpx v[NU][8], eq[NU], tot[NU], lam[NU], L32[NU], f1[NU], W0[NU], W1[NU], wk0[NU], wk1[NU];
  {
    f32x4 st[4];
#pragma unroll
    for (int e = 0; e < 4; ++e) st[e] = ldsr<f32x4>(sd.a_st + e * 16);
    const unsigned a_pi = sd.a_p8 & ~384u;
#pragma unroll
    for (int u = 0; u < NU; ++u) {
      const unsigned to = L_POW(TYPE, u) - L_POW(TYPE, 2);
      lam[u] = ldsr<cpx>(a_pi + to + 23 * 128);
      const cpx L8 = ldsr<cpx>(a_pi + to + 4 * 128), u2 = ldsr<cpx>(sd.a_p8 + to), u1 = ldsr<cpx>(sd.a_p8 + to + 128), u0 = ldsr<cpx>(sd.a_p8 + to + 256);
      f1[u] = ldsr<cpx>(sd.a_p8 + to + 384); L32[u] = ldsr<cpx>(a_pi + to + 9 * 128);
      W0[u] = ldsr<cpx>(sd.a_w0 + to); W1[u] = ldsr<cpx>(sd.a_w1 + to);
      const cpx lg = ldsr<cpx>(a_pi + (to + 8 * 128) + (wt * 2 + wn) * 128);
      wk0[u] = ldsr<cpx>(sd.a_k0 + to); wk1[u] = ldsr<cpx>(sd.a_k1 + to);
      const unsigned ac = sd.a_css + u * 1024;
      const float csx = sumq(ldsr<float>(ac) + ldsr<float>(ac + 512)), csy = sumq(ldsr<float>(ac + 64) + ldsr<float>(ac + 576));
#pragma unroll
      for (int j = 0; j < 8; ++j) {
        const float mu = (j & 1) ? st[j >> 1].z : st[j >> 1].x, rs = (j & 1) ? st[j >> 1].w : st[j >> 1].y;
        float a = fmaf(-mu, csx, g.accs[2 * u][j >> 2][j & 3]), b = fmaf(-mu, csy, g.accs[2 * u + 1][j >> 2][j & 3]);
        asm("" : "+v"(a), "+v"(b));
        v[u][j] = cpx{a * rs, b * rs};
        asm("" : "+v"(v[u][j]));
      }
      cpx h = v[u][0];
#pragma unroll
      for (int j = 1; j < 8; ++j) h = cfma(lam[u], h, v[u][j]);
      float ax[4], ay[4];
      gatherq(h.x, ax); gatherq(h.y, ay);
      eq[u] = cfma(u0, cpx{ax[0], ay[0]}, cfma(u1, cpx{ax[1], ay[1]}, cpx{u2.x * ax[2], u2.x * ay[2]}));
      tot[u] = cfma(L8, eq[u], h);
      if (l >= 48) ldsw<f32x2>(sd.a_aw + u * 1024, tot[u]);
      wk0[u] = cmul(wk0[u], lg); wk1[u] = cmul(wk1[u], lg);
    }
  }
  PRI(3);
  BAR();
  cpx pm[NU];
#pragma unroll
  for (int u = 0; u < NU; ++u) {
    const f32x4 t = ldsr<f32x4>(sd.a_ar + u * 1024);
    pm[u] = cfma(W0[u], cpx{t.x, t.y}, cmul(W1[u], cpx{t.z, t.w}));
  }
  if (w == 7) {
#pragma unroll
    for (int u = 0; u < NU; ++u) {
      const cpx P = cfma(L32[u], cpx{sumq(pm[u].x), sumq(pm[u].y)}, tot[u]);
      st128(c, v4u{__float_as_uint(P.x), c.T, __float_as_uint(P.y), c.T2}, sd.v_pub + u * 256);
    }
  }
  SB0();
  if (TYPE == 0) {
    if (w == 7) waitv<1>(); else waitv<0>();
    st_flag(c, sd.v_fg);
  } else {
    if (si == 39 && w == 0) __builtin_amdgcn_raw_buffer_store_b32(c.T, c.rws, WS_EPOCH, 0, 16);
  }
  asm volatile("s_barrier" ::: "memory");
  PRI(2);
  if (TYPE == 0) { w2c2(); ldw<1>(c, c.wf[1]); ldw<2>(c, c.wf[2]); ldw<3>(c, c.wf[3]); }
  PRI(3);
  {
    const f32x2 st = ldsr<f32x2>(sd.a_bs);
    const float nm = -st.x * st.y;
    auto item = [&](int a) {
      const unsigned xo = sd.a_bx + a * 32;
      const v2u xv = ldsr<v2u>(xo < 8192 ? L_XT0 + xo : L_XT1 - 8192 + xo), dv = ldsr<v2u>(L_XT1 + 2048 + q * 8 + a * 32);
      const unsigned x0 = xv.x, x1 = xv.y, d0 = dv.x, d1 = dv.y;
      const float xf[4] = {bf2f(x0), bfhi(x0), bf2f(x1), bfhi(x1)}, df[4] = {bf2f(d0), bfhi(d0), bf2f(d1), bfhi(d1)};
      f32x4 r;
#pragma unroll
      for (int e = 0; e < 4; ++e) {
        float xn = fmaf(xf[e], st.y, nm);
        asm("" : "+v"(xn));
        r[e] = fmaf(xn, df[e], xf[e]);
      }
      return r;
    };
    const f32x4 z = {0.f, 0.f, 0.f, 0.f};
    if (wn == 0) { c.acc0[0] = item(0); c.acc0[1] = item(1); c.acc0[2] = item(2); c.acc0[3] = z; c.acc0[4] = z; }
    else { c.acc0[0] = z; c.acc0[1] = z; c.acc0[2] = z; c.acc0[3] = item(3); c.acc0[4] = item(4); }
#pragma unroll
    for (int a = 0; a < 5; ++a) PIN(c.acc0[a]);
    SB0();
  }
  v4u lk0[NU], lk1[NU];
#pragma unroll
  for (int u = 0; u < NU; ++u) { lk0[u] = ld128(c, sd.v_lk0 + u * 256); lk1[u] = ld128(c, sd.v_lk1 + u * 256); }
  if (TYPE == 0) c.fl = ld64(c, c.vfl);
  else { PRI(1); ldw<2>(c, c.wf[2]); ldw<3>(c, c.wf[3]); PRI(3); }
  auto hwait = [&]() {
    if ((fresh(c, c.fl) & 0xfffull) != 0xfffull) {
      SPIN_LOOP {
        asm volatile("" ::: "memory");
        c.fl = ld64(c, c.vfl);
        if ((fresh(c, c.fl) & 0xfffull) == 0xfffull) break;
      }
    }
    __builtin_amdgcn_fence(__ATOMIC_ACQUIRE, "wavefront");
    PRI(1);
    ldh<0>(c, c.hf[0]); ldh<1>(c, c.hf[1]); ldh<2>(c, c.hf[2]);
    PRI(3);
  };
  if (TYPE == 0) hwait();
  auto lkok = [&]() {
    bool ok = true;
#pragma unroll
    for (int u = 0; u < NU; ++u)
      ok = ok && (sd.v_lk0 >= OOB || (lk0[u].y == c.T && lk0[u].w == c.T2)) && (sd.v_lk1 >= OOB || (lk1[u].y == c.T && lk1[u].w == c.T2));
    return __all(ok);
  };
  if (!lkok()) {
    SPIN_LOOP {
      asm volatile("" ::: "memory");
#pragma unroll
      for (int u = 0; u < NU; ++u) { lk0[u] = ld128(c, sd.v_lk0 + u * 256); lk1[u] = ld128(c, sd.v_lk1 + u * 256); }
      if (lkok()) break;
    }
  }
  if (TYPE == 1) c.fl = ld64(c, c.vfl);
  SB0();
#pragma unroll
  for (int u = 0; u < NU; ++u) {
    const unsigned x0 = lk0[u].x, y0 = lk0[u].z, x1 = lk1[u].x, y1 = lk1[u].z;
    const cpx cy = cfma(wk1[u], cpx{__uint_as_float(x1), __uint_as_float(y1)}, cfma(wk0[u], cpx{__uint_as_float(x0), __uint_as_float(y0)}, pm[u]));
    cpx h = cfma(f1[u], cpx{sumq(cy.x), sumq(cy.y)}, eq[u]);
#pragma unroll
    for (int j = 0; j < 8; ++j) { h = cfma(lam[u], h, v[u][j]); v[u][j] = h; }
    ldsw<v4u>(sd.a_tw, v4u{pk2(v[u][0].x, v[u][1].x), pk2(v[u][2].x, v[u][3].x), pk2(v[u][4].x, v[u][5].x), pk2(v[u][6].x, v[u][7].x)});
    ldsw<v4u>(sd.a_tw + 16384, v4u{pk2(v[u][0].y, v[u][1].y), pk2(v[u][2].y, v[u][3].y), pk2(v[u][4].y, v[u][5].y), pk2(v[u][6].y, v[u][7].y)});
    asm volatile("s_waitcnt lgkmcnt(0)" ::: "memory");
#pragma unroll
    for (int mm = 0; mm < 2; ++mm) st128(c, __builtin_bit_cast(v4u, cat(trread(sd.a_tr + mm * 8), trread(sd.a_tr + mm * 8 + 16384))), sd.v_hs[u] + mm * 1024);
    if (u + 1 < NU) asm volatile("s_waitcnt lgkmcnt(0)" ::: "memory");
  }
  SB0();
  c.vfs = sd.v_fs;
  if (TYPE == 1) {
    if ((fresh(c, c.fl) & 0xfffull) != 0xfffull) {
      waitv<0>();
      st_flag(c, sd.v_fs);
      c.vfs = OOB;
    }
    hwait();
  }
  SB0();
}

DI void phase2(Ctx& c, float* out) {
  const int w = c.w, rt = c.rt, ct = c.ct;
  v4u wf[12][5], hf[12][4];
  f32x4 acc[4][5];
  v2u fl = c.fl;
#pragma unroll
  for (int a = 0; a < 5; ++a) acc[0][a] = c.acc0[a];
  auto body = [&](auto JJ) {
    constexpr int j = JJ.v;
    if constexpr (lds_chunk(j + 1) >= 0) {
      constexpr int ch = lds_chunk(j + 1);
      int l = threadIdx.x & 63; PIN(l);
      const unsigned a4 = tile_base(ch, 0) + w * 4096 + l * 16;
#pragma unroll
      for (int a = 0; a < 4; ++a) wf[j + 1][a] = ldsr<v4u>(a4 + a * 1024);
      wf[j + 1][4] = ldsr<v4u>(tile_base(ch, 4) + w * 1024 + l * 16);
    }
    if constexpr (j < 4) {
#pragma unroll
      for (int a = 0; a < 5; ++a) wf[j][a] = c.wf[j][a];
    }
    if constexpr (j < 3) {
#pragma unroll
      for (int m = 0; m < 4; ++m) hf[j][m] = c.hf[j][m];
    }
    if constexpr (j + 3 < 12 && j + 3 >= 4 && lds_chunk(j + 3) < 0) { ldw<j + 3>(c, wf[j + 3]); SB0(); }
    if constexpr (j == 4 || j == 6) {
      const unsigned long long msk = j == 4 ? (15ull << 12) : ((w >= 6 ? (1ull << 22) : (1ull << 24)) - (1ull << 16));
      if (__builtin_expect((fresh(c, fl) & msk) != msk, 0)) {
        SPIN_LOOP {
          asm volatile("" ::: "memory");
          const v2u f = ld64(c, c.vfl);
          if ((fresh(c, f) & msk) == msk) break;
        }
      }
      __builtin_amdgcn_fence(__ATOMIC_ACQUIRE, "wavefront");
    }
    if constexpr (j == 3 || j == 5) fl = ld64(c, c.vfl);
    if constexpr (j + 2 < 12 && j + 2 >= 3) ldh<j + 2>(c, hf[j + 2]);
    SB0();
#pragma unroll
    for (int a = 0; a < 5; ++a)
#pragma unroll
      for (int m = 0; m < 4; ++m) {
        if (j == 0 && m > 0) mfz(acc[m][a], __builtin_bit_cast(bf16x8, wf[j][a]), __builtin_bit_cast(bf16x8, hf[j][m]));
        else mfa(acc[m][a], __builtin_bit_cast(bf16x8, wf[j][a]), __builtin_bit_cast(bf16x8, hf[j][m]));
      }
    if constexpr (j == 0) {
      if (c.cg < 27) waitv<0>(); else waitv<8>();
      st_flag(c, c.vfs);
    }
    if constexpr (j == 2 || j == 5 || j == 8) __builtin_amdgcn_s_setprio(j == 2 ? 2 : (j == 5 ? 1 : 0));
    if constexpr (j == 1) BAR();
    SB0();
  };
  body(IC<0>{}); body(IC<1>{}); body(IC<2>{}); body(IC<3>{}); body(IC<4>{}); body(IC<5>{});
  body(IC<6>{}); body(IC<7>{}); body(IC<8>{}); body(IC<9>{}); body(IC<10>{});
  int l = threadIdx.x & 63; PIN(l);
  unsigned b4 = w * 4096 + l * 16, b4h = b4 + 65536, b1 = 98304 + w * 1024 + l * 16, b2 = 147456 + w * 2048 + l * 16;
  PIN(b4); PIN(b4h); PIN(b1); PIN(b2);
  const int tl = ((w >> 1) & 1) * 32 + 8 * ((l & 15) >> 2) + 4 * (w & 1) + (l & 3);
  GAS float* op = (GAS float*)out + (size_t)(c.rb * 256 + rt * 64 + tl) * D_ + ct * 80 + (w >> 2) * 16 + 4 * (l >> 4);
  PIN(op);
  unsigned r4[4], r1[4], r2;
#pragma unroll
  for (int x = 0; x < 4; ++x) { r4[x] = ((w & 3) ^ x) * 4096 + l * 16 + (x == 1 ? 65536 : 0); r1[x] = 98304 + ((w & 3) ^ x) * 1024 + l * 16; PIN(r4[x]); PIN(r1[x]); }
  r2 = 147456 + ((w & 3) ^ 3) * 2048 + l * 16; PIN(r2);
  auto redw = [&](int t) {
    const int a = t >> 2, m = t & 3;
    const unsigned tb = tile_base(m, a), ts = tile_wstride(m, a);
    ldsw<f32x4>(ts == 4096 ? (tb >= 65536 ? b4h + (tb - 65536) : b4 + tb) : (ts == 1024 ? b1 + (tb - 98304) : b2 + (tb - 147456)), acc[m][a]);
  };
  if (w < 6) {
#pragma unroll
    for (int t = 0; t < 20; ++t) {
      mfa(acc[t & 3][t >> 2], __builtin_bit_cast(bf16x8, wf[11][t >> 2]), __builtin_bit_cast(bf16x8, hf[11][t & 3]));
      if (t >= 8) redw(t - 8);
      SB0();
    }
    MFMA_DRAIN();
    SB0();
#pragma unroll
    for (int t = 12; t < 20; ++t) redw(t);
  } else {
    MFMA_DRAIN();
    SB0();
#pragma unroll
    for (int t = 0; t < 20; ++t) redw(t);
  }
  BAR();
  auto sum = [&](auto AA) {
    constexpr int a = AA.v;
    f32x4 p[8];
#pragma unroll
    for (int x = 0; x < 4; ++x)
#pragma unroll
      for (int hh = 0; hh < 2; ++hh) {
        const unsigned tb = tile_base(x, a), ts = tile_wstride(x, a);
        p[x * 2 + hh] = ldsr<f32x4>(ts == 4096 ? r4[x] + (tb - (x == 1 ? 65536 : 0)) + hh * 16384 : (ts == 1024 ? r1[x] + (tb - 98304) + hh * 4096 : r2 + (tb - 147456) + hh * 8192));
      }
    return ((p[0] + p[1]) + (p[2] + p[3])) + ((p[4] + p[5]) + (p[6] + p[7]));
  };
  if (w < 4) {
    const f32x4 s0 = sum(IC<0>{}), s1 = sum(IC<2>{}), s2 = sum(IC<4>{});
    *(GAS f32x4*)op = s0; *(GAS f32x4*)(op + 32) = s1; *(GAS f32x4*)(op + 64) = s2;
  } else {
    const f32x4 s0 = sum(IC<1>{}), s1 = sum(IC<3>{});
    *(GAS f32x4*)op = s0; *(GAS f32x4*)(op + 32) = s1;
  }
}

__global__ void __launch_bounds__(512, 2)
fused_kernel(const u16* x, const u16* lre, const u16* lim, const u16* bre, const u16* bim, const u16* cre, const u16* cim, const u16* ldt,
             const u16* dsk, const u16* wg, const u16* wu, const u16* wd, float* out, char* ws) {
  Ctx c;
  asm volatile("s_getpc_b64 %0" : "=s"(c.pc));
  c.dsk = dsk; c.x = x; c.bre = bre; c.bim = bim; c.cre = cre; c.cim = cim; c.wg = wg; c.wu = wu; c.wd = wd; c.ws = ws;
  c.lane = threadIdx.x & 63;
  c.w = __builtin_amdgcn_readfirstlane(threadIdx.x >> 6);
  const int xc = blockIdx.x & 7, ix = blockIdx.x >> 3;
  const int idx = (xc & 1) * 16 + (ix & 15);
  c.rb = (xc >> 1) * 2 + (ix >> 4);
  c.cg = idx < 13 ? idx : (idx < 16 ? idx + 14 : (idx < 30 ? idx - 3 : idx));
  c.rt = idx >> 3; c.ct = idx & 7;
  c.rws = __builtin_amdgcn_make_buffer_rsrc(ws, 0, WS_END, 0x00020000);
  if (__builtin_expect(c.cg < 27, 1)) phase1<0>(c, lre, lim, ldt);
  else phase1<1>(c, lre, lim, ldt);
  phase2(c, out);
}

extern "C" void kernel_launch(void* const* d_in, const int* in_sizes, int n_in, void* d_out, int out_size, void* d_ws, size_t ws_size,
                              hipStream_t stream) {
  (void)in_sizes; (void)n_in; (void)out_size; (void)ws_size;
  (void)hipFuncSetAttribute((const void*)fused_kernel, hipFuncAttributeMaxDynamicSharedMemorySize, LDS_BYTES);
  fused_kernel<<<256, 512, LDS_BYTES, stream>>>((const u16*)d_in[0], (const u16*)d_in[1], (const u16*)d_in[2], (const u16*)d_in[3], (const u16*)d_in[4],
                                                (const u16*)d_in[5], (const u16*)d_in[6], (const u16*)d_in[7], (const u16*)d_in[8], (const u16*)d_in[10],
                                                (const u16*)d_in[11], (const u16*)d_in[12], (float*)d_out, (char*)d_ws);
}
```
